```python
import torch

def _softmax(x, axis=-1):
    return torch.softmax(x, dim=axis)
def _gelu(x):
    return torch.nn.functional.gelu(x, approximate="tanh")
def _silu(x):
    return torch.nn.functional.silu(x)
def _sigmoid(x):
    return torch.sigmoid(x)
def _layernorm(x, axis=-1, eps=1e-5):
    m = x.mean(dim=axis, keepdim=True)
    v = x.var(dim=axis, keepdim=True, unbiased=False)
    return (x - m) / torch.sqrt(v + eps)
def _scatter_add(tgt, idx, src, axis=0):
    return tgt.clone().index_add_(axis, idx, src)
def _segment_sum(src, seg, n):
    out = torch.zeros((n,) + src.shape[1:], dtype=src.dtype)
    return out.index_add_(0, seg, src)
def _pad(x, pw):
    flat = tuple(v for pair in reversed(pw) for v in pair)
    return torch.nn.functional.pad(x, flat)

kernel_name = 'interleave_layers__ssm_conv_h3__dense_activation_relu__prompt'

def setup_inputs():

    _w = (lambda a: (lambda t: t.to(torch.bfloat16) if t.is_floating_point() else t)(torch.as_tensor(a)))
    def _A_setup():
        B, S, D = 1, 2048, 512
        ks = 4
        return {
            "q": _w((lambda *s: torch.randn(*s, dtype=torch.bfloat16) * 0.05)(B, S, D)),
            "k": _w((lambda *s: torch.randn(*s, dtype=torch.bfloat16) * 0.8)(B, S, D)),
            "v": _w((lambda *s: torch.randn(*s, dtype=torch.bfloat16) * 0.8)(B, S, D)),
            "shift_k": _w((lambda *s: torch.randn(*s, dtype=torch.bfloat16) * 0.8)(D, ks) * 0.5),
            "ssm_k": _w((lambda *s: torch.randn(*s, dtype=torch.bfloat16) * 0.8)(D, S) * (1.0 / S ** 0.5)),
            "ssm_D": _w((lambda *s: torch.randn(*s, dtype=torch.bfloat16) * 0.8)(D)),
        }
    _A_in = _A_setup()

    def _B_setup():
        B, S, D, Dff = 1, 2048, 512, 1376
        return {
            "x": _w((lambda *s: torch.randn(*s, dtype=torch.bfloat16) * 0.05)(B, S, D)),
            "w1": _w((lambda *s: torch.randn(*s, dtype=torch.bfloat16) * 0.1)(D, Dff)),
            "w2": _w((lambda *s: torch.randn(*s, dtype=torch.bfloat16) * 0.1)(Dff, D)),
        }
    _B_in = _B_setup()

    return {
        "A_q": _A_in["q"],
        "A_k": _A_in["k"],
        "A_v": _A_in["v"],
        "A_shift_k": _A_in["shift_k"],
        "A_ssm_k": _A_in["ssm_k"],
        "A_ssm_D": _A_in["ssm_D"],
        "B_x": _B_in["x"],
        "B_w1": _B_in["w1"],
        "B_w2": _B_in["w2"],
    }

def reference(A_q, A_k, A_v, A_shift_k, A_ssm_k, A_ssm_D, B_x, B_w1, B_w2):
    def _A_ref(q, k, v, shift_k, ssm_k, ssm_D):
        B, S, D = q.shape
        ks = shift_k.shape[-1]
        k_pad = _pad(k, ((0, 0), (ks - 1, 0), (0, 0)))
        k_shift = torch.zeros_like(k)
        for t in range(ks):
            k_shift = k_shift + k_pad[:, t:t + S, :] * shift_k[:, ks - 1 - t][None, None, :]
        kv = k_shift * v
        L = 2 * S
        kv_pad = _pad(kv, ((0, 0), (0, S), (0, 0)))
        kf_pad = _pad(ssm_k, ((0, 0), (0, S)))
        kv_f = (lambda x, axes=(-2,-1): torch.fft.rfft2(x.float(), dim=axes))(kv_pad[:, None, :, :], axes=(1, 2))
        kf_f = (lambda x, axes=(-2,-1): torch.fft.rfft2(x.float(), dim=axes))(kf_pad[:, None, :], axes=(1, 2))
        kf_f = (lambda x, *d: x.permute(*d))(kf_f, 1, 2, 0)
        y_f = kv_f * kf_f[None, :, :, :]
        y = (lambda x, s, axes=(-2,-1): torch.fft.irfft2(x, s=s, dim=axes).to(torch.bfloat16))(y_f, (1, L), axes=(1, 2))[:, 0, :S, :]
        y = y + kv * ssm_D[None, None, :]
        return q * y

    def _B_ref(x, w1, w2):
        h = torch.einsum("bsd,df->bsf", x, w1)
        zero = torch.zeros_like(h)
        h = torch.maximum(h, zero)
        return torch.einsum("bsf,fd->bsd", h, w2)

    x = A_q
    x = x + _A_ref(_layernorm(x, axis=-1), A_k, A_v, A_shift_k, A_ssm_k, A_ssm_D)
    x = x + _B_ref(_layernorm(x, axis=-1), B_w1, B_w2)
    x = x + _A_ref(_layernorm(x, axis=-1), A_k, A_v, A_shift_k, A_ssm_k, A_ssm_D)
    x = x + _B_ref(_layernorm(x, axis=-1), B_w1, B_w2)
    return x
```

```cpp
#include <hip/hip_runtime.h>
#include <hip/hip_bf16.h>
typedef unsigned short u16;
static __device__ __forceinline__ float bf2f(u16 b) { return __uint_as_float((unsigned)b << 16); }
static __device__ __forceinline__ u16 f2bf(float f) { typedef float f2 __attribute__((ext_vector_type(2))); typedef __bf16 b2 __attribute__((ext_vector_type(2)));
  f2 v = {f, f}; b2 r = __builtin_convertvector(v, b2); return (u16)(__builtin_bit_cast(unsigned, r) & 0xffff); }
static __device__ __forceinline__ float rb(float f) { return bf2f(f2bf(f)); }
constexpr int S = 2048, D = 512, F = 1376;
__global__ void k_kv(const u16* k, const u16* v, const u16* sk, float* kvT) {
  int i = blockIdx.x * 256 + threadIdx.x; int t = i / D, c = i % D;
  float a = 0.f;
  for (int j = 0; j < 4; ++j) { int tt = t - 3 + j; float kk = tt >= 0 ? bf2f(k[tt * D + c]) : 0.f; a = rb(a + rb(kk * bf2f(sk[c * 4 + 3 - j]))); }
  kvT[c * S + t] = rb(a * bf2f(v[i]));
}
__global__ void k_conv(const float* kvT, const u16* h, const u16* Dv, float* G) {
  __shared__ float su[S], sh[S];
  int c = blockIdx.x;
  for (int t = threadIdx.x; t < S; t += 256) { su[t] = kvT[c * S + t]; sh[t] = bf2f(h[c * S + t]); }
  __syncthreads();
  for (int t = threadIdx.x; t < S; t += 256) { float a = 0.f; for (int s = 0; s <= t; ++s) a += sh[t - s] * su[s];
    G[t * D + c] = rb(rb(a) + rb(su[t] * bf2f(Dv[c]))); }
}
__global__ void k_ln(float* x, const u16* q, const float* G, float* xn) {
  __shared__ float red[8];
  int t = blockIdx.x, l = threadIdx.x; float a[8];
  for (int j = 0; j < 8; ++j) a[j] = q ? bf2f(q[t * D + l + 64 * j]) : x[t * D + l + 64 * j];
  for (int pass = 0; pass < 2; ++pass) {
    float s = 0; for (int j = 0; j < 8; ++j) s += a[j];
    for (int o = 32; o; o >>= 1) s += __shfl_xor(s, o);
    float m = s / D, vv = 0; for (int j = 0; j < 8; ++j) vv += (a[j] - m) * (a[j] - m);
    for (int o = 32; o; o >>= 1) vv += __shfl_xor(vv, o);
    float mb = rb(m), den = rb(sqrtf(rb(rb(vv / D) + 1e-5f)));
    for (int j = 0; j < 8; ++j) { float n = rb(rb(a[j] - mb) / den);
      if (pass == 0) { a[j] = rb(a[j] + rb(n * G[t * D + l + 64 * j])); x[t * D + l + 64 * j] = a[j]; } else xn[t * D + l + 64 * j] = n; }
  }
}
__global__ void k_g1(const float* xn, const u16* w1, float* h) {
  __shared__ float sx[4][D]; int t0 = blockIdx.x * 4;
  for (int i = threadIdx.x; i < 4 * D; i += 256) sx[i / D][i % D] = xn[t0 * D + i];
  __syncthreads();
  for (int f = threadIdx.x; f < F; f += 256) { float a[4] = {0, 0, 0, 0};
    for (int k = 0; k < D; ++k) { float w = bf2f(w1[k * F + f]); for (int r = 0; r < 4; ++r) a[r] += sx[r][k] * w; }
    for (int r = 0; r < 4; ++r) h[(t0 + r) * F + f] = fmaxf(rb(a[r]), 0.f); }
}
__global__ void k_g2(const float* h, const u16* w2, float* x, u16* out) {
  __shared__ float sx[4][F]; int t0 = blockIdx.x * 4;
  for (int i = threadIdx.x; i < 4 * F; i += 256) sx[i / F][i % F] = h[t0 * F + i];
  __syncthreads();
  for (int c = threadIdx.x; c < D; c += 256) { float a[4] = {0, 0, 0, 0};
    for (int k = 0; k < F; ++k) { float w = bf2f(w2[k * D + c]); for (int r = 0; r < 4; ++r) a[r] += sx[r][k] * w; }
    for (int r = 0; r < 4; ++r) { float y = rb(x[(t0 + r) * D + c] + rb(a[r])); x[(t0 + r) * D + c] = y; if (out) out[(t0 + r) * D + c] = f2bf(y); } }
}
extern "C" void kernel_launch(void* const* d_in, const int* in_sizes, int n_in, void* d_out, int out_size, void* d_ws, size_t ws_size, hipStream_t stream) {
  const u16 *q = (const u16*)d_in[0], *k = (const u16*)d_in[1], *v = (const u16*)d_in[2], *sk = (const u16*)d_in[3], *hk = (const u16*)d_in[4], *Dv = (const u16*)d_in[5];
  const u16 *w1 = (const u16*)d_in[7], *w2 = (const u16*)d_in[8];
  float* ws = (float*)d_ws; float *kvT = ws, *G = ws + S * D, *x = ws + 2 * S * D, *xn = ws + 3 * S * D, *h = ws + 4 * S * D;
  k_kv<<<S * D / 256, 256, 0, stream>>>(k, v, sk, kvT);
  k_conv<<<D, 256, 0, stream>>>(kvT, hk, Dv, G);
  for (int L = 0; L < 2; ++L) {
    k_ln<<<S, 64, 0, stream>>>(x, L == 0 ? q : nullptr, G, xn);
    k_g1<<<S / 4, 256, 0, stream>>>(xn, w1, h);
    k_g2<<<S / 4, 256, 0, stream>>>(h, w2, x, L == 1 ? (u16*)d_out : nullptr);
  }
}
```

```cpp
#include <hip/hip_runtime.h>
#include <hip/hip_bf16.h>
typedef unsigned short u16;
static __device__ __forceinline__ float bf2f(u16 b) { return __uint_as_float((unsigned)b << 16); }
static __device__ __forceinline__ u16 f2bf(float f) { typedef float f2 __attribute__((ext_vector_type(2))); typedef __bf16 b2 __attribute__((ext_vector_type(2)));
  f2 v = {f, f}; b2 r = __builtin_convertvector(v, b2); return (u16)(__builtin_bit_cast(unsigned, r) & 0xffff); }
static __device__ __forceinline__ float rb(float f) { return bf2f(f2bf(f)); }
constexpr int S = 2048, D = 512, F = 1376;
__global__ void k_kv(const u16* k, const u16* v, const u16* sk, float* kvT) {
  int i = blockIdx.x * 256 + threadIdx.x; int t = i / D, c = i % D;
  float a = 0.f;
  for (int j = 0; j < 4; ++j) { int tt = t - 3 + j; float kk = tt >= 0 ? bf2f(k[tt * D + c]) : 0.f; a = rb(a + rb(kk * bf2f(sk[c * 4 + 3 - j]))); }
  kvT[c * S + t] = rb(a * bf2f(v[i]));
}
__global__ void k_conv(const float* kvT, const u16* h, const u16* Dv, float* G) {
  __shared__ float su[S], sh[S];
  int c = blockIdx.x;
  for (int t = threadIdx.x; t < S; t += 256) { su[t] = kvT[c * S + t]; sh[t] = bf2f(h[c * S + t]); }
  __syncthreads();
  for (int t = threadIdx.x; t < S; t += 256) { float a = 0.f; for (int s = 0; s <= t; ++s) a += sh[t - s] * su[s];
    G[t * D + c] = rb(rb(a) + rb(su[t] * bf2f(Dv[c]))); }
}
__global__ void k_ln(float* x, const u16* q, const float* G, float* xn) {
  __shared__ float red[8];
  int t = blockIdx.x, l = threadIdx.x; float a[8];
  for (int j = 0; j < 8; ++j) a[j] = q ? bf2f(q[t * D + l + 64 * j]) : x[t * D + l + 64 * j];
  for (int pass = 0; pass < 2; ++pass) {
    float s = 0; for (int j = 0; j < 8; ++j) s += a[j];
    for (int o = 32; o; o >>= 1) s += __shfl_xor(s, o);
    float m = s / D, vv = 0; for (int j = 0; j < 8; ++j) vv += (a[j] - m) * (a[j] - m);
    for (int o = 32; o; o >>= 1) vv += __shfl_xor(vv, o);
    float mb = rb(m), den = rb(sqrtf(rb(rb(vv / D) + 1e-5f)));
    for (int j = 0; j < 8; ++j) { float n = rb(rb(a[j] - mb) / den);
      if (pass == 0) { a[j] = rb(a[j] + rb(n * G[t * D + l + 64 * j])); x[t * D + l + 64 * j] = a[j]; } else xn[t * D + l + 64 * j] = n; }
  }
}
__global__ void k_g1(const float* xn, const u16* w1, float* h) {
  __shared__ float sx[4][D]; int t0 = blockIdx.x * 4;
  for (int i = threadIdx.x; i < 4 * D; i += 256) sx[i / D][i % D] = xn[t0 * D + i];
  __syncthreads();
  for (int f = threadIdx.x; f < F; f += 256) { float a[4] = {0, 0, 0, 0};
    for (int k = 0; k < D; ++k) { float w = bf2f(w1[k * F + f]); for (int r = 0; r < 4; ++r) a[r] += sx[r][k] * w; }
    for (int r = 0; r < 4; ++r) h[(t0 + r) * F + f] = fmaxf(rb(a[r]), 0.f); }
}
__global__ void k_g2(const float* h, const u16* w2, float* x, u16* out) {
  __shared__ float sx[4][F]; int t0 = blockIdx.x * 4;
  for (int i = threadIdx.x; i < 4 * F; i += 256) sx[i / F][i % F] = h[t0 * F + i];
  __syncthreads();
  for (int c = threadIdx.x; c < D; c += 256) { float a[4] = {0, 0, 0, 0};
    for (int k = 0; k < F; ++k) { float w = bf2f(w2[k * D + c]); for (int r = 0; r < 4; ++r) a[r] += sx[r][k] * w; }
    for (int r = 0; r < 4; ++r) { float y = rb(x[(t0 + r) * D + c] + rb(a[r])); x[(t0 + r) * D + c] = y; if (out) out[(t0 + r) * D + c] = f2bf(y); } }
}
#define MODE 4

typedef unsigned v4u __attribute__((ext_vector_type(4)));
#define GAS __attribute__((address_space(1)))
__global__ void __launch_bounds__(256,1) k_probe(const unsigned char* k, const unsigned char* v, const unsigned char* w, unsigned* sink) {
  const int b = blockIdx.x, tid = threadIdx.x; const int j = b & 7, i = b >> 3;
  unsigned long long r0 = __builtin_amdgcn_s_memrealtime();
  asm volatile("s_waitcnt lgkmcnt(0)" :: "s"(k), "s"(v), "s"(w) : "memory");
  __builtin_amdgcn_sched_barrier(0);
  unsigned long long t0 = __builtin_amdgcn_s_memtime();
  __builtin_amdgcn_sched_barrier(0);
  unsigned long long dt;
  dt = 0;
}
extern "C" void kernel_launch(void* const* d_in, const int* in_sizes, int n_in, void* d_out, int out_size, void* d_ws, size_t ws_size, hipStream_t stream) {
  const u16 *q = (const u16*)d_in[0], *k = (const u16*)d_in[1], *v = (const u16*)d_in[2], *sk = (const u16*)d_in[3], *hk = (const u16*)d_in[4], *Dv = (const u16*)d_in[5];
  const u16 *w1 = (const u16*)d_in[7], *w2 = (const u16*)d_in[8];
  float* ws = (float*)d_ws; float *kvT = ws, *G = ws + S * D, *x = ws + 2 * S * D, *xn = ws + 3 * S * D, *h = ws + 4 * S * D;
  k_probe<<<256, 256, 0, stream>>>((const unsigned char*)k, (const unsigned char*)v, (const unsigned char*)w2, (unsigned*)d_ws + 60000000);
  k_kv<<<S * D / 256, 256, 0, stream>>>(k, v, sk, kvT);
  k_conv<<<D, 256, 0, stream>>>(kvT, hk, Dv, G);
  for (int L = 0; L < 2; ++L) {
    k_ln<<<S, 64, 0, stream>>>(x, L == 0 ? q : nullptr, G, xn);
    k_g1<<<S / 4, 256, 0, stream>>>(xn, w1, h);
    k_g2<<<S / 4, 256, 0, stream>>>(h, w2, x, L == 1 ? (u16*)d_out : nullptr);
  }
}
```

```cpp
#include <hip/hip_runtime.h>
#include <stdint.h>

#define PROF 0

namespace {
typedef unsigned v4u __attribute__((ext_vector_type(4)));
typedef unsigned v2u __attribute__((ext_vector_type(2)));
typedef float f2 __attribute__((ext_vector_type(2)));
typedef float f4 __attribute__((ext_vector_type(4)));
typedef float f16v __attribute__((ext_vector_type(16)));
typedef __bf16 bf8 __attribute__((ext_vector_type(8)));
typedef __bf16 bf2 __attribute__((ext_vector_type(2)));
typedef short s4 __attribute__((ext_vector_type(4)));
typedef short sh2 __attribute__((ext_vector_type(2)));
typedef unsigned long long u64;
#define GAS __attribute__((address_space(1)))
#define LAS __attribute__((address_space(3)))
#define DI __device__ __forceinline__
#define SB() __builtin_amdgcn_sched_barrier(0)
typedef __amdgpu_buffer_rsrc_t rsrc_t;
typedef LAS unsigned char* lptr;

constexpr int S_ = 2048, D_ = 512, F_ = 1376;
constexpr int SC1 = 16;
constexpr unsigned SENT = 0xFFFFFFFFu;
constexpr unsigned SPIN_CAP = 1u << 15;

constexpr unsigned WS_EPOCH = 0, WS_F1 = 4096, WS_F2 = 8192, WS_PROF = 16384;
constexpr unsigned WS_ST = 1u << 19;
constexpr unsigned WS_KVT = 1u << 20;
constexpr unsigned WS_GT = WS_KVT + (2u << 20);
constexpr unsigned WS_X2 = WS_GT + (2u << 20);
constexpr unsigned WS_HB = WS_X2 + (2u << 20);
constexpr unsigned WS_END = WS_HB + 32u * 2u * 86u * 1024u;

constexpr unsigned L_HT = 0, HT_SZ = 2056 * 16;
constexpr unsigned L_U = 2 * HT_SZ, U_SZ = 144 * 16;
constexpr unsigned L_P0 = L_U + 4 * U_SZ;
constexpr unsigned L_STG = L_P0 + 8192;
constexpr unsigned STG_W = 6 * 3072;
constexpr unsigned L_CRED = L_STG;
constexpr unsigned L_GI = 0;
constexpr unsigned L_X3 = 65536;
constexpr unsigned L_MISC = L_STG + 4 * STG_W;
constexpr unsigned L_CS = L_MISC;
constexpr unsigned L_ST1 = L_CS + 768;
constexpr unsigned L_ST2 = L_ST1 + 1024;
constexpr unsigned L_END = L_ST2 + 1024;
static_assert(L_END <= 163840, "LDS");

struct Params {
  const unsigned char *q, *k, *v, *sk, *hk, *dv, *w1, *w2;
  unsigned char *out, *ws;
};

DI rsrc_t mkrs(const void* p, unsigned bytes) { return __builtin_amdgcn_make_buffer_rsrc((void*)p, 0, bytes, 0x00020000); }
DI v4u ld16(rsrc_t r, unsigned off, unsigned so = 0) { return __builtin_amdgcn_raw_buffer_load_b128(r, off, so, 0); }
DI v4u ld16c(rsrc_t r, unsigned off, unsigned so = 0) { return __builtin_amdgcn_raw_buffer_load_b128(r, off, so, SC1); }
DI v2u ld8c(rsrc_t r, unsigned off, unsigned so = 0) { return __builtin_amdgcn_raw_buffer_load_b64(r, off, so, SC1); }
DI void st16c(rsrc_t r, unsigned off, v4u v, unsigned so = 0) { __builtin_amdgcn_raw_buffer_store_b128(v, r, off, so, SC1); }
DI void st8c(rsrc_t r, unsigned off, v2u v, unsigned so = 0) { __builtin_amdgcn_raw_buffer_store_b64(v, r, off, so, SC1); }
DI v4u lds16(lptr p) { return *(LAS v4u*)p; }
DI void ldsw16(lptr p, v4u v) { *(LAS v4u*)p = v; }
DI v4u trfrag(lptr p0, lptr p1) {
  v2u a = __builtin_bit_cast(v2u, __builtin_amdgcn_ds_read_tr16_b64_v4i16((LAS s4*)p0));
  v2u b = __builtin_bit_cast(v2u, __builtin_amdgcn_ds_read_tr16_b64_v4i16((LAS s4*)p1));
  return (v4u){a.x, a.y, b.x, b.y};
}
DI f4 mfma16(v4u a, v4u b, f4 c) { return __builtin_amdgcn_mfma_f32_16x16x32_bf16(__builtin_bit_cast(bf8, a), __builtin_bit_cast(bf8, b), c, 0, 0, 0); }
DI f16v mfma32(v4u a, v4u b, f16v c) { return __builtin_amdgcn_mfma_f32_32x32x16_bf16(__builtin_bit_cast(bf8, a), __builtin_bit_cast(bf8, b), c, 0, 0, 0); }
DI unsigned cvtpk(float lo, float hi) { f2 v = {lo, hi}; return __builtin_bit_cast(unsigned, __builtin_convertvector(v, bf2)); }
DI float blo(unsigned d) { return __uint_as_float(d << 16); }
DI float bhi(unsigned d) { return __uint_as_float(d & 0xffff0000u); }
DI unsigned guard(unsigned d) { return d < 0xFFFFFFFEu ? d : 0xFFFFFFFEu; }
DI unsigned umax3(unsigned a, unsigned b, unsigned c) { unsigned m = a > b ? a : b; return m > c ? m : c; }
DI unsigned chk4(unsigned m, v4u v) { return umax3(umax3(m, v.x, v.y), v.z, v.w); }
DI void bar() { asm volatile("s_waitcnt lgkmcnt(0)\n\ts_barrier" ::: "memory"); }
DI void drain() { asm volatile("s_waitcnt vmcnt(0)" ::: "memory"); }
DI bool allok(bool ok) { return __builtin_amdgcn_ballot_w64(!ok) == 0ull; }
DI void dma16(rsrc_t r, unsigned voff, unsigned soff, unsigned ldsbase) {
  asm volatile("s_mov_b32 m0, %0\n\ts_nop 0\n\tbuffer_load_dwordx4 %1, %2, %3 offen lds" ::"s"(ldsbase), "v"(voff), "s"(r), "s"(soff) : "memory");
}
#define STAMP(i)
#define PROF_BEGIN()
#define PROF_END()

__global__ void __launch_bounds__(256, 1) fused_kernel(Params p) {
  extern __shared__ __attribute__((aligned(16))) unsigned char smem_[];
  lptr L = (lptr)smem_;
  const unsigned ldsb = (unsigned)(uintptr_t)L;
  const int tid = threadIdx.x, lane = tid & 63, w = __builtin_amdgcn_readfirstlane(tid >> 6);
  const int b = blockIdx.x, g8 = b & 7, i32 = b >> 3;
  const int cl = g8 * 4 + (i32 >> 3), j = i32 & 7, c0 = 64 * g8 + 2 * i32;
  const int nun = j < 6 ? 11 : 10, u0 = j < 6 ? 11 * j : 66 + 10 * (j - 6);
  const int ucnt = (w < nun - 8) ? 3 : 2;
  const int ust = u0 + ((nun == 10 && w == 3) ? 8 : 3 * w);
  const int k2st = w < 2 ? 22 * w : 44 + 21 * (w - 2);
  const rsrc_t rws = mkrs(p.ws, WS_END);
  const v4u sent4 = {SENT, SENT, SENT, SENT};
  PROF_BEGIN();
  STAMP(0);

  const v2u ep = ld8c(rws, WS_EPOCH);
  const unsigned kvt_off = WS_KVT + ((64 * g8 + 16 * w + (lane >> 3)) * 256 + 8 * i32 + (lane & 7)) * 16;
  st16c(rws, kvt_off, sent4);
  st16c(rws, kvt_off + 8 * 4096, sent4);
  SB();
  const int rho = lane >> 3, ch8 = lane & 7;
  const int r0 = 64 * i32 + 16 * w + 2 * rho;
  v4u kk[5], vv[2], skw[4], hc[2], hp[2];
  {
    const rsrc_t rk = mkrs(p.k, S_ * D_ * 2), rv = mkrs(p.v, S_ * D_ * 2), rsk = mkrs(p.sk, D_ * 8), rh = mkrs(p.hk, D_ * S_ * 2);
    const unsigned colb = 128 * g8 + 16 * ch8;
#pragma unroll
    for (int m = 0; m < 5; ++m) {
      const int row = r0 - 3 + m;
      unsigned off = row >= 0 ? (unsigned)row * 1024u + colb : 0x80000000u;
      asm volatile("" : "+v"(off));
      kk[m] = ld16(rk, off);
    }
    vv[0] = ld16(rv, r0 * 1024 + colb);
    vv[1] = ld16(rv, r0 * 1024 + colb + 1024);
#pragma unroll
    for (int m = 0; m < 4; ++m) skw[m] = ld16(rsk, (64 * g8 + 8 * ch8) * 8 + 16 * m);
#pragma unroll
    for (int ch = 0; ch < 2; ++ch) {
      hc[ch] = ld16(rh, (c0 + ch) * 4096 + tid * 16);
      unsigned off = tid > 0 ? (unsigned)((c0 + ch) * 4096 + tid * 16 - 16) : 0x80000000u;
      asm volatile("" : "+v"(off));
      hp[ch] = ld16(rh, off);
    }
  }
  SB();
  asm volatile("s_waitcnt vmcnt(15)" ::: "memory");
  u64 tag;
  u64 epoch;
  {
    epoch = ((u64)__builtin_amdgcn_readfirstlane(ep.y) << 32) | (unsigned)__builtin_amdgcn_readfirstlane(ep.x);
    u64 z = epoch + 0x9E3779B97F4A7C15ull;
    z = (z ^ (z >> 30)) * 0xBF58476D1CE4E5B9ull;
    z = (z ^ (z >> 27)) * 0x94D049BB133111EBull;
    tag = z ^ (z >> 31);
  }
  const v2u tagv = {(unsigned)tag, (unsigned)(tag >> 32)};
  asm volatile("s_barrier" ::: "memory");
  if (tid == 0) st8c(rws, WS_F1 + (g8 * 32 + i32) * 8, tagv);
  STAMP(1);

  {
#pragma unroll
    for (int e = 0; e < 8; ++e) {
      const unsigned wa = skw[e >> 1][(e & 1) * 2], wb = skw[e >> 1][(e & 1) * 2 + 1];
      const float w0 = blo(wa), w1 = bhi(wa), w2 = blo(wb), w3 = bhi(wb);
      float kf[5];
#pragma unroll
      for (int m = 0; m < 5; ++m) kf[m] = (e & 1) ? bhi(kk[m][e >> 1]) : blo(kk[m][e >> 1]);
      const float s0 = kf[0] * w3 + kf[1] * w2 + kf[2] * w1 + kf[3] * w0;
      const float s1 = kf[1] * w3 + kf[2] * w2 + kf[3] * w1 + kf[4] * w0;
      const float v0 = (e & 1) ? bhi(vv[0][e >> 1]) : blo(vv[0][e >> 1]);
      const float v1 = (e & 1) ? bhi(vv[1][e >> 1]) : blo(vv[1][e >> 1]);
      const unsigned d = guard(cvtpk(s1 * v1, s0 * v0));
      const int oct = 2 * w + (rho >> 2);
      *(LAS unsigned*)(L + L_P0 + (8 * ch8 + e) * 128 + 16 * (oct ^ ch8) + 4 * (3 - (rho & 3))) = d;
    }
    bar();
#pragma unroll
    for (int z = 0; z < 2; ++z) {
      const int c = 16 * w + 8 * z + (lane >> 3);
      st16c(rws, kvt_off + z * 8 * 4096, lds16(L + L_P0 + c * 128 + 16 * ((lane & 7) ^ (c >> 3))));
    }
  }
  STAMP(2);
  const unsigned gt_off = WS_GT + (c0 + (w >> 1)) * 4096 + (128 * (w & 1) + lane) * 16;
  const unsigned hb_cl = WS_HB + cl * (2 * 86 * 1024);
  const unsigned hst_v = ((lane & 15) + 16 * (lane >> 5) + 32 * ((lane >> 4) & 1)) * 16, hst_s = hb_cl + ust * 1024;
  const unsigned x2st_v = (lane >> 5) * 1024 + ((lane & 7) + 16 * ((lane >> 3) & 3)) * 16, x2st_s = WS_X2 + ((cl * 4 + (w >> 1)) * 16 + 2 * j) * 1024 + 8 * (w & 1) * 16;
  st16c(rws, gt_off, sent4);
  st16c(rws, gt_off + 1024, sent4);
#pragma unroll
  for (int H = 0; H < 2; ++H) {
#pragma unroll
    for (int u = 0; u < 3; ++u)
      if (u < ucnt) st16c(rws, hst_v, sent4, hst_s + H * 86 * 1024 + u * 1024);
    st16c(rws, x2st_v, sent4, x2st_s + H * 2 * 16 * 1024);
  }
  if (tid < 32) st16c(rws, WS_ST + ((cl * 4 + (tid >> 3)) * 8 + j) * 128 + (tid & 7) * 16, sent4);

  {
    const int o1 = tid + 1;
    const unsigned sig = ((o1 & 1) << 2) | ((o1 >> 1) & 3);
#pragma unroll
    for (int ch = 0; ch < 2; ++ch) {
      unsigned d[8] = {hp[ch].x, hp[ch].y, hp[ch].z, hp[ch].w, hc[ch].x, hc[ch].y, hc[ch].z, hc[ch].w}, e[7];
#pragma unroll
      for (int m = 0; m < 7; ++m) e[m] = __builtin_amdgcn_alignbit(d[m + 1], d[m], 16);
#pragma unroll
      for (int s = 0; s < 8; ++s) {
        const v4u win = (s & 1) ? (v4u){d[(s + 1) / 2], d[(s + 1) / 2 + 1], d[(s + 1) / 2 + 2], d[(s + 1) / 2 + 3]}
                                : (v4u){e[s / 2], e[s / 2 + 1], e[s / 2 + 2], e[s / 2 + 3]};
        ldsw16(L + L_HT + ch * HT_SZ + (8 * o1 + (s ^ sig)) * 16, win);
      }
    }
    const v4u z4 = {0, 0, 0, 0};
    if (tid < 16) ldsw16(L + L_HT + (tid >> 3) * HT_SZ + (tid & 7) * 16, z4);
    if (tid >= 64 && tid < 128) ldsw16(L + L_U + ((tid - 64) >> 4) * U_SZ + (tid & 15) * 16, z4);
  }
  STAMP(3);

  v4u w1f[3][16], w2f[22][2];
  {
    const rsrc_t rw1 = mkrs(p.w1, D_ * F_ * 2), rw2 = mkrs(p.w2, F_ * D_ * 2);
    const unsigned stg = L_STG + w * STG_W;
    unsigned o1[3];
#pragma unroll
    for (int i = 0; i < 3; ++i) {
      const int id = 64 * i + lane, row = id / 6, cc = id % 6;
      const int un = (cc >> 1) < ucnt ? (cc >> 1) : 0;
      o1[i] = row * (F_ * 2) + (ust + un) * 32 + (cc & 1) * 16;
    }
    const int g = lane >> 4, q = (lane >> 2) & 3, pp = lane & 3;
    lptr t1 = L + stg + (16 * (g >> 1) + 4 * (g & 1) + q) * 96 + 8 * pp;
#pragma unroll
    for (int s = 0; s < 5; ++s)
#pragma unroll
      for (int i = 0; i < 3; ++i) dma16(rw1, o1[i], s * 32 * F_ * 2, ldsb + stg + s * 3072 + i * 1024);
    const unsigned o2 = (16 * k2st + (lane >> 3)) * 1024 + 128 * j + (((lane & 7) ^ (((lane >> 4) & 1) << 2)) * 16);
    const int hh = lane >> 5, gl = (lane >> 4) & 1;
    lptr t2[2];
#pragma unroll
    for (int ct = 0; ct < 2; ++ct) t2[ct] = L + stg + (4 * hh + q) * 128 + (((4 * ct + 2 * gl + (pp >> 1)) ^ (((q >> 1) & 1) << 2)) * 16) + 8 * (pp & 1);
#pragma unroll
    for (int s = 0; s < 16; ++s) {
      if (s <= 11) asm volatile("s_waitcnt vmcnt(12)" ::: "memory");
      else if (s == 12) asm volatile("s_waitcnt vmcnt(9)" ::: "memory");
      else if (s == 13) asm volatile("s_waitcnt vmcnt(6)" ::: "memory");
      else if (s == 14) asm volatile("s_waitcnt vmcnt(3)" ::: "memory");
      else asm volatile("s_waitcnt vmcnt(0)" ::: "memory");
#pragma unroll
      for (int u = 0; u < 3; ++u) {
        w1f[u][s] = trfrag(t1 + (s % 6) * 3072 + 32 * u, t1 + (s % 6) * 3072 + 32 * u + 8 * 96);
        if (s < 3) asm volatile("" : "+a"(w1f[u][s]));
      }
      if (s + 5 < 16) {
        asm volatile("s_waitcnt lgkmcnt(6)" ::: "memory");
#pragma unroll
        for (int i = 0; i < 3; ++i) dma16(rw1, o1[i], (s + 5) * 32 * F_ * 2, ldsb + stg + ((s + 5) % 6) * 3072 + i * 1024);
      }
      SB();
    }
    asm volatile("s_waitcnt lgkmcnt(0)" ::: "memory");
#pragma unroll
    for (int s = 0; s < 8; ++s) {
      dma16(rw2, o2, s * 16384, ldsb + stg + s * 2048);
      dma16(rw2, o2, s * 16384 + 8192, ldsb + stg + s * 2048 + 1024);
    }
#pragma unroll
    for (int s = 0; s < 22; ++s) {
      const int infl = (s + 7 < 22 ? 7 : 21 - s) * 2;
      asm volatile("s_waitcnt vmcnt(%0)" ::"i"(infl) : "memory");
#pragma unroll
      for (int ct = 0; ct < 2; ++ct) {
        w2f[s][ct] = trfrag(t2[ct] + (s % 9) * 2048, t2[ct] + (s % 9) * 2048 + 1024);
        asm volatile("" : "+a"(w2f[s][ct]));
      }
      if (s + 8 < 22) {
        asm volatile("s_waitcnt lgkmcnt(4)" ::: "memory");
        dma16(rw2, o2, (s + 8) * 16384, ldsb + stg + ((s + 8) % 9) * 2048);
        dma16(rw2, o2, (s + 8) * 16384 + 8192, ldsb + stg + ((s + 8) % 9) * 2048 + 1024);
      }
      SB();
    }
    const v4u ones = {0x3f803f80u, 0x3f803f80u, 0x3f803f80u, 0x3f803f80u};
#pragma unroll
    for (int u = 0; u < 3; ++u) {
      f4 a = {0, 0, 0, 0};
#pragma unroll
      for (int s = 0; s < 16; ++s) a = mfma16(w1f[u][s], ones, a);
      if ((lane & 15) == 0) *(LAS f4*)(L + L_CS + ((w * 3 + u) * 4 + (lane >> 4)) * 16) = a;
    }
  }
  bar();
  if (tid == 0) st8c(rws, WS_F2 + b * 8, tagv);
  STAMP(4);

  {
    _Pragma("unroll 1") for (unsigned sp = 0; sp < SPIN_CAP; ++sp) {
      const v2u f = ld8c(rws, WS_F1 + (g8 * 32 + (lane & 31)) * 8);
      if (allok(f.x == tagv.x && f.y == tagv.y)) break;
      __builtin_amdgcn_s_sleep(2);
    }
    __builtin_amdgcn_fence(__ATOMIC_ACQUIRE, "wavefront");
    v4u u0v, u1v;
    _Pragma("unroll 1") for (unsigned sp = 0; sp < SPIN_CAP; ++sp) {
      u0v = ld16c(rws, WS_KVT + (c0 * 256 + tid) * 16);
      u1v = ld16c(rws, WS_KVT + ((c0 + 1) * 256 + tid) * 16);
      if (allok(chk4(chk4(0u, u0v), u1v) != SENT)) break;
      __builtin_amdgcn_s_sleep(2);
    }
    ldsw16(L + L_U + (tid & 1) * U_SZ + ((tid >> 1) + 16) * 16, u0v);
    ldsw16(L + L_U + (2 + (tid & 1)) * U_SZ + ((tid >> 1) + 16) * 16, u1v);
    bar();
  }
  STAMP(5);

  {
    const int ch = w >> 1, r = lane & 15, g = lane >> 4, pi = ((g & 1) << 1) | (g >> 1);
    f4 acc[8];
#pragma unroll
    for (int i = 0; i < 8; ++i) acc[i] = (f4){0, 0, 0, 0};
    lptr ub = L + L_U + (2 * ch + (g >> 1)) * U_SZ + (r - 1 + (g & 1) + 16) * 16;
#pragma unroll
    for (int bb = 0; bb < 4; ++bb) {
      const int bq = 4 * (w & 1) + bb;
      const int x1 = r + 24 - 8 * pi + 32 * bq;
      const int o = x1 >> 3;
      lptr ab = L + L_HT + ch * HT_SZ + (x1 ^ (((o & 1) << 2) | ((o >> 1) & 3))) * 16;
      v4u B[8];
#pragma unroll
      for (int d = 0; d < 8; ++d) B[d] = lds16(ub + 256 * d - 32 * bq);
#pragma unroll
      for (int a = 0; a < 8; ++a) {
        const v4u A = lds16(ab + 4096 * a);
#pragma unroll
        for (int d = 0; d < 8 - a; ++d) acc[a + d] = mfma16(A, B[d], acc[a + d]);
      }
    }
    STAMP(6);
#pragma unroll
    for (int i = 0; i < 8; ++i) *(LAS f4*)(L + L_CRED + w * 8192 + ((64 * i + 4 * r + g) ^ ((r >> 1) & 3)) * 16) = acc[i];
    const float dval = blo((unsigned)*(const GAS unsigned short*)(p.dv + (c0 + ch) * 2));
    bar();
#pragma unroll
    for (int z = 0; z < 2; ++z) {
      const int o = 128 * (w & 1) + 64 * z + lane;
      const int sw = (o >> 2) & 3;
      lptr pa = L + L_CRED + (2 * ch) * 8192;
      const f4 y0 = *(LAS f4*)(pa + ((2 * o) ^ sw) * 16) + *(LAS f4*)(pa + 8192 + ((2 * o) ^ sw) * 16);
      const f4 y1 = *(LAS f4*)(pa + ((2 * o + 1) ^ sw) * 16) + *(LAS f4*)(pa + 8192 + ((2 * o + 1) ^ sw) * 16);
      const v4u u = lds16(L + L_U + (2 * ch + (o & 1)) * U_SZ + ((o >> 1) + 16) * 16);
      v4u gq;
      gq.x = guard(cvtpk(y0.x + bhi(u.w) * dval, y0.y + blo(u.w) * dval));
      gq.y = guard(cvtpk(y0.z + bhi(u.z) * dval, y0.w + blo(u.z) * dval));
      gq.z = guard(cvtpk(y1.x + bhi(u.y) * dval, y1.y + blo(u.y) * dval));
      gq.w = guard(cvtpk(y1.z + bhi(u.x) * dval, y1.w + blo(u.x) * dval));
      st16c(rws, gt_off + z * 1024, gq);
    }
  }
  STAMP(7);

  const int tu = w & 1, kh = w >> 1, tl = lane & 15, gq4 = lane >> 4;
  const v4u ones = {0x3f803f80u, 0x3f803f80u, 0x3f803f80u, 0x3f803f80u};
  {
    v4u xf[2][8];
    const rsrc_t rq = mkrs(p.q, S_ * D_ * 2);
#pragma unroll
    for (int H = 0; H < 2; ++H)
#pragma unroll
      for (int s = 0; s < 8; ++s) xf[H][s] = ld16(rq, tl * 1024 + gq4 * 16 + s * 64, (64 * cl + 32 * H + 16 * tu) * 1024 + 8 * kh * 64);
#pragma unroll
    for (int H = 0; H < 2; ++H) {
      f4 a1 = {0, 0, 0, 0}, a2 = {0, 0, 0, 0};
      if (H == 0) bar();
#pragma unroll
      for (int s = 0; s < 8; ++s) {
        auto r1 = __builtin_amdgcn_permlane16_swap(xf[H][s].x, xf[H][s].z, false, false);
        auto r2 = __builtin_amdgcn_permlane16_swap(xf[H][s].y, xf[H][s].w, false, false);
        xf[H][s] = (v4u){r1[0], r2[0], r1[1], r2[1]};
        a1 = mfma16(ones, xf[H][s], a1);
        a2 = mfma16(xf[H][s], xf[H][s], a2);
        ldsw16(L + L_X3 + ((2 * H + tu) * 16 + 8 * kh + s) * 1024 + lane * 16, xf[H][s]);
      }
      const float sq = (tl & 2) ? ((tl & 1) ? a2.w : a2.z) : ((tl & 1) ? a2.y : a2.x);
      if ((tl >> 2) == gq4) *(LAS f2*)(L + L_ST1 + (((2 * H + tu) * 2 + kh) * 16 + tl) * 8) = (f2){a1.x, sq};
    }
    _Pragma("unroll 1") for (unsigned sp = 0; sp < SPIN_CAP; ++sp) {
      const v2u f = ld8c(rws, WS_F2 + tid * 8);
      const v2u f2_ = ld8c(rws, WS_F2 + ((tid + 64) & 255) * 8), f3 = ld8c(rws, WS_F2 + ((tid + 128) & 255) * 8), f4_ = ld8c(rws, WS_F2 + ((tid + 192) & 255) * 8);
      if (allok(f.x == tagv.x && f.y == tagv.y && f2_.x == tagv.x && f2_.y == tagv.y && f3.x == tagv.x && f3.y == tagv.y && f4_.x == tagv.x && f4_.y == tagv.y)) break;
      __builtin_amdgcn_s_sleep(2);
    }
    __builtin_amdgcn_fence(__ATOMIC_ACQUIRE, "wavefront");
    STAMP(8);
    v4u gr[16];
    _Pragma("unroll 1") for (unsigned sp = 0; sp < SPIN_CAP; ++sp) {
      unsigned m = 0;
#pragma unroll
      for (int r = 0; r < 16; ++r) gr[r] = ld16c(rws, (tid >> 3) * 4096 + (tid & 7) * 16, WS_GT + 32 * r * 4096 + cl * 128);
#pragma unroll
      for (int r = 0; r < 16; ++r) m = chk4(m, gr[r]);
      if (allok(m != SENT)) break;
      __builtin_amdgcn_s_sleep(4);
    }
#pragma unroll
    for (int r = 0; r < 16; ++r) ldsw16(L + L_GI + (32 * r + (tid >> 3)) * 128 + ((tid & 7) ^ (((tid >> 4) & 3) << 1)) * 16, gr[r]);
    bar();
  }
  STAMP(9);

  const int q4 = (lane >> 2) & 3, pp = lane & 3;
  const int grow = 16 * (gq4 >> 1) + 4 * (gq4 & 1) + q4;
#pragma unroll 1
  for (int layer = 0; layer < 2; ++layer) {
    v4u res[2];
#pragma unroll
    for (int H = 0; H < 2; ++H) {
      float s1, s2;
      v4u xf[8];
      if (layer == 0) {
#pragma unroll
        for (int s = 0; s < 8; ++s) xf[s] = lds16(L + L_X3 + ((2 * H + tu) * 16 + 8 * kh + s) * 1024 + lane * 16);
        const f2 a = *(LAS f2*)(L + L_ST1 + (((2 * H + tu) * 2 + 0) * 16 + tl) * 8), c = *(LAS f2*)(L + L_ST1 + (((2 * H + tu) * 2 + 1) * 16 + tl) * 8);
        s1 = a.x + c.x; s2 = a.y + c.y;
      } else {
        v2u sa, sb;
        _Pragma("unroll 1") for (unsigned sp = 0; sp < SPIN_CAP; ++sp) {
          unsigned m = 0;
          sa = ld8c(rws, (2 * gq4 * 16 + tl) * 8, WS_ST + (cl * 4 + 2 * H + tu) * 1024);
          sb = ld8c(rws, (2 * gq4 * 16 + tl) * 8 + 128, WS_ST + (cl * 4 + 2 * H + tu) * 1024);
#pragma unroll
          for (int s = 0; s < 8; ++s) xf[s] = ld16c(rws, lane * 16 + (s & 3) * 1024, WS_X2 + ((cl * 4 + 2 * H + tu) * 16 + 8 * kh + (s & 4)) * 1024);
          m = umax3(umax3(m, sa.x, sa.y), sb.x, sb.y);
#pragma unroll
          for (int s = 0; s < 8; ++s) m = chk4(m, xf[s]);
          if (allok(m != SENT)) break;
          __builtin_amdgcn_s_sleep(2);
        }
        s1 = __uint_as_float(sa.x) + __uint_as_float(sb.x);
        s2 = __uint_as_float(sa.y) + __uint_as_float(sb.y);
        s1 += __shfl_xor(s1, 16); s2 += __shfl_xor(s2, 16);
        s1 += __shfl_xor(s1, 32); s2 += __shfl_xor(s2, 32);
      }
      const float mean = s1 * (1.f / D_);
      const float rstd = __builtin_amdgcn_rsqf(fmaxf(s2 * (1.f / D_) - mean * mean, 0.f) + 1e-5f);
      const float mr = -mean * rstd;
      f4 a1 = {0, 0, 0, 0}, a2 = {0, 0, 0, 0};
#pragma unroll
      for (int s = 0; s < 8; ++s) {
        const int c = 32 * (8 * kh + s) + grow;
        lptr gp = L + L_GI + c * 128 + (((2 * H + tu) ^ ((c >> 1) & 3)) * 32) + 8 * pp;
        const v4u gf = trfrag(gp, gp + 8 * 128);
        v4u y;
#pragma unroll
        for (int d = 0; d < 4; ++d) {
          const float xl = blo(xf[s][d]), xh = bhi(xf[s][d]);
          y[d] = cvtpk(xl + (xl * rstd + mr) * blo(gf[d]), xh + (xh * rstd + mr) * bhi(gf[d]));
        }
        ldsw16(L + L_X3 + ((2 * H + tu) * 16 + 8 * kh + s) * 1024 + lane * 16, y);
        a1 = mfma16(ones, y, a1);
        a2 = mfma16(y, y, a2);
      }
      {
        const float sq = (tl & 2) ? ((tl & 1) ? a2.w : a2.z) : ((tl & 1) ? a2.y : a2.x);
        if ((tl >> 2) == gq4) *(LAS f2*)(L + L_ST2 + (((2 * H + tu) * 2 + kh) * 16 + tl) * 8) = (f2){a1.x, sq};
      }
      bar();
      STAMP(10 + 10 * layer + 2 * H);
      {
        const int tt = layer == 0 ? (lane & 7) : (lane >> 3), pc = layer == 0 ? (lane >> 3) : (lane & 7);
        res[H] = lds16(L + L_X3 + ((2 * H + (w >> 1)) * 16 + 2 * j + (pc >> 2)) * 1024 + (8 * (w & 1) + tt + 16 * (pc & 3)) * 16);
      }
      f4 acc[3][2];
#pragma unroll
      for (int u = 0; u < 3; ++u) acc[u][0] = acc[u][1] = (f4){0, 0, 0, 0};
#pragma unroll
      for (int s = 0; s < 16; ++s) {
        const v4u x0 = lds16(L + L_X3 + ((2 * H) * 16 + s) * 1024 + lane * 16), x1 = lds16(L + L_X3 + ((2 * H + 1) * 16 + s) * 1024 + lane * 16);
#pragma unroll
        for (int u = 0; u < 3; ++u) {
          acc[u][0] = mfma16(w1f[u][s], x0, acc[u][0]);
          acc[u][1] = mfma16(w1f[u][s], x1, acc[u][1]);
        }
      }
      float r3[2], mr3[2];
#pragma unroll
      for (int t2 = 0; t2 < 2; ++t2) {
        const f2 a = *(LAS f2*)(L + L_ST2 + (((2 * H + t2) * 2 + 0) * 16 + tl) * 8), c = *(LAS f2*)(L + L_ST2 + (((2 * H + t2) * 2 + 1) * 16 + tl) * 8);
        const float m = (a.x + c.x) * (1.f / D_);
        r3[t2] = __builtin_amdgcn_rsqf(fmaxf((a.y + c.y) * (1.f / D_) - m * m, 0.f) + 1e-5f);
        mr3[t2] = -m * r3[t2];
      }
      const unsigned sgn = layer == 0 ? 0u : 0x80008000u;
#pragma unroll
      for (int u = 0; u < 3; ++u) {
        const f4 cs = *(LAS f4*)(L + L_CS + ((w * 3 + u) * 4 + gq4) * 16);
        unsigned pk[2][2];
#pragma unroll
        for (int t2 = 0; t2 < 2; ++t2) {
          const f4 hv = acc[u][t2] * r3[t2] + cs * mr3[t2];
          pk[t2][0] = __builtin_bit_cast(unsigned, __builtin_elementwise_max(__builtin_bit_cast(sh2, cvtpk(hv.x, hv.y)), (sh2){0, 0})) | sgn;
          pk[t2][1] = __builtin_bit_cast(unsigned, __builtin_elementwise_max(__builtin_bit_cast(sh2, cvtpk(hv.z, hv.w)), (sh2){0, 0})) | sgn;
        }
        auto ra = __builtin_amdgcn_permlane32_swap(pk[0][0], pk[1][0], false, false);
        auto rb = __builtin_amdgcn_permlane32_swap(pk[0][1], pk[1][1], false, false);
        if (u < ucnt) st16c(rws, hst_v, (v4u){ra[0], rb[0], ra[1], rb[1]}, hst_s + H * 86 * 1024 + u * 1024);
      }
      STAMP(11 + 10 * layer + 2 * H);
    }
#pragma unroll
    for (int H = 0; H < 2; ++H) {
      const unsigned want = layer == 0 ? 0u : 0x80008000u;
      f16v a2[2];
#pragma unroll
      for (int i = 0; i < 16; ++i) a2[0][i] = a2[1][i] = 0.f;
#pragma unroll
      for (int bt = 0; bt < 2; ++bt) {
        v4u hf[11];
        _Pragma("unroll 1") for (unsigned sp = 0; sp < SPIN_CAP; ++sp) {
          unsigned m = 0;
#pragma unroll
          for (int i = 0; i < 11; ++i) {
            const int s = 11 * bt + i;
            hf[i] = ld16c(rws, lane * 16 + (s == 21 ? 0 : (s & 3) * 1024), hb_cl + (H * 86 + k2st + (s == 21 ? (w >= 2 ? 0 : 21) : (s & ~3))) * 1024);
          }
#pragma unroll
          for (int i = 0; i < 11; ++i) m |= ((hf[i].x ^ want) | (hf[i].y ^ want)) | ((hf[i].z ^ want) | (hf[i].w ^ want));
          if (allok((m & 0x80008000u) == 0u)) break;
          __builtin_amdgcn_s_sleep(2);
        }
        if (bt == 0) STAMP(14 + 10 * layer + 3 * H);
#pragma unroll
        for (int i = 0; i < 11; ++i) {
          const int s = 11 * bt + i;
          if (s < 21 || w < 2) {
            a2[0] = mfma32(w2f[s][0], hf[i], a2[0]);
            a2[1] = mfma32(w2f[s][1], hf[i], a2[1]);
          }
        }
      }
      STAMP(15 + 10 * layer + 3 * H);
      if (H == 0) bar();
      lptr red = L + L_X3 + H * 32768;
#pragma unroll
      for (int ct = 0; ct < 2; ++ct)
#pragma unroll
        for (int rq = 0; rq < 4; ++rq)
          *(LAS f4*)(red + w * 8192 + (ct * 4 + rq) * 1024 + lane * 16) = (f4){a2[ct][4 * rq], a2[ct][4 * rq + 1], a2[ct][4 * rq + 2], a2[ct][4 * rq + 3]};
      bar();
      {
        const int tt = layer == 0 ? (lane & 7) : (lane >> 3), pc = layer == 0 ? (lane >> 3) : (lane & 7);
        lptr rp = red + ((pc >> 2) * 4 + 2 * ((pc >> 1) & 1)) * 1024 + (8 * w + tt + 32 * (pc & 1)) * 16;
        f4 y0 = *(LAS f4*)(rp), y1 = *(LAS f4*)(rp + 1024);
#pragma unroll
        for (int ww = 1; ww < 4; ++ww) { y0 += *(LAS f4*)(rp + ww * 8192); y1 += *(LAS f4*)(rp + ww * 8192 + 1024); }
        const float sg = layer == 0 ? 1.f : -1.f;
        y0 = y0 * sg + (f4){blo(res[H].x), bhi(res[H].x), blo(res[H].y), bhi(res[H].y)};
        y1 = y1 * sg + (f4){blo(res[H].z), bhi(res[H].z), blo(res[H].w), bhi(res[H].w)};
        v4u o = {cvtpk(y0.x, y0.y), cvtpk(y0.z, y0.w), cvtpk(y1.x, y1.y), cvtpk(y1.z, y1.w)};
        if (layer == 0) {
          o = (v4u){guard(o.x), guard(o.y), guard(o.z), guard(o.w)};
          st16c(rws, x2st_v, o, x2st_s + H * 2 * 16 * 1024);
          float t1 = (y0.x + y0.y) + (y0.z + y0.w) + (y1.x + y1.y) + (y1.z + y1.w);
          float t2 = (y0.x * y0.x + y0.y * y0.y) + (y0.z * y0.z + y0.w * y0.w) + (y1.x * y1.x + y1.y * y1.y) + (y1.z * y1.z + y1.w * y1.w);
          t1 += __shfl_xor(t1, 8); t2 += __shfl_xor(t2, 8);
          t1 += __shfl_xor(t1, 16); t2 += __shfl_xor(t2, 16);
          t1 += __shfl_xor(t1, 32); t2 += __shfl_xor(t2, 32);
          if (lane < 8) st8c(rws, lane * 8, (v2u){guard(__float_as_uint(t1)), guard(__float_as_uint(t2))}, WS_ST + (((cl * 4 + 2 * H + (w >> 1)) * 8 + j) * 16 + 8 * (w & 1)) * 8);
        } else {
          const bool odd = lane & 1;
          const unsigned sx = odd ? o.x : o.z, sy = odd ? o.y : o.w;
          const unsigned rx = __builtin_amdgcn_update_dpp(0, (int)sx, 0xB1, 0xf, 0xf, true), ry = __builtin_amdgcn_update_dpp(0, (int)sy, 0xB1, 0xf, 0xf, true);
          const v4u of = odd ? (v4u){rx, ry, o.z, o.w} : (v4u){o.x, o.y, rx, ry};
          const rsrc_t ro = mkrs(p.out, S_ * D_ * 2);
          __builtin_amdgcn_raw_buffer_store_b128(of, ro, tt * 1024 + pc * 16, (64 * cl + 32 * H + 8 * w) * 1024 + 128 * j, 0);
        }
      }
      STAMP(16 + 10 * layer + 3 * H);
    }
    bar();
  }
  if (b == 0 && tid == 0) { const u64 ne = epoch + 1; st8c(rws, WS_EPOCH, (v2u){(unsigned)ne, (unsigned)(ne >> 32)}); }
  STAMP(31);
  PROF_END();
}
}

extern "C" void kernel_launch(void* const* d_in, const int* in_sizes, int n_in, void* d_out, int out_size, void* d_ws, size_t ws_size, hipStream_t stream) {
  Params p;
  p.q = (const unsigned char*)d_in[0]; p.k = (const unsigned char*)d_in[1]; p.v = (const unsigned char*)d_in[2];
  p.sk = (const unsigned char*)d_in[3]; p.hk = (const unsigned char*)d_in[4]; p.dv = (const unsigned char*)d_in[5];
  p.w1 = (const unsigned char*)d_in[7]; p.w2 = (const unsigned char*)d_in[8];
  p.out = (unsigned char*)d_out; p.ws = (unsigned char*)d_ws;
  (void)hipFuncSetAttribute((const void*)fused_kernel, hipFuncAttributeMaxDynamicSharedMemorySize, 163840);
  fused_kernel<<<256, 256, 163840, stream>>>(p);
}
```

```cpp
#include <hip/hip_runtime.h>
#include <stdint.h>
#define PROF 0
struct Params {
  const void *k, *v, *sk, *ws, *hk, *q, *w1, *w2, *dv, *out;
};
__global__ void __launch_bounds__(256, 1) fused_kernel(Params p) {
  unsigned tid = threadIdx.x;
  asm volatile(
"s_mov_b64 s[4:5], %1\n"
"s_mov_b32 s6, %2\n"
"v_mov_b32 v0, %0\n"
"s_load_dwordx2 s[24:25], s[4:5], 0\n"
"s_load_dwordx2 s[28:29], s[4:5], 8\n"
"s_load_dwordx2 s[32:33], s[4:5], 16\n"
"s_load_dwordx2 s[8:9], s[4:5], 24\n"
"s_load_dwordx2 s[36:37], s[4:5], 32\n"
"s_load_dwordx2 s[20:21], s[4:5], 40\n"
"s_load_dwordx2 s[12:13], s[4:5], 48\n"
"s_load_dwordx2 s[16:17], s[4:5], 56\n"
"s_load_dwordx2 s[44:45], s[4:5], 64\n"
"s_load_dwordx2 s[40:41], s[4:5], 0x48\n"
"Lkstart:\n"
"v_lshrrev_b32 v143, 6, v0\n"
"s_and_b32 s46, s6, 7\n"
"v_readfirstlane_b32 s7, v143\n"
"s_lshr_b32 s47, s6, 3\n"
"v_and_b32 v143, 63, v0\n"
"s_lshr_b32 s50, s47, 3\n"
"v_lshlrev_b32 v142, 4, v143\n"
"s_and_b32 s49, s47, 7\n"
"s_lshl_b32 s48, s46, 2\n"
"s_add_u32 s48, s48, s50\n"
"s_lshl_b32 s51, s46, 6\n"
"s_lshl_b32 s53, s47, 1\n"
"s_add_u32 s51, s51, s53\n"
"s_mov_b32 s61, 0xffff0000\n"
"v_lshrrev_b32 v139, 3, v143\n"
"v_and_b32 v138, 7, v143\n"
"s_lshl_b32 s53, s47, 16\n"
"s_lshl_b32 s58, s7, 14\n"
"s_add_u32 s53, s53, s58\n"
"s_lshl_b32 s58, s46, 7\n"
"s_add_u32 s53, s53, s58\n"
"v_lshlrev_b32 v141, 4, v138\n"
"v_lshl_add_u32 v19, v139, 11, v141\n"
"v_add_u32 v19, s53, v19\n"
"s_sub_u32 s53, s53, 0xc00\n"
"v_lshl_add_u32 v16, v139, 10, v141\n"
"v_add_u32 v16, s53, v16\n"
"v_add_u32 v17, 0x1c00, v16\n"
"v_add_u32 v18, 0x3800, v16\n"
"v_mov_b32 v140, 0x80000000\n"
"v_cmp_gt_u32 vcc, 3, v139\n"
"v_lshl_add_u32 v121, v139, 12, v141\n"
"s_nop 0\n"
"v_cndmask_b32 v18, v140, v18, vcc\n"
"v_cmp_gt_u32 vcc, 32, v143\n"
"s_mul_i32 s73, s7, 0x5000\n"
"s_add_u32 s73, s73, 0x12500\n"
"v_cndmask_b32 v119, v140, v142, vcc\n"
"s_lshl_b32 s53, s46, 18\n"
"s_lshl_b32 s58, s7, 16\n"
"s_add_u32 s53, s53, s58\n"
"s_lshl_b32 s58, s47, 7\n"
"s_add_u32 s53, s53, s58\n"
"s_add_u32 s69, s53, 0x200000\n"
"s_add_u32 s72, s69, 0x8000\n"
"v_lshlrev_b32 v120, 4, v0\n"
"v_mov_b32 v20, -1\n"
"v_mov_b32 v21, -1\n"
"v_mov_b32 v22, -1\n"
"v_mov_b32 v23, -1\n"
"v_mov_b32 v118, 0\n"
"s_mov_b32 s10, 0x1080000\n"
"s_mov_b32 s11, 0x20000\n"
"s_mov_b32 s26, 0x200000\n"
"s_mov_b32 s27, 0x20000\n"
"s_mov_b32 s30, 0x200000\n"
"s_mov_b32 s31, 0x20000\n"
"s_mov_b32 s34, 0x1000\n"
"s_mov_b32 s35, 0x20000\n"
"s_mov_b32 s38, 0x200000\n"
"s_mov_b32 s39, 0x20000\n"
"s_mov_b32 s22, 0x200000\n"
"s_mov_b32 s23, 0x20000\n"
"s_mov_b32 s14, 0x158000\n"
"s_mov_b32 s15, 0x20000\n"
"s_mov_b32 s18, 0x158000\n"
"s_mov_b32 s19, 0x20000\n"
"s_mov_b32 s42, 0x200000\n"
"s_mov_b32 s43, 0x20000\n"
"s_lshl_b32 s53, s46, 9\n"
"s_lshl_b32 s74, s51, 12\n"
"s_add_u32 s75, s74, 0x1000\n"
"s_add_u32 s76, s73, 0x4000\n"
"s_add_u32 s58, s76, 0xc00\n"
"s_mov_b32 m0, s76\n"
"s_waitcnt lgkmcnt(0)\n"
"buffer_load_dwordx4 v16, s[24:27], 0 offen nt lds\n"
"buffer_load_dwordx4 v17, s[24:27], 0 offen offset:1024 nt lds\n"
"buffer_load_dwordx4 v18, s[24:27], 0 offen offset:2048 nt lds\n"
"s_mov_b32 m0, s58\n"
"buffer_load_dwordx4 v[164:167], v19, s[28:31], 0 offen nt\n"
"buffer_load_dwordx4 v119, s[32:35], s53 offen lds\n"
"buffer_load_dwordx4 v[168:171], v19, s[28:31], 0 offen offset:1024 nt\n"
"buffer_load_dwordx2 v[116:117], v118, s[8:11], 0 offen sc1\n"
"buffer_store_dwordx4 v[20:23], v121, s[8:11], s69 offen sc1\n"
"buffer_store_dwordx4 v[20:23], v121, s[8:11], s72 offen sc1\n"
"s_getpc_b64 s[62:63]\n"
"Lpc:\n"
"s_sub_u32 s62, s62, Lpc-Lkstart\n"
"s_subb_u32 s63, s63, 0\n"
"s_lshl_b32 s53, s47, 11\n"
"v_lshlrev_b32 v115, 3, v0\n"
"v_add_u32 v141, s53, v115\n"
"s_mov_b32 s58, Lkend-Lkstart-8\n"
"v_min_u32 v141, s58, v141\n"
"global_load_dwordx2 a[96:97], v141, s[62:63]\n"
"s_mul_i32 s77, s6, 0x1580\n"
"s_add_u32 s53, s77, 0x1000\n"
"buffer_load_dwordx4 a[100:103], v120, s[12:15], s77 offen\n"
"buffer_load_dwordx2 a[104:105], v115, s[12:15], s53 offen\n"
"buffer_load_dwordx4 v[28:31], v120, s[36:39], s74 offen nt\n"
"buffer_load_dwordx4 v[36:39], v120, s[36:39], s75 offen nt\n"
"v_add_u32 v140, -16, v120\n"
"v_cmp_eq_u32 vcc, 0, v143\n"
"v_mov_b32 v141, 0x80000000\n"
"s_nop 0\n"
"v_cndmask_b32 v140, v141, v140, vcc\n"
"buffer_load_dwordx4 v[24:27], v140, s[36:39], s74 offen nt\n"
"buffer_load_dwordx4 v[32:35], v140, s[36:39], s75 offen nt\n"
"v_lshrrev_b32 v141, 2, v139\n"
"s_lshl_b32 s53, s7, 1\n"
"v_add_u32 v141, s53, v141\n"
"v_xor_b32 v141, v141, v138\n"
"v_lshlrev_b32 v141, 4, v141\n"
"v_lshl_add_u32 v106, v138, 10, v141\n"
"v_and_b32 v141, 3, v139\n"
"v_sub_u32 v141, 3, v141\n"
"v_lshl_add_u32 v106, v141, 2, v106\n"
"v_add_u32 v106, 0x10100, v106\n"
"v_xor_b32 v141, s53, v138\n"
"v_lshlrev_b32 v141, 4, v141\n"
"v_lshl_add_u32 v141, v139, 7, v141\n"
"s_lshl_b32 s53, s7, 11\n"
"s_add_u32 s53, s53, 0x10100\n"
"v_add_u32 v104, s53, v141\n"
"v_xor_b32 v105, 16, v104\n"
"v_lshrrev_b32 v141, 1, v139\n"
"v_lshlrev_b32 v141, 5, v141\n"
"v_and_b32 v140, 1, v139\n"
"v_lshl_add_u32 v141, v140, 3, v141\n"
"v_lshl_add_u32 v103, v138, 10, v141\n"
"s_lshl_b32 s53, s46, 12\n"
"s_lshl_b32 s58, s47, 7\n"
"s_add_u32 s32, s53, s58\n"
"v_cmp_eq_u32 vcc, 0, v143\n"
"v_mov_b32 v141, 0x80000000\n"
"s_lshl_b32 s58, s7, 3\n"
"s_add_u32 s53, s32, s58\n"
"v_cndmask_b32 v99, v141, v118, vcc\n"
"s_add_u32 s53, s53, 0x180000\n"
"s_mov_b32 s36, s53\n"
"v_lshlrev_b32 v98, 4, v138\n"
"v_lshl_add_u32 v98, v139, 8, v98\n"
"v_add_u32 v98, s76, v98\n"
"v_lshl_add_u32 v97, v138, 6, s76\n"
"s_lshl_b32 s53, s48, 16\n"
"s_lshl_b32 s58, s7, 13\n"
"s_add_u32 s53, s53, s58\n"
"s_lshl_b32 s58, s49, 7\n"
"s_add_u32 s53, s53, s58\n"
"s_add_u32 s58, s53, 0x8000\n"
"buffer_load_dwordx2 v[52:53], v103, s[20:23], s53 offen nt\n"
"buffer_load_dwordx2 v[54:55], v103, s[20:23], s53 offen offset:16 nt\n"
"buffer_load_dwordx2 v[56:57], v103, s[20:23], s58 offen nt\n"
"buffer_load_dwordx2 v[58:59], v103, s[20:23], s58 offen offset:16 nt\n"
"s_mov_b32 s76, s53\n"
"s_mov_b32 s94, 0x3f803f80\n"
"s_waitcnt vmcnt(15)\n"
"ds_read_b128 v[144:147], v98\n"
"ds_read_b128 v[148:151], v98 offset:128\n"
"ds_read_b128 v[152:155], v98 offset:256\n"
"ds_read_b128 v[156:159], v98 offset:384\n"
"ds_read_b128 v[160:163], v98 offset:512\n"
"ds_read_b128 v[172:175], v97 offset:3072\n"
"ds_read_b128 v[176:179], v97 offset:3088\n"
"ds_read_b128 v[180:183], v97 offset:3104\n"
"ds_read_b128 v[184:187], v97 offset:3120\n"
"s_waitcnt lgkmcnt(8)\n"
"v_lshlrev_b32 v246, 16, v144\n"
"v_and_b32 v247, s61, v144\n"
"s_waitcnt lgkmcnt(7)\n"
"v_lshlrev_b32 v244, 16, v148\n"
"v_and_b32 v245, s61, v148\n"
"s_waitcnt lgkmcnt(6)\n"
"v_lshlrev_b32 v242, 16, v152\n"
"v_and_b32 v243, s61, v152\n"
"s_waitcnt lgkmcnt(5)\n"
"v_lshlrev_b32 v240, 16, v156\n"
"v_and_b32 v241, s61, v156\n"
"s_waitcnt lgkmcnt(4)\n"
"v_lshlrev_b32 v238, 16, v160\n"
"v_and_b32 v239, s61, v160\n"
"s_waitcnt lgkmcnt(3)\n"
"v_lshlrev_b32 v254, 16, v172\n"
"v_lshlrev_b32 v255, 16, v174\n"
"v_and_b32 v252, s61, v172\n"
"v_and_b32 v253, s61, v174\n"
"v_lshlrev_b32 v250, 16, v173\n"
"v_lshlrev_b32 v251, 16, v175\n"
"v_and_b32 v248, s61, v173\n"
"v_and_b32 v249, s61, v175\n"
"v_pk_mul_f32 v[232:233], v[246:247], v[248:249]\n"
"v_pk_mul_f32 v[230:231], v[244:245], v[248:249]\n"
"v_pk_mul_f32 v[228:229], v[244:245], v[250:251]\n"
"v_pk_mul_f32 v[226:227], v[242:243], v[250:251]\n"
"v_pk_mul_f32 v[224:225], v[242:243], v[252:253]\n"
"v_pk_mul_f32 v[222:223], v[240:241], v[252:253]\n"
"v_pk_mul_f32 v[220:221], v[240:241], v[254:255]\n"
"v_pk_mul_f32 v[218:219], v[238:239], v[254:255]\n"
"v_cvt_pk_bf16_f32 v232, v232, v228\n"
"v_cvt_pk_bf16_f32 v233, v233, v229\n"
"v_cvt_pk_bf16_f32 v230, v230, v226\n"
"v_cvt_pk_bf16_f32 v231, v231, v227\n"
"v_dot2_f32_bf16 v232, v232, s94, 0\n"
"v_dot2_f32_bf16 v233, v233, s94, 0\n"
"v_dot2_f32_bf16 v230, v230, s94, 0\n"
"v_dot2_f32_bf16 v231, v231, s94, 0\n"
"v_cvt_pk_bf16_f32 v232, v232, v224\n"
"v_cvt_pk_bf16_f32 v233, v233, v225\n"
"v_cvt_pk_bf16_f32 v230, v230, v222\n"
"v_cvt_pk_bf16_f32 v231, v231, v223\n"
"v_dot2_f32_bf16 v232, v232, s94, 0\n"
"v_dot2_f32_bf16 v233, v233, s94, 0\n"
"v_dot2_f32_bf16 v230, v230, s94, 0\n"
"v_dot2_f32_bf16 v231, v231, s94, 0\n"
"v_cvt_pk_bf16_f32 v232, v232, v220\n"
"v_cvt_pk_bf16_f32 v233, v233, v221\n"
"v_cvt_pk_bf16_f32 v230, v230, v218\n"
"v_cvt_pk_bf16_f32 v231, v231, v219\n"
"v_dot2_f32_bf16 v232, v232, s94, 0\n"
"v_dot2_f32_bf16 v233, v233, s94, 0\n"
"v_dot2_f32_bf16 v230, v230, s94, 0\n"
"v_dot2_f32_bf16 v231, v231, s94, 0\n"
"s_nop 0\n"
"v_cvt_pk_bf16_f32 v216, v232, v233\n"
"s_nop 0\n"
"v_cvt_pk_bf16_f32 v217, v230, v231\n"
"v_lshlrev_b32 v246, 16, v145\n"
"v_and_b32 v247, s61, v145\n"
"v_lshlrev_b32 v244, 16, v149\n"
"v_and_b32 v245, s61, v149\n"
"v_lshlrev_b32 v242, 16, v153\n"
"v_and_b32 v243, s61, v153\n"
"v_lshlrev_b32 v240, 16, v157\n"
"v_and_b32 v241, s61, v157\n"
"v_lshlrev_b32 v238, 16, v161\n"
"v_and_b32 v239, s61, v161\n"
"s_waitcnt lgkmcnt(2)\n"
"v_lshlrev_b32 v254, 16, v176\n"
"v_lshlrev_b32 v255, 16, v178\n"
"v_and_b32 v252, s61, v176\n"
"v_and_b32 v253, s61, v178\n"
"v_lshlrev_b32 v250, 16, v177\n"
"v_lshlrev_b32 v251, 16, v179\n"
"v_and_b32 v248, s61, v177\n"
"v_and_b32 v249, s61, v179\n"
"v_pk_mul_f32 v[232:233], v[246:247], v[248:249]\n"
"v_pk_mul_f32 v[230:231], v[244:245], v[248:249]\n"
"v_pk_mul_f32 v[228:229], v[244:245], v[250:251]\n"
"v_pk_mul_f32 v[226:227], v[242:243], v[250:251]\n"
"v_pk_mul_f32 v[224:225], v[242:243], v[252:253]\n"
"v_pk_mul_f32 v[222:223], v[240:241], v[252:253]\n"
"v_pk_mul_f32 v[220:221], v[240:241], v[254:255]\n"
"v_pk_mul_f32 v[218:219], v[238:239], v[254:255]\n"
"v_cvt_pk_bf16_f32 v232, v232, v228\n"
"v_cvt_pk_bf16_f32 v233, v233, v229\n"
"v_cvt_pk_bf16_f32 v230, v230, v226\n"
"v_cvt_pk_bf16_f32 v231, v231, v227\n"
"v_dot2_f32_bf16 v232, v232, s94, 0\n"
"v_dot2_f32_bf16 v233, v233, s94, 0\n"
"v_dot2_f32_bf16 v230, v230, s94, 0\n"
"v_dot2_f32_bf16 v231, v231, s94, 0\n"
"v_cvt_pk_bf16_f32 v232, v232, v224\n"
"v_cvt_pk_bf16_f32 v233, v233, v225\n"
"v_cvt_pk_bf16_f32 v230, v230, v222\n"
"v_cvt_pk_bf16_f32 v231, v231, v223\n"
"v_dot2_f32_bf16 v232, v232, s94, 0\n"
"v_dot2_f32_bf16 v233, v233, s94, 0\n"
"v_dot2_f32_bf16 v230, v230, s94, 0\n"
"v_dot2_f32_bf16 v231, v231, s94, 0\n"
"v_cvt_pk_bf16_f32 v232, v232, v220\n"
"v_cvt_pk_bf16_f32 v233, v233, v221\n"
"v_cvt_pk_bf16_f32 v230, v230, v218\n"
"v_cvt_pk_bf16_f32 v231, v231, v219\n"
"v_dot2_f32_bf16 v232, v232, s94, 0\n"
"v_dot2_f32_bf16 v233, v233, s94, 0\n"
"v_dot2_f32_bf16 v230, v230, s94, 0\n"
"v_dot2_f32_bf16 v231, v231, s94, 0\n"
"s_nop 0\n"
"v_cvt_pk_bf16_f32 v214, v232, v233\n"
"s_nop 0\n"
"v_cvt_pk_bf16_f32 v215, v230, v231\n"
"v_lshlrev_b32 v246, 16, v146\n"
"v_and_b32 v247, s61, v146\n"
"v_lshlrev_b32 v244, 16, v150\n"
"v_and_b32 v245, s61, v150\n"
"v_lshlrev_b32 v242, 16, v154\n"
"v_and_b32 v243, s61, v154\n"
"v_lshlrev_b32 v240, 16, v158\n"
"v_and_b32 v241, s61, v158\n"
"v_lshlrev_b32 v238, 16, v162\n"
"v_and_b32 v239, s61, v162\n"
"s_waitcnt lgkmcnt(1)\n"
"v_lshlrev_b32 v254, 16, v180\n"
"v_lshlrev_b32 v255, 16, v182\n"
"v_and_b32 v252, s61, v180\n"
"v_and_b32 v253, s61, v182\n"
"v_lshlrev_b32 v250, 16, v181\n"
"v_lshlrev_b32 v251, 16, v183\n"
"v_and_b32 v248, s61, v181\n"
"v_and_b32 v249, s61, v183\n"
"v_pk_mul_f32 v[232:233], v[246:247], v[248:249]\n"
"v_pk_mul_f32 v[230:231], v[244:245], v[248:249]\n"
"v_pk_mul_f32 v[228:229], v[244:245], v[250:251]\n"
"v_pk_mul_f32 v[226:227], v[242:243], v[250:251]\n"
"v_pk_mul_f32 v[224:225], v[242:243], v[252:253]\n"
"v_pk_mul_f32 v[222:223], v[240:241], v[252:253]\n"
"v_pk_mul_f32 v[220:221], v[240:241], v[254:255]\n"
"v_pk_mul_f32 v[218:219], v[238:239], v[254:255]\n"
"v_cvt_pk_bf16_f32 v232, v232, v228\n"
"v_cvt_pk_bf16_f32 v233, v233, v229\n"
"v_cvt_pk_bf16_f32 v230, v230, v226\n"
"v_cvt_pk_bf16_f32 v231, v231, v227\n"
"v_dot2_f32_bf16 v232, v232, s94, 0\n"
"v_dot2_f32_bf16 v233, v233, s94, 0\n"
"v_dot2_f32_bf16 v230, v230, s94, 0\n"
"v_dot2_f32_bf16 v231, v231, s94, 0\n"
"v_cvt_pk_bf16_f32 v232, v232, v224\n"
"v_cvt_pk_bf16_f32 v233, v233, v225\n"
"v_cvt_pk_bf16_f32 v230, v230, v222\n"
"v_cvt_pk_bf16_f32 v231, v231, v223\n"
"v_dot2_f32_bf16 v232, v232, s94, 0\n"
"v_dot2_f32_bf16 v233, v233, s94, 0\n"
"v_dot2_f32_bf16 v230, v230, s94, 0\n"
"v_dot2_f32_bf16 v231, v231, s94, 0\n"
"v_cvt_pk_bf16_f32 v232, v232, v220\n"
"v_cvt_pk_bf16_f32 v233, v233, v221\n"
"v_cvt_pk_bf16_f32 v230, v230, v218\n"
"v_cvt_pk_bf16_f32 v231, v231, v219\n"
"v_dot2_f32_bf16 v232, v232, s94, 0\n"
"v_dot2_f32_bf16 v233, v233, s94, 0\n"
"v_dot2_f32_bf16 v230, v230, s94, 0\n"
"v_dot2_f32_bf16 v231, v231, s94, 0\n"
"s_nop 0\n"
"v_cvt_pk_bf16_f32 v212, v232, v233\n"
"s_nop 0\n"
"v_cvt_pk_bf16_f32 v213, v230, v231\n"
"v_lshlrev_b32 v246, 16, v147\n"
"v_and_b32 v247, s61, v147\n"
"v_lshlrev_b32 v244, 16, v151\n"
"v_and_b32 v245, s61, v151\n"
"v_lshlrev_b32 v242, 16, v155\n"
"v_and_b32 v243, s61, v155\n"
"v_lshlrev_b32 v240, 16, v159\n"
"v_and_b32 v241, s61, v159\n"
"v_lshlrev_b32 v238, 16, v163\n"
"v_and_b32 v239, s61, v163\n"
"s_waitcnt lgkmcnt(0)\n"
"v_lshlrev_b32 v254, 16, v184\n"
"v_lshlrev_b32 v255, 16, v186\n"
"v_and_b32 v252, s61, v184\n"
"v_and_b32 v253, s61, v186\n"
"v_lshlrev_b32 v250, 16, v185\n"
"v_lshlrev_b32 v251, 16, v187\n"
"v_and_b32 v248, s61, v185\n"
"v_and_b32 v249, s61, v187\n"
"v_pk_mul_f32 v[232:233], v[246:247], v[248:249]\n"
"v_pk_mul_f32 v[230:231], v[244:245], v[248:249]\n"
"v_pk_mul_f32 v[228:229], v[244:245], v[250:251]\n"
"v_pk_mul_f32 v[226:227], v[242:243], v[250:251]\n"
"v_pk_mul_f32 v[224:225], v[242:243], v[252:253]\n"
"v_pk_mul_f32 v[222:223], v[240:241], v[252:253]\n"
"v_pk_mul_f32 v[220:221], v[240:241], v[254:255]\n"
"v_pk_mul_f32 v[218:219], v[238:239], v[254:255]\n"
"v_cvt_pk_bf16_f32 v232, v232, v228\n"
"v_cvt_pk_bf16_f32 v233, v233, v229\n"
"v_cvt_pk_bf16_f32 v230, v230, v226\n"
"v_cvt_pk_bf16_f32 v231, v231, v227\n"
"v_dot2_f32_bf16 v232, v232, s94, 0\n"
"v_dot2_f32_bf16 v233, v233, s94, 0\n"
"v_dot2_f32_bf16 v230, v230, s94, 0\n"
"v_dot2_f32_bf16 v231, v231, s94, 0\n"
"v_cvt_pk_bf16_f32 v232, v232, v224\n"
"v_cvt_pk_bf16_f32 v233, v233, v225\n"
"v_cvt_pk_bf16_f32 v230, v230, v222\n"
"v_cvt_pk_bf16_f32 v231, v231, v223\n"
"v_dot2_f32_bf16 v232, v232, s94, 0\n"
"v_dot2_f32_bf16 v233, v233, s94, 0\n"
"v_dot2_f32_bf16 v230, v230, s94, 0\n"
"v_dot2_f32_bf16 v231, v231, s94, 0\n"
"v_cvt_pk_bf16_f32 v232, v232, v220\n"
"v_cvt_pk_bf16_f32 v233, v233, v221\n"
"v_cvt_pk_bf16_f32 v230, v230, v218\n"
"v_cvt_pk_bf16_f32 v231, v231, v219\n"
"v_dot2_f32_bf16 v232, v232, s94, 0\n"
"v_dot2_f32_bf16 v233, v233, s94, 0\n"
"v_dot2_f32_bf16 v230, v230, s94, 0\n"
"v_dot2_f32_bf16 v231, v231, s94, 0\n"
"s_nop 0\n"
"v_cvt_pk_bf16_f32 v210, v232, v233\n"
"s_nop 0\n"
"v_cvt_pk_bf16_f32 v211, v230, v231\n"
"s_waitcnt vmcnt(14)\n"
"s_setprio 0\n"
"v_lshlrev_b32 v246, 16, v164\n"
"v_and_b32 v247, s61, v164\n"
"v_lshlrev_b32 v242, 16, v216\n"
"v_and_b32 v243, s61, v216\n"
"v_lshlrev_b32 v244, 16, v168\n"
"v_and_b32 v245, s61, v168\n"
"v_lshlrev_b32 v240, 16, v217\n"
"v_and_b32 v241, s61, v217\n"
"v_pk_mul_f32 v[246:247], v[246:247], v[242:243]\n"
"v_pk_mul_f32 v[244:245], v[244:245], v[240:241]\n"
"v_cvt_pk_bf16_f32 v242, v244, v246\n"
"v_cvt_pk_bf16_f32 v243, v245, v247\n"
"v_min_u32 v242, -2, v242\n"
"v_min_u32 v243, -2, v243\n"
"ds_write_b32 v106, v242\n"
"ds_write_b32 v106, v243 offset:128\n"
"v_lshlrev_b32 v236, 16, v165\n"
"v_and_b32 v237, s61, v165\n"
"v_lshlrev_b32 v254, 16, v214\n"
"v_and_b32 v255, s61, v214\n"
"v_lshlrev_b32 v234, 16, v169\n"
"v_and_b32 v235, s61, v169\n"
"v_lshlrev_b32 v252, 16, v215\n"
"v_and_b32 v253, s61, v215\n"
"v_pk_mul_f32 v[236:237], v[236:237], v[254:255]\n"
"v_pk_mul_f32 v[234:235], v[234:235], v[252:253]\n"
"v_cvt_pk_bf16_f32 v254, v234, v236\n"
"v_cvt_pk_bf16_f32 v255, v235, v237\n"
"v_min_u32 v254, -2, v254\n"
"v_min_u32 v255, -2, v255\n"
"ds_write_b32 v106, v254 offset:256\n"
"ds_write_b32 v106, v255 offset:384\n"
"s_waitcnt vmcnt(13)\n"
"v_readfirstlane_b32 s56, v116\n"
"v_readfirstlane_b32 s57, v117\n"
"s_xor_b32 s54, s56, 0x7f4a7c15\n"
"s_xor_b32 s55, s57, 0x9e3779b9\n"
"s_mul_i32 s53, s54, 0xbf58476d\n"
"s_mul_hi_u32 s58, s54, 0x1ce4e5b9\n"
"s_mul_i32 s55, s55, 0x1ce4e5b9\n"
"s_add_u32 s53, s53, s58\n"
"s_mul_i32 s54, s54, 0x1ce4e5b9\n"
"s_add_u32 s55, s55, s53\n"
"s_lshr_b64 s[62:63], s[54:55], 29\n"
"s_xor_b64 s[54:55], s[62:63], s[54:55]\n"
"v_mov_b32 v100, s54\n"
"v_mov_b32 v101, s55\n"
"s_waitcnt vmcnt(11)\n"
"buffer_store_dwordx2 v[100:101], v99, s[8:11], s36 offen sc1\n"
"v_lshlrev_b32 v246, 16, v166\n"
"v_and_b32 v247, s61, v166\n"
"v_lshlrev_b32 v242, 16, v212\n"
"v_and_b32 v243, s61, v212\n"
"v_lshlrev_b32 v244, 16, v170\n"
"v_and_b32 v245, s61, v170\n"
"v_lshlrev_b32 v240, 16, v213\n"
"v_and_b32 v241, s61, v213\n"
"v_pk_mul_f32 v[246:247], v[246:247], v[242:243]\n"
"v_pk_mul_f32 v[244:245], v[244:245], v[240:241]\n"
"v_cvt_pk_bf16_f32 v242, v244, v246\n"
"v_cvt_pk_bf16_f32 v243, v245, v247\n"
"v_min_u32 v242, -2, v242\n"
"v_min_u32 v243, -2, v243\n"
"ds_write_b32 v106, v242 offset:512\n"
"ds_write_b32 v106, v243 offset:640\n"
"v_lshlrev_b32 v236, 16, v167\n"
"v_and_b32 v237, s61, v167\n"
"v_lshlrev_b32 v254, 16, v210\n"
"v_and_b32 v255, s61, v210\n"
"v_lshlrev_b32 v234, 16, v171\n"
"v_and_b32 v235, s61, v171\n"
"v_lshlrev_b32 v252, 16, v211\n"
"v_and_b32 v253, s61, v211\n"
"v_pk_mul_f32 v[236:237], v[236:237], v[254:255]\n"
"v_pk_mul_f32 v[234:235], v[234:235], v[252:253]\n"
"v_cvt_pk_bf16_f32 v254, v234, v236\n"
"v_cvt_pk_bf16_f32 v255, v235, v237\n"
"v_min_u32 v254, -2, v254\n"
"v_min_u32 v255, -2, v255\n"
"ds_write_b32 v106, v254 offset:768\n"
"ds_write_b32 v106, v255 offset:896\n"
"s_waitcnt lgkmcnt(0)\n"
"s_barrier\n"
"ds_read_b128 v[16:19], v104\n"
"ds_read_b128 v[60:63], v105 offset:1024\n"
"s_waitcnt lgkmcnt(1)\n"
"buffer_store_dwordx4 v[16:19], v121, s[8:11], s69 offen sc1\n"
"s_waitcnt lgkmcnt(0)\n"
"buffer_store_dwordx4 v[60:63], v121, s[8:11], s72 offen sc1\n"
"s_barrier\n"
"s_lshl_b32 s31, s46, 12\n"
"s_lshl_b32 s58, s7, 10\n"
"s_add_u32 s31, s31, s58\n"
"s_add_u32 s31, s31, 0x180000\n"
"v_bfe_u32 v141, v143, 1, 3\n"
"v_and_b32 v140, 1, v143\n"
"v_lshlrev_b32 v140, 4, v140\n"
"v_lshl_add_u32 v133, v141, 7, v140\n"
"v_add_u32 v141, 1, v0\n"
"v_and_b32 v140, 1, v141\n"
"v_lshlrev_b32 v140, 6, v140\n"
"v_lshl_add_u32 v140, v141, 7, v140\n"
"v_bfe_u32 v141, v141, 1, 2\n"
"v_lshl_add_u32 v40, v141, 4, v140\n"
"v_xor_b32 v41, 16, v40\n"
"v_xor_b32 v42, 32, v40\n"
"v_xor_b32 v43, 48, v40\n"
"v_xor_b32 v44, 64, v40\n"
"v_xor_b32 v45, 0x50, v40\n"
"v_xor_b32 v46, 0x60, v40\n"
"v_xor_b32 v47, 0x70, v40\n"
"s_lshl_b32 s53, s51, 1\n"
"s_load_dword s68, s[44:45], s53\n"
"s_lshl_b32 s36, s51, 12\n"
"s_add_u32 s36, s36, 0x200000\n"
"s_add_u32 s69, s36, 0x1000\n"
"s_waitcnt vmcnt(8)\n"
"v_mov_b32_dpp v24, v28 wave_shr:1 row_mask:0xf bank_mask:0xf\n"
"v_mov_b32_dpp v25, v29 wave_shr:1 row_mask:0xf bank_mask:0xf\n"
"v_mov_b32_dpp v26, v30 wave_shr:1 row_mask:0xf bank_mask:0xf\n"
"v_mov_b32_dpp v27, v31 wave_shr:1 row_mask:0xf bank_mask:0xf\n"
"ds_write_b128 v43, v[26:29]\n"
"v_alignbit_b32 v232, v25, v24, 16\n"
"v_alignbit_b32 v233, v26, v25, 16\n"
"v_alignbit_b32 v234, v27, v26, 16\n"
"v_alignbit_b32 v235, v28, v27, 16\n"
"v_alignbit_b32 v236, v29, v28, 16\n"
"v_alignbit_b32 v237, v30, v29, 16\n"
"ds_write_b128 v47, v[28:31]\n"
"v_alignbit_b32 v240, v26, v25, 16\n"
"v_alignbit_b32 v241, v27, v26, 16\n"
"v_alignbit_b32 v242, v28, v27, 16\n"
"v_alignbit_b32 v243, v29, v28, 16\n"
"v_alignbit_b32 v244, v30, v29, 16\n"
"v_alignbit_b32 v245, v31, v30, 16\n"
"ds_write_b128 v40, v[232:235]\n"
"v_mov_b32 v248, v25\n"
"v_mov_b32 v249, v26\n"
"v_mov_b32 v250, v27\n"
"v_mov_b32 v251, v28\n"
"v_mov_b32 v252, v29\n"
"v_mov_b32 v253, v30\n"
"ds_write_b128 v44, v[234:237]\n"
"ds_write_b128 v42, v[240:243]\n"
"ds_write_b128 v46, v[242:245]\n"
"ds_write_b128 v41, v[248:251]\n"
"ds_write_b128 v45, v[250:253]\n"
"s_waitcnt vmcnt(7)\n"
"v_mov_b32_dpp v32, v36 wave_shr:1 row_mask:0xf bank_mask:0xf\n"
"v_mov_b32_dpp v33, v37 wave_shr:1 row_mask:0xf bank_mask:0xf\n"
"v_mov_b32_dpp v34, v38 wave_shr:1 row_mask:0xf bank_mask:0xf\n"
"v_mov_b32_dpp v35, v39 wave_shr:1 row_mask:0xf bank_mask:0xf\n"
"ds_write_b128 v43, v[34:37] offset:32896\n"
"v_alignbit_b32 v232, v33, v32, 16\n"
"v_alignbit_b32 v233, v34, v33, 16\n"
"v_alignbit_b32 v234, v35, v34, 16\n"
"v_alignbit_b32 v235, v36, v35, 16\n"
"v_alignbit_b32 v236, v37, v36, 16\n"
"v_alignbit_b32 v237, v38, v37, 16\n"
"buffer_load_dwordx4 v[48:51], v133, s[8:11], s31 offen sc1\n"
"ds_write_b128 v47, v[36:39] offset:32896\n"
"v_alignbit_b32 v240, v34, v33, 16\n"
"v_alignbit_b32 v241, v35, v34, 16\n"
"v_alignbit_b32 v242, v36, v35, 16\n"
"v_alignbit_b32 v243, v37, v36, 16\n"
"v_alignbit_b32 v244, v38, v37, 16\n"
"v_alignbit_b32 v245, v39, v38, 16\n"
"ds_write_b128 v40, v[232:235] offset:32896\n"
"v_mov_b32 v248, v33\n"
"v_mov_b32 v249, v34\n"
"v_mov_b32 v250, v35\n"
"v_mov_b32 v251, v36\n"
"v_mov_b32 v252, v37\n"
"v_mov_b32 v253, v38\n"
"ds_write_b128 v44, v[234:237] offset:32896\n"
"ds_write_b128 v42, v[240:243] offset:32896\n"
"ds_write_b128 v46, v[242:245] offset:32896\n"
"ds_write_b128 v41, v[248:251] offset:32896\n"
"ds_write_b128 v45, v[250:253] offset:32896\n"
"s_mul_i32 s53, s49, 11\n"
"s_mul_i32 s58, s49, 10\n"
"s_add_u32 s58, s58, 6\n"
"s_cmp_lt_u32 s49, 6\n"
"s_cselect_b32 s52, s53, s58\n"
"s_cselect_b32 s4, 3, 2\n"
"s_cselect_b32 s60, 9, 8\n"
"s_mul_i32 s53, s7, 3\n"
"s_cmp_eq_u32 s7, 3\n"
"s_cselect_b32 s53, s60, s53\n"
"s_add_u32 s52, s52, s53\n"
"s_cmp_lt_u32 s7, s4\n"
"s_cselect_b64 s[70:71], -1, 0\n"
"v_lshrrev_b32 v110, 4, v143\n"
"v_bfe_u32 v109, v143, 2, 2\n"
"v_and_b32 v108, 3, v143\n"
"v_and_b32 v107, 15, v143\n"
"s_lshr_b32 s53, s7, 1\n"
"s_add_u32 s53, s53, s51\n"
"s_lshl_b32 s53, s53, 12\n"
"s_and_b32 s58, s7, 1\n"
"s_lshl_b32 s58, s58, 11\n"
"s_add_u32 s28, s53, s58\n"
"s_add_u32 s28, s28, 0x400000\n"
"s_mul_i32 s29, s48, 0x2c000\n"
"s_add_u32 s29, s29, 0xa00000\n"
"s_lshr_b32 s53, s49, 1\n"
"s_mul_i32 s53, s53, 22\n"
"s_add_u32 s53, s53, s7\n"
"s_and_b32 s58, s49, 1\n"
"s_mul_i32 s59, s58, s4\n"
"s_lshl_b32 s58, s58, 2\n"
"s_add_u32 s58, s58, s53\n"
"s_lshl_b32 s58, s58, 10\n"
"s_add_u32 s33, s29, s58\n"
"s_add_u32 s53, s53, s59\n"
"s_add_u32 s53, s53, 16\n"
"s_lshl_b32 s53, s53, 10\n"
"s_add_u32 s53, s53, s29\n"
"s_lshl_b32 s59, s6, 2\n"
"s_add_u32 s59, s59, s7\n"
"s_lshl_b32 s59, s59, 10\n"
"s_add_u32 s59, s59, 0xf80000\n"
"s_cmp_lg_u64 s[70:71], 0\n"
"s_cselect_b32 s34, s53, s59\n"
"s_cselect_b32 s35, 0x16000, 0\n"
"s_lshl_b32 s53, s48, 16\n"
"s_lshr_b32 s58, s7, 1\n"
"s_lshl_b32 s58, s58, 14\n"
"s_add_u32 s53, s53, s58\n"
"s_lshl_b32 s58, s49, 11\n"
"s_add_u32 s53, s53, s58\n"
"s_and_b32 s58, s7, 1\n"
"s_lshl_b32 s58, s58, 7\n"
"s_add_u32 s53, s53, s58\n"
"s_add_u32 s30, s53, 0x600000\n"
"v_lshrrev_b32 v141, 5, v143\n"
"v_lshl_add_u32 v13, v141, 4, v107\n"
"v_and_b32 v140, 1, v110\n"
"v_lshl_add_u32 v13, v140, 5, v13\n"
"v_lshlrev_b32 v13, 4, v13\n"
"v_and_b32 v140, 3, v139\n"
"v_lshl_add_u32 v15, v140, 4, v138\n"
"v_lshlrev_b32 v15, 4, v15\n"
"v_lshl_add_u32 v15, v141, 10, v15\n"
"v_lshrrev_b32 v141, 5, v0\n"
"v_lshlrev_b32 v141, 17, v141\n"
"v_and_b32 v140, 3, v139\n"
"v_lshl_add_u32 v141, v140, 10, v141\n"
"v_lshl_add_u32 v141, v138, 4, v141\n"
"v_mov_b32 v140, 0x80000000\n"
"v_cmp_gt_u32 vcc, 0x80, v0\n"
"s_nop 1\n"
"v_cndmask_b32 v95, v140, v141, vcc\n"
"v_mov_b32 v16, 0\n"
"v_mov_b32 v17, 0\n"
"v_mov_b32 v18, 0\n"
"v_mov_b32 v19, 0\n"
"v_lshrrev_b32 v141, 3, v0\n"
"v_mul_u32_u24 v141, 0x8080, v141\n"
"v_lshl_add_u32 v141, v138, 4, v141\n"
"v_add_u32 v140, 0x26800, v142\n"
"v_cmp_gt_u32 vcc, 16, v0\n"
"s_nop 1\n"
"v_cndmask_b32 v141, v140, v141, vcc\n"
"v_mul_u32_u24 v140, 0x900, v110\n"
"v_lshl_add_u32 v140, v107, 4, v140\n"
"v_add_u32 v140, 0x10100, v140\n"
"s_cmp_eq_u32 s7, 1\n"
"s_cselect_b64 vcc, -1, 0\n"
"v_cndmask_b32 v141, v141, v140, vcc\n"
"ds_write_b128 v141, v[16:19]\n"
"s_waitcnt vmcnt(7)\n"
"v_lshlrev_b32 v16, 16, v52\n"
"v_and_b32 v17, s61, v52\n"
"v_lshlrev_b32 v18, 16, v53\n"
"v_and_b32 v19, s61, v53\n"
"s_waitcnt vmcnt(6)\n"
"v_lshlrev_b32 v24, 16, v54\n"
"v_and_b32 v25, s61, v54\n"
"v_lshlrev_b32 v26, 16, v55\n"
"v_and_b32 v27, s61, v55\n"
"v_add_f32 v28, v16, v17\n"
"v_mul_f32 v29, v16, v16\n"
"v_add_f32 v30, v18, v19\n"
"v_mul_f32 v31, v17, v17\n"
"v_add_f32 v28, v28, v24\n"
"v_fmac_f32 v29, v18, v18\n"
"v_add_f32 v30, v30, v25\n"
"v_fmac_f32 v31, v19, v19\n"
"v_add_f32 v28, v28, v26\n"
"v_fmac_f32 v29, v24, v24\n"
"v_add_f32 v30, v30, v27\n"
"v_fmac_f32 v31, v25, v25\n"
"v_add_f32 v28, v28, v30\n"
"v_fmac_f32 v29, v26, v26\n"
"v_fmac_f32 v31, v27, v27\n"
"v_add_f32 v29, v29, v31\n"
"v_add_f32_dpp v30, v28, v28 row_ror:8 row_mask:0xf bank_mask:0xf\n"
"s_nop 0\n"
"v_add_f32_dpp v31, v29, v29 row_ror:8 row_mask:0xf bank_mask:0xf\n"
"s_nop 1\n"
"v_permlane16_swap_b32 v30, v31\n"
"v_add_f32 v30, v30, v31\n"
"v_mov_b32 v31, v30\n"
"s_nop 1\n"
"v_permlane32_swap_b32 v30, v31\n"
"v_add_f32 v28, v30, v31\n"
"v_mov_b32 v29, v28\n"
"v_mov_b32 v30, v28\n"
"s_nop 1\n"
"v_permlane16_swap_b32 v30, v29\n"
"v_min_u32 v28, -2, v28\n"
"v_min_u32 v29, -2, v29\n"
"s_waitcnt vmcnt(5)\n"
"v_lshlrev_b32 v16, 16, v56\n"
"v_and_b32 v17, s61, v56\n"
"v_lshlrev_b32 v18, 16, v57\n"
"v_and_b32 v19, s61, v57\n"
"s_waitcnt vmcnt(4)\n"
"v_lshlrev_b32 v24, 16, v58\n"
"v_and_b32 v25, s61, v58\n"
"v_lshlrev_b32 v26, 16, v59\n"
"v_and_b32 v27, s61, v59\n"
"v_add_f32 v32, v16, v17\n"
"v_mul_f32 v33, v16, v16\n"
"v_add_f32 v34, v18, v19\n"
"v_mul_f32 v35, v17, v17\n"
"v_add_f32 v32, v32, v24\n"
"v_fmac_f32 v33, v18, v18\n"
"v_add_f32 v34, v34, v25\n"
"v_fmac_f32 v35, v19, v19\n"
"v_add_f32 v32, v32, v26\n"
"v_fmac_f32 v33, v24, v24\n"
"v_add_f32 v34, v34, v27\n"
"v_fmac_f32 v35, v25, v25\n"
"v_add_f32 v32, v32, v34\n"
"v_fmac_f32 v33, v26, v26\n"
"v_fmac_f32 v35, v27, v27\n"
"v_add_f32 v33, v33, v35\n"
"v_add_f32_dpp v34, v32, v32 row_ror:8 row_mask:0xf bank_mask:0xf\n"
"s_nop 0\n"
"v_add_f32_dpp v35, v33, v33 row_ror:8 row_mask:0xf bank_mask:0xf\n"
"s_nop 1\n"
"v_permlane16_swap_b32 v34, v35\n"
"v_add_f32 v34, v34, v35\n"
"v_mov_b32 v35, v34\n"
"s_nop 1\n"
"v_permlane32_swap_b32 v34, v35\n"
"v_add_f32 v32, v34, v35\n"
"v_mov_b32 v33, v32\n"
"v_mov_b32 v34, v32\n"
"s_nop 1\n"
"v_permlane16_swap_b32 v34, v33\n"
"v_min_u32 v32, -2, v32\n"
"v_min_u32 v33, -2, v33\n"
"s_waitcnt vmcnt(0)\n"
"v_cmp_ne_u64 s[62:63], s[54:55], v[48:49]\n"
"v_cmp_ne_u64 vcc, s[54:55], v[50:51]\n"
"s_or_b64 vcc, vcc, s[62:63]\n"
"s_cbranch_vccnz Lslow1_f1\n"
"Lback1_f1:\n"
"buffer_load_dwordx4 v[64:67], v120, s[8:11], s36 offen sc1\n"
"buffer_load_dwordx4 v[68:71], v120, s[8:11], s69 offen sc1\n"
"s_or_b32 s62, s54, 0x11111111\n"
"s_or_b32 s63, s55, 0x11111111\n"
"v_mov_b32 v141, 0x80000000\n"
"v_cndmask_b32 v132, 0, v141, s[62:63]\n"
"v_lshrrev_b32 v141, 2, v13\n"
"ds_bpermute_b32 v14, v141, v132\n"
"v_mov_b32 v16, 0x3f803f80\n"
"v_mov_b32 v17, 0x3f803f80\n"
"v_mov_b32 v18, 0x3f803f80\n"
"v_mov_b32 v19, 0x3f803f80\n"
"s_add_u32 s59, s28, 0\n"
"buffer_store_dwordx4 v[20:23], v142, s[8:11], s59 offen sc1\n"
"s_add_u32 s59, s28, 0x400\n"
"buffer_store_dwordx4 v[20:23], v142, s[8:11], s59 offen sc1\n"
"s_add_u32 s59, s30, 0\n"
"buffer_store_dwordx4 v[20:23], v15, s[8:11], s59 offen sc1\n"
"s_add_u32 s59, s30, 0x8000\n"
"buffer_store_dwordx4 v[20:23], v15, s[8:11], s59 offen sc1\n"
"s_lshl_b32 s60, s48, 12\n"
"s_lshl_b32 s59, s49, 7\n"
"s_add_u32 s60, s60, s59\n"
"s_add_u32 s59, s60, 0x100000\n"
"buffer_store_dwordx4 v[20:23], v95, s[8:11], s59 offen sc1\n"
"s_lshl_b32 s53, s52, 5\n"
"s_movk_i32 s58, 0xab\n"
"s_movk_i32 s59, 0xac0\n"
"v_add_u32 v141, 0, v143\n"
"v_mul_u32_u24 v140, s58, v141\n"
"v_lshrrev_b32 v140, 10, v140\n"
"v_mul_u32_u24 v114, 6, v140\n"
"v_sub_u32 v141, v141, v114\n"
"v_cmp_gt_u32 vcc, 4, v141\n"
"s_or_b64 vcc, vcc, s[70:71]\n"
"v_add_u32 v114, -4, v141\n"
"v_cndmask_b32 v141, v114, v141, vcc\n"
"v_lshl_add_u32 v141, v141, 4, s53\n"
"v_mad_u32_u24 v114, v140, s59, v141\n"
"v_add_u32 v141, 64, v143\n"
"v_mul_u32_u24 v140, s58, v141\n"
"v_lshrrev_b32 v140, 10, v140\n"
"v_mul_u32_u24 v113, 6, v140\n"
"v_sub_u32 v141, v141, v113\n"
"v_cmp_gt_u32 vcc, 4, v141\n"
"s_or_b64 vcc, vcc, s[70:71]\n"
"v_add_u32 v113, -4, v141\n"
"v_cndmask_b32 v141, v113, v141, vcc\n"
"v_lshl_add_u32 v141, v141, 4, s53\n"
"v_mad_u32_u24 v113, v140, s59, v141\n"
"v_add_u32 v113, 0xfffffc00, v113\n"
"v_add_u32 v141, 0x80, v143\n"
"v_mul_u32_u24 v140, s58, v141\n"
"v_lshrrev_b32 v140, 10, v140\n"
"v_mul_u32_u24 v112, 6, v140\n"
"v_sub_u32 v141, v141, v112\n"
"v_cmp_gt_u32 vcc, 4, v141\n"
"s_or_b64 vcc, vcc, s[70:71]\n"
"v_add_u32 v112, -4, v141\n"
"v_cndmask_b32 v141, v112, v141, vcc\n"
"v_lshl_add_u32 v141, v141, 4, s53\n"
"v_mad_u32_u24 v112, v140, s59, v141\n"
"v_add_u32 v112, 0xfffff800, v112\n"
"s_add_u32 s60, s50, 0\n"
"s_and_b32 s60, s60, 3\n"
"s_mul_i32 s5, s60, 0x56000\n"
"s_add_u32 s60, s50, 1\n"
"s_and_b32 s60, s60, 3\n"
"s_mul_i32 s24, s60, 0x56000\n"
"s_add_u32 s60, s50, 2\n"
"s_and_b32 s60, s60, 3\n"
"s_mul_i32 s25, s60, 0x56000\n"
"s_add_u32 s60, s50, 3\n"
"s_and_b32 s60, s60, 3\n"
"s_mul_i32 s26, s60, 0x56000\n"
"v_lshrrev_b32 v141, 1, v110\n"
"v_lshl_add_u32 v140, v141, 4, v109\n"
"v_and_b32 v141, 1, v110\n"
"v_lshl_add_u32 v140, v141, 2, v140\n"
"v_mul_u32_u24 v111, 0x60, v140\n"
"v_lshl_add_u32 v111, v108, 3, v111\n"
"v_add_u32 v111, s73, v111\n"
"s_mov_b32 s37, 0x3b000000\n"
"s_mov_b32 s38, 0x80008000\n"
"s_lshl_b32 s53, s48, 12\n"
"s_lshr_b32 s58, s7, 1\n"
"s_lshl_b32 s58, s58, 10\n"
"s_add_u32 s74, s53, s58\n"
"s_and_b32 s58, s7, 1\n"
"s_lshl_b32 s58, s58, 6\n"
"s_add_u32 s74, s74, s58\n"
"s_add_u32 s74, s74, 0x100000\n"
"s_lshl_b32 s58, s49, 7\n"
"s_add_u32 s39, s74, s58\n"
"v_lshlrev_b32 v141, 3, v143\n"
"v_mov_b32 v140, 0x80000000\n"
"v_cmp_gt_u32 vcc, 8, v143\n"
"s_nop 1\n"
"v_cndmask_b32 v137, v140, v141, vcc\n"
"s_lshr_b32 s53, s7, 1\n"
"s_and_b32 s58, s7, 1\n"
"s_mul_i32 s59, s53, 0x1200\n"
"s_lshl_b32 s60, s58, 7\n"
"s_sub_u32 s59, s59, s60\n"
"s_add_u32 s59, s59, 0x10190\n"
"v_lshrrev_b32 v141, 1, v110\n"
"v_mul_u32_u24 v94, 0x900, v141\n"
"v_and_b32 v140, 1, v110\n"
"v_add_u32 v96, v107, v140\n"
"v_lshl_add_u32 v94, v96, 4, v94\n"
"v_add_u32 v94, s59, v94\n"
"v_lshl_add_u32 v141, v140, 1, v141\n"
"v_lshlrev_b32 v141, 3, v141\n"
"v_sub_u32 v141, v107, v141\n"
"s_add_u32 s60, s60, 24\n"
"v_add_u32 v141, s60, v141\n"
"s_mul_i32 s59, s53, 0x8080\n"
"v_add_u32 v96, 0, v141\n"
"v_bfe_u32 v140, v96, 3, 1\n"
"v_lshlrev_b32 v140, 2, v140\n"
"v_xor_b32 v93, v96, v140\n"
"v_bfe_u32 v140, v96, 4, 2\n"
"v_xor_b32 v93, v93, v140\n"
"v_lshlrev_b32 v93, 4, v93\n"
"v_add_u32 v93, s59, v93\n"
"v_add_u32 v96, 32, v141\n"
"v_bfe_u32 v140, v96, 3, 1\n"
"v_lshlrev_b32 v140, 2, v140\n"
"v_xor_b32 v92, v96, v140\n"
"v_bfe_u32 v140, v96, 4, 2\n"
"v_xor_b32 v92, v92, v140\n"
"v_lshlrev_b32 v92, 4, v92\n"
"v_add_u32 v92, s59, v92\n"
"v_lshl_add_u32 v91, v107, 2, v110\n"
"v_bfe_u32 v140, v143, 1, 2\n"
"v_xor_b32 v91, v91, v140\n"
"v_lshlrev_b32 v91, 4, v91\n"
"s_lshl_b32 s59, s7, 13\n"
"s_add_u32 s59, s59, 0\n"
"v_add_u32 v91, s59, v91\n"
"v_and_b32 v141, 1, v0\n"
"v_mul_u32_u24 v90, 0x900, v141\n"
"v_lshrrev_b32 v141, 1, v0\n"
"v_lshl_add_u32 v90, v141, 4, v90\n"
"v_add_u32 v90, 0x10200, v90\n"
"s_lshl_b32 s59, s53, 14\n"
"s_lshl_b32 s60, s58, 12\n"
"s_add_u32 s59, s59, s60\n"
"s_add_u32 s59, s59, 0\n"
"v_lshlrev_b32 v89, 1, v143\n"
"v_xor_b32 v89, v89, v109\n"
"v_lshlrev_b32 v89, 4, v89\n"
"v_add_u32 v89, s59, v89\n"
"v_xor_b32 v88, 16, v89\n"
"s_mul_i32 s59, s53, 0x1200\n"
"s_lshl_b32 s60, s58, 10\n"
"s_add_u32 s59, s59, s60\n"
"s_add_u32 s59, s59, 0x10200\n"
"v_and_b32 v141, 1, v143\n"
"v_mul_u32_u24 v87, 0x900, v141\n"
"v_lshrrev_b32 v141, 1, v143\n"
"v_lshl_add_u32 v87, v141, 4, v87\n"
"v_add_u32 v87, s59, v87\n"
"s_lshl_b32 s59, s49, 12\n"
"s_lshl_b32 s60, s7, 10\n"
"s_add_u32 s59, s59, s60\n"
"v_lshl_add_u32 v102, v138, 7, s59\n"
"s_lshl_b32 s60, s46, 12\n"
"s_lshl_b32 s59, s50, 10\n"
"s_add_u32 s62, s60, s59\n"
"v_lshl_add_u32 v96, v138, 7, s62\n"
"v_and_b32 v141, 1, v139\n"
"v_cmp_eq_u32 vcc, 0, v141\n"
"s_nop 1\n"
"v_cndmask_b32 v102, v96, v102, vcc\n"
"v_lshlrev_b32 v141, 15, v0\n"
"v_mov_b32 v140, 0x80000000\n"
"v_cmp_gt_u32 vcc, 8, v0\n"
"s_lshl_b32 s75, s46, 15\n"
"s_nop 0\n"
"v_cndmask_b32 v86, v140, v141, vcc\n"
"s_add_u32 s75, s75, 0x1c0000\n"
"s_waitcnt vmcnt(6)\n"
"v_max3_u32 v96, v64, v65, v66\n"
"s_waitcnt vmcnt(5)\n"
"v_max3_u32 v96, v96, v67, v68\n"
"v_max3_u32 v96, v96, v69, v70\n"
"v_max_u32 v96, v96, v71\n"
"v_cmp_eq_u32 vcc, -1, v96\n"
"s_cbranch_vccnz Lslow2_kvt\n"
"Lback2_kvt:\n"
"s_add_u32 s53, s77, 0x1000\n"
"buffer_load_dwordx4 a[108:111], v120, s[16:19], s77 offen\n"
"buffer_load_dwordx2 a[112:113], v115, s[16:19], s53 offen\n"
"ds_write_b128 v90, v[64:67]\n"
"ds_write_b128 v90, v[68:71] offset:4608\n"
"s_waitcnt lgkmcnt(0)\n"
"s_barrier\n"
"ds_read_b128 a[64:67], v94 offset:96\n"
"ds_read_b128 a[32:35], v93\n"
"ds_read_b128 a[68:71], v94 offset:352\n"
"ds_read_b128 a[72:75], v94 offset:608\n"
"ds_read_b128 a[76:79], v94 offset:864\n"
"ds_read_b128 a[80:83], v94 offset:1120\n"
"ds_read_b128 a[84:87], v94 offset:1376\n"
"ds_read_b128 a[88:91], v94 offset:1632\n"
"ds_read_b128 a[92:95], v94 offset:1888\n"
"ds_read_b128 a[36:39], v93 offset:4096\n"
"ds_read_b128 a[40:43], v93 offset:8192\n"
"ds_read_b128 a[44:47], v93 offset:12288\n"
"s_waitcnt lgkmcnt(10)\n"
"v_mfma_f32_16x16x32_bf16 a[0:3], a[32:35], a[64:67], 0\n"
"s_waitcnt lgkmcnt(9)\n"
"v_mfma_f32_16x16x32_bf16 a[4:7], a[32:35], a[68:71], 0\n"
"s_waitcnt lgkmcnt(8)\n"
"v_mfma_f32_16x16x32_bf16 a[8:11], a[32:35], a[72:75], 0\n"
"ds_read_b128 a[48:51], v93 offset:16384\n"
"s_waitcnt lgkmcnt(8)\n"
"v_mfma_f32_16x16x32_bf16 a[12:15], a[32:35], a[76:79], 0\n"
"s_waitcnt lgkmcnt(7)\n"
"v_mfma_f32_16x16x32_bf16 a[16:19], a[32:35], a[80:83], 0\n"
"ds_read_b128 a[52:55], v93 offset:20480\n"
"s_waitcnt lgkmcnt(7)\n"
"v_mfma_f32_16x16x32_bf16 a[20:23], a[32:35], a[84:87], 0\n"
"s_waitcnt lgkmcnt(6)\n"
"v_mfma_f32_16x16x32_bf16 a[24:27], a[32:35], a[88:91], 0\n"
"ds_read_b128 a[56:59], v93 offset:24576\n"
"s_waitcnt lgkmcnt(6)\n"
"v_mfma_f32_16x16x32_bf16 a[28:31], a[32:35], a[92:95], 0\n"
"ds_read_b128 a[92:95], v94 offset:1856\n"
"s_waitcnt lgkmcnt(6)\n"
"v_mfma_f32_16x16x32_bf16 a[4:7], a[36:39], a[64:67], a[4:7]\n"
"ds_read_b128 a[60:63], v93 offset:28672\n"
"ds_read_b128 a[32:35], v92\n"
"v_mfma_f32_16x16x32_bf16 a[8:11], a[36:39], a[68:71], a[8:11]\n"
"v_mfma_f32_16x16x32_bf16 a[12:15], a[36:39], a[72:75], a[12:15]\n"
"v_mfma_f32_16x16x32_bf16 a[16:19], a[36:39], a[76:79], a[16:19]\n"
"v_mfma_f32_16x16x32_bf16 a[20:23], a[36:39], a[80:83], a[20:23]\n"
"v_mfma_f32_16x16x32_bf16 a[24:27], a[36:39], a[84:87], a[24:27]\n"
"v_mfma_f32_16x16x32_bf16 a[28:31], a[36:39], a[88:91], a[28:31]\n"
"ds_read_b128 a[88:91], v94 offset:1600\n"
"s_waitcnt lgkmcnt(8)\n"
"v_mfma_f32_16x16x32_bf16 a[8:11], a[40:43], a[64:67], a[8:11]\n"
"ds_read_b128 a[36:39], v92 offset:4096\n"
"v_mfma_f32_16x16x32_bf16 a[12:15], a[40:43], a[68:71], a[12:15]\n"
"v_mfma_f32_16x16x32_bf16 a[16:19], a[40:43], a[72:75], a[16:19]\n"
"v_mfma_f32_16x16x32_bf16 a[20:23], a[40:43], a[76:79], a[20:23]\n"
"v_mfma_f32_16x16x32_bf16 a[24:27], a[40:43], a[80:83], a[24:27]\n"
"v_mfma_f32_16x16x32_bf16 a[28:31], a[40:43], a[84:87], a[28:31]\n"
"ds_read_b128 a[84:87], v94 offset:1344\n"
"s_waitcnt lgkmcnt(9)\n"
"v_mfma_f32_16x16x32_bf16 a[12:15], a[44:47], a[64:67], a[12:15]\n"
"ds_read_b128 a[40:43], v92 offset:8192\n"
"v_mfma_f32_16x16x32_bf16 a[16:19], a[44:47], a[68:71], a[16:19]\n"
"v_mfma_f32_16x16x32_bf16 a[20:23], a[44:47], a[72:75], a[20:23]\n"
"v_mfma_f32_16x16x32_bf16 a[24:27], a[44:47], a[76:79], a[24:27]\n"
"v_mfma_f32_16x16x32_bf16 a[28:31], a[44:47], a[80:83], a[28:31]\n"
"ds_read_b128 a[80:83], v94 offset:1088\n"
"s_waitcnt lgkmcnt(10)\n"
"v_mfma_f32_16x16x32_bf16 a[16:19], a[48:51], a[64:67], a[16:19]\n"
"ds_read_b128 a[44:47], v92 offset:12288\n"
"v_mfma_f32_16x16x32_bf16 a[20:23], a[48:51], a[68:71], a[20:23]\n"
"v_mfma_f32_16x16x32_bf16 a[24:27], a[48:51], a[72:75], a[24:27]\n"
"v_mfma_f32_16x16x32_bf16 a[28:31], a[48:51], a[76:79], a[28:31]\n"
"ds_read_b128 a[76:79], v94 offset:832\n"
"s_waitcnt lgkmcnt(11)\n"
"v_mfma_f32_16x16x32_bf16 a[20:23], a[52:55], a[64:67], a[20:23]\n"
"ds_read_b128 a[48:51], v92 offset:16384\n"
"s_add_u32 m0, s73, 0\n"
"s_add_u32 s27, s5, 0\n"
"buffer_load_dwordx4 v114, s[12:15], s27 offen lds\n"
"buffer_load_dwordx4 v113, s[12:15], s27 offen offset:1024 lds\n"
"buffer_load_dwordx4 v112, s[12:15], s27 offen offset:2048 lds\n"
"v_mfma_f32_16x16x32_bf16 a[24:27], a[52:55], a[68:71], a[24:27]\n"
"v_mfma_f32_16x16x32_bf16 a[28:31], a[52:55], a[72:75], a[28:31]\n"
"ds_read_b128 a[72:75], v94 offset:576\n"
"s_waitcnt lgkmcnt(12)\n"
"v_mfma_f32_16x16x32_bf16 a[24:27], a[56:59], a[64:67], a[24:27]\n"
"ds_read_b128 a[52:55], v92 offset:20480\n"
"s_add_u32 m0, s73, 0xc00\n"
"s_add_u32 s27, s5, 0x15800\n"
"buffer_load_dwordx4 v114, s[12:15], s27 offen lds\n"
"buffer_load_dwordx4 v113, s[12:15], s27 offen offset:1024 lds\n"
"buffer_load_dwordx4 v112, s[12:15], s27 offen offset:2048 lds\n"
"v_mfma_f32_16x16x32_bf16 a[28:31], a[56:59], a[68:71], a[28:31]\n"
"ds_read_b128 a[68:71], v94 offset:320\n"
"s_waitcnt lgkmcnt(12)\n"
"v_mfma_f32_16x16x32_bf16 a[28:31], a[60:63], a[64:67], a[28:31]\n"
"ds_read_b128 a[64:67], v94 offset:64\n"
"ds_read_b128 a[56:59], v92 offset:24576\n"
"s_waitcnt lgkmcnt(13)\n"
"v_mfma_f32_16x16x32_bf16 a[28:31], a[32:35], a[92:95], a[28:31]\n"
"ds_read_b128 a[60:63], v92 offset:28672\n"
"ds_read_b128 a[92:95], v94 offset:1824\n"
"s_waitcnt lgkmcnt(14)\n"
"v_mfma_f32_16x16x32_bf16 a[24:27], a[32:35], a[88:91], a[24:27]\n"
"s_add_u32 m0, s73, 0x1800\n"
"s_add_u32 s27, s24, 0\n"
"buffer_load_dwordx4 v114, s[12:15], s27 offen lds\n"
"buffer_load_dwordx4 v113, s[12:15], s27 offen offset:1024 lds\n"
"buffer_load_dwordx4 v112, s[12:15], s27 offen offset:2048 lds\n"
"s_waitcnt lgkmcnt(12)\n"
"v_mfma_f32_16x16x32_bf16 a[20:23], a[32:35], a[84:87], a[20:23]\n"
"s_waitcnt lgkmcnt(10)\n"
"v_mfma_f32_16x16x32_bf16 a[16:19], a[32:35], a[80:83], a[16:19]\n"
"s_waitcnt lgkmcnt(8)\n"
"v_mfma_f32_16x16x32_bf16 a[12:15], a[32:35], a[76:79], a[12:15]\n"
"s_waitcnt lgkmcnt(6)\n"
"v_mfma_f32_16x16x32_bf16 a[8:11], a[32:35], a[72:75], a[8:11]\n"
"s_add_u32 m0, s73, 0x2400\n"
"s_add_u32 s27, s25, 0\n"
"buffer_load_dwordx4 v114, s[12:15], s27 offen lds\n"
"buffer_load_dwordx4 v113, s[12:15], s27 offen offset:1024 lds\n"
"buffer_load_dwordx4 v112, s[12:15], s27 offen offset:2048 lds\n"
"s_waitcnt lgkmcnt(4)\n"
"v_mfma_f32_16x16x32_bf16 a[4:7], a[32:35], a[68:71], a[4:7]\n"
"s_waitcnt lgkmcnt(3)\n"
"v_mfma_f32_16x16x32_bf16 a[0:3], a[32:35], a[64:67], a[0:3]\n"
"v_mfma_f32_16x16x32_bf16 a[4:7], a[36:39], a[64:67], a[4:7]\n"
"ds_read_b128 a[32:35], v93 offset:1024\n"
"v_mfma_f32_16x16x32_bf16 a[8:11], a[36:39], a[68:71], a[8:11]\n"
"s_add_u32 m0, s73, 0x3000\n"
"s_add_u32 s27, s26, 0\n"
"buffer_load_dwordx4 v114, s[12:15], s27 offen lds\n"
"buffer_load_dwordx4 v113, s[12:15], s27 offen offset:1024 lds\n"
"buffer_load_dwordx4 v112, s[12:15], s27 offen offset:2048 lds\n"
"v_mfma_f32_16x16x32_bf16 a[12:15], a[36:39], a[72:75], a[12:15]\n"
"v_mfma_f32_16x16x32_bf16 a[16:19], a[36:39], a[76:79], a[16:19]\n"
"v_mfma_f32_16x16x32_bf16 a[20:23], a[36:39], a[80:83], a[20:23]\n"
"v_mfma_f32_16x16x32_bf16 a[24:27], a[36:39], a[84:87], a[24:27]\n"
"s_waitcnt vmcnt(12)\n"
"ds_read_b64_tr_b16 a[176:177], v111\n"
"ds_read_b64_tr_b16 a[178:179], v111 offset:768\n"
"v_mfma_f32_16x16x32_bf16 a[28:31], a[36:39], a[88:91], a[28:31]\n"
"ds_read_b128 a[88:91], v94 offset:1568\n"
"v_mfma_f32_16x16x32_bf16 a[8:11], a[40:43], a[64:67], a[8:11]\n"
"ds_read_b128 a[36:39], v93 offset:5120\n"
"v_mfma_f32_16x16x32_bf16 a[12:15], a[40:43], a[68:71], a[12:15]\n"
"ds_read_b64_tr_b16 a[180:181], v111 offset:32\n"
"ds_read_b64_tr_b16 a[182:183], v111 offset:800\n"
"v_mfma_f32_16x16x32_bf16 a[16:19], a[40:43], a[72:75], a[16:19]\n"
"v_mfma_f32_16x16x32_bf16 a[20:23], a[40:43], a[76:79], a[20:23]\n"
"v_mfma_f32_16x16x32_bf16 a[24:27], a[40:43], a[80:83], a[24:27]\n"
"v_mfma_f32_16x16x32_bf16 a[28:31], a[40:43], a[84:87], a[28:31]\n"
"ds_read_b128 a[84:87], v94 offset:1312\n"
"ds_read_b64_tr_b16 a[184:185], v111 offset:64\n"
"ds_read_b64_tr_b16 a[186:187], v111 offset:832\n"
"v_mfma_f32_16x16x32_bf16 a[12:15], a[44:47], a[64:67], a[12:15]\n"
"ds_read_b128 a[40:43], v93 offset:9216\n"
"v_mfma_f32_16x16x32_bf16 a[16:19], a[44:47], a[68:71], a[16:19]\n"
"v_mfma_f32_16x16x32_bf16 a[20:23], a[44:47], a[72:75], a[20:23]\n"
"v_mfma_f32_16x16x32_bf16 a[24:27], a[44:47], a[76:79], a[24:27]\n"
"s_add_u32 m0, s73, 0x3c00\n"
"s_add_u32 s27, s5, 0x2b000\n"
"buffer_load_dwordx4 v114, s[12:15], s27 offen lds\n"
"buffer_load_dwordx4 v113, s[12:15], s27 offen offset:1024 lds\n"
"buffer_load_dwordx4 v112, s[12:15], s27 offen offset:2048 lds\n"
"v_mfma_f32_16x16x32_bf16 a[28:31], a[44:47], a[80:83], a[28:31]\n"
"ds_read_b128 a[80:83], v94 offset:1056\n"
"v_mfma_f32_16x16x32_bf16 a[16:19], a[48:51], a[64:67], a[16:19]\n"
"ds_read_b128 a[44:47], v93 offset:13312\n"
"v_mfma_f32_16x16x32_bf16 a[20:23], a[48:51], a[68:71], a[20:23]\n"
"v_mfma_f32_16x16x32_bf16 a[24:27], a[48:51], a[72:75], a[24:27]\n"
"s_waitcnt vmcnt(12)\n"
"ds_read_b64_tr_b16 a[188:189], v111 offset:3072\n"
"ds_read_b64_tr_b16 a[190:191], v111 offset:3840\n"
"v_mfma_f32_16x16x32_bf16 a[28:31], a[48:51], a[76:79], a[28:31]\n"
"ds_read_b128 a[76:79], v94 offset:800\n"
"v_mfma_f32_16x16x32_bf16 a[20:23], a[52:55], a[64:67], a[20:23]\n"
"ds_read_b128 a[48:51], v93 offset:17408\n"
"v_mfma_f32_16x16x32_bf16 a[24:27], a[52:55], a[68:71], a[24:27]\n"
"v_mfma_f32_16x16x32_bf16 a[28:31], a[52:55], a[72:75], a[28:31]\n"
"ds_read_b128 a[72:75], v94 offset:544\n"
"ds_read_b64_tr_b16 a[192:193], v111 offset:3104\n"
"ds_read_b64_tr_b16 a[194:195], v111 offset:3872\n"
"s_waitcnt lgkmcnt(14)\n"
"v_mfma_f32_16x16x32_bf16 a[24:27], a[56:59], a[64:67], a[24:27]\n"
"ds_read_b128 a[52:55], v93 offset:21504\n"
"v_mfma_f32_16x16x32_bf16 a[28:31], a[56:59], a[68:71], a[28:31]\n"
"ds_read_b128 a[68:71], v94 offset:288\n"
"v_mfma_f32_16x16x32_bf16 a[28:31], a[60:63], a[64:67], a[28:31]\n"
"ds_read_b128 a[64:67], v94 offset:32\n"
"ds_read_b128 a[56:59], v93 offset:25600\n"
"v_mfma_f32_16x16x32_bf16 a[28:31], a[32:35], a[92:95], a[28:31]\n"
"ds_read_b128 a[60:63], v93 offset:29696\n"
"ds_read_b128 a[92:95], v94 offset:1792\n"
"ds_read_b64_tr_b16 a[196:197], v111 offset:3136\n"
"ds_read_b64_tr_b16 a[198:199], v111 offset:3904\n"
"v_mfma_f32_16x16x32_bf16 a[24:27], a[32:35], a[88:91], a[24:27]\n"
"s_waitcnt lgkmcnt(14)\n"
"v_mfma_f32_16x16x32_bf16 a[20:23], a[32:35], a[84:87], a[20:23]\n"
"v_mfma_f32_16x16x32_bf16 a[16:19], a[32:35], a[80:83], a[16:19]\n"
"s_add_u32 m0, s73, 0\n"
"s_add_u32 s27, s24, 0x15800\n"
"buffer_load_dwordx4 v114, s[12:15], s27 offen lds\n"
"buffer_load_dwordx4 v113, s[12:15], s27 offen offset:1024 lds\n"
"buffer_load_dwordx4 v112, s[12:15], s27 offen offset:2048 lds\n"
"s_waitcnt lgkmcnt(12)\n"
"v_mfma_f32_16x16x32_bf16 a[12:15], a[32:35], a[76:79], a[12:15]\n"
"s_waitcnt lgkmcnt(10)\n"
"v_mfma_f32_16x16x32_bf16 a[8:11], a[32:35], a[72:75], a[8:11]\n"
"s_waitcnt lgkmcnt(6)\n"
"v_mfma_f32_16x16x32_bf16 a[4:7], a[32:35], a[68:71], a[4:7]\n"
"s_waitcnt lgkmcnt(5)\n"
"v_mfma_f32_16x16x32_bf16 a[0:3], a[32:35], a[64:67], a[0:3]\n"
"s_waitcnt vmcnt(12)\n"
"ds_read_b64_tr_b16 a[224:225], v111 offset:6144\n"
"ds_read_b64_tr_b16 a[226:227], v111 offset:6912\n"
"v_mfma_f32_16x16x32_bf16 a[4:7], a[36:39], a[64:67], a[4:7]\n"
"ds_read_b128 a[32:35], v92 offset:1024\n"
"v_mfma_f32_16x16x32_bf16 a[8:11], a[36:39], a[68:71], a[8:11]\n"
"v_mfma_f32_16x16x32_bf16 a[12:15], a[36:39], a[72:75], a[12:15]\n"
"v_mfma_f32_16x16x32_bf16 a[16:19], a[36:39], a[76:79], a[16:19]\n"
"ds_read_b64_tr_b16 a[228:229], v111 offset:6176\n"
"ds_read_b64_tr_b16 a[230:231], v111 offset:6944\n"
"v_mfma_f32_16x16x32_bf16 a[20:23], a[36:39], a[80:83], a[20:23]\n"
"v_mfma_f32_16x16x32_bf16 a[24:27], a[36:39], a[84:87], a[24:27]\n"
"v_mfma_f32_16x16x32_bf16 a[28:31], a[36:39], a[88:91], a[28:31]\n"
"ds_read_b128 a[88:91], v94 offset:1536\n"
"v_mfma_f32_16x16x32_bf16 a[8:11], a[40:43], a[64:67], a[8:11]\n"
"ds_read_b128 a[36:39], v92 offset:5120\n"
"ds_read_b64_tr_b16 a[232:233], v111 offset:6208\n"
"ds_read_b64_tr_b16 a[234:235], v111 offset:6976\n"
"v_mfma_f32_16x16x32_bf16 a[12:15], a[40:43], a[68:71], a[12:15]\n"
"v_mfma_f32_16x16x32_bf16 a[16:19], a[40:43], a[72:75], a[16:19]\n"
"v_mfma_f32_16x16x32_bf16 a[20:23], a[40:43], a[76:79], a[20:23]\n"
"v_mfma_f32_16x16x32_bf16 a[24:27], a[40:43], a[80:83], a[24:27]\n"
"s_add_u32 m0, s73, 0xc00\n"
"s_add_u32 s27, s25, 0x15800\n"
"s_waitcnt lgkmcnt(9)\n"
"buffer_load_dwordx4 v114, s[12:15], s27 offen lds\n"
"buffer_load_dwordx4 v113, s[12:15], s27 offen offset:1024 lds\n"
"buffer_load_dwordx4 v112, s[12:15], s27 offen offset:2048 lds\n"
"v_mfma_f32_16x16x32_bf16 a[28:31], a[40:43], a[84:87], a[28:31]\n"
"ds_read_b128 a[84:87], v94 offset:1280\n"
"v_mfma_f32_16x16x32_bf16 a[12:15], a[44:47], a[64:67], a[12:15]\n"
"ds_read_b128 a[40:43], v92 offset:9216\n"
"v_mfma_f32_16x16x32_bf16 a[16:19], a[44:47], a[68:71], a[16:19]\n"
"v_mfma_f32_16x16x32_bf16 a[20:23], a[44:47], a[72:75], a[20:23]\n"
"s_waitcnt vmcnt(12)\n"
"ds_read_b64_tr_b16 v[160:161], v111 offset:9216\n"
"ds_read_b64_tr_b16 v[162:163], v111 offset:9984\n"
"v_mfma_f32_16x16x32_bf16 a[24:27], a[44:47], a[76:79], a[24:27]\n"
"v_mfma_f32_16x16x32_bf16 a[28:31], a[44:47], a[80:83], a[28:31]\n"
"ds_read_b128 a[80:83], v94 offset:1024\n"
"v_mfma_f32_16x16x32_bf16 a[16:19], a[48:51], a[64:67], a[16:19]\n"
"ds_read_b128 a[44:47], v92 offset:13312\n"
"ds_read_b64_tr_b16 v[164:165], v111 offset:9248\n"
"ds_read_b64_tr_b16 v[166:167], v111 offset:10016\n"
"v_mfma_f32_16x16x32_bf16 a[20:23], a[48:51], a[68:71], a[20:23]\n"
"v_mfma_f32_16x16x32_bf16 a[24:27], a[48:51], a[72:75], a[24:27]\n"
"v_mfma_f32_16x16x32_bf16 a[28:31], a[48:51], a[76:79], a[28:31]\n"
"ds_read_b128 a[76:79], v94 offset:768\n"
"v_mfma_f32_16x16x32_bf16 a[20:23], a[52:55], a[64:67], a[20:23]\n"
"ds_read_b128 a[48:51], v92 offset:17408\n"
"ds_read_b64_tr_b16 v[168:169], v111 offset:9280\n"
"ds_read_b64_tr_b16 v[170:171], v111 offset:10048\n"
"v_mfma_f32_16x16x32_bf16 a[24:27], a[52:55], a[68:71], a[24:27]\n"
"v_mfma_f32_16x16x32_bf16 a[28:31], a[52:55], a[72:75], a[28:31]\n"
"ds_read_b128 a[72:75], v94 offset:512\n"
"v_mfma_f32_16x16x32_bf16 a[24:27], a[56:59], a[64:67], a[24:27]\n"
"ds_read_b128 a[52:55], v92 offset:21504\n"
"v_mfma_f32_16x16x32_bf16 a[28:31], a[56:59], a[68:71], a[28:31]\n"
"ds_read_b128 a[68:71], v94 offset:256\n"
"s_add_u32 m0, s73, 0x1800\n"
"s_add_u32 s27, s26, 0x15800\n"
"s_waitcnt lgkmcnt(14)\n"
"buffer_load_dwordx4 v114, s[12:15], s27 offen lds\n"
"buffer_load_dwordx4 v113, s[12:15], s27 offen offset:1024 lds\n"
"buffer_load_dwordx4 v112, s[12:15], s27 offen offset:2048 lds\n"
"v_mfma_f32_16x16x32_bf16 a[28:31], a[60:63], a[64:67], a[28:31]\n"
"ds_read_b128 a[64:67], v94\n"
"ds_read_b128 a[56:59], v92 offset:25600\n"
"v_mfma_f32_16x16x32_bf16 a[28:31], a[32:35], a[92:95], a[28:31]\n"
"ds_read_b128 a[60:63], v92 offset:29696\n"
"v_mfma_f32_16x16x32_bf16 a[28:31], a[36:39], a[88:91], a[28:31]\n"
"s_waitcnt lgkmcnt(14)\n"
"v_mfma_f32_16x16x32_bf16 a[28:31], a[40:43], a[84:87], a[28:31]\n"
"s_waitcnt vmcnt(12)\n"
"ds_read_b64_tr_b16 v[208:209], v111 offset:12288\n"
"ds_read_b64_tr_b16 v[210:211], v111 offset:13056\n"
"s_waitcnt lgkmcnt(14)\n"
"v_mfma_f32_16x16x32_bf16 a[28:31], a[44:47], a[80:83], a[28:31]\n"
"v_mfma_f32_16x16x32_bf16 a[24:27], a[32:35], a[88:91], a[24:27]\n"
"v_mfma_f32_16x16x32_bf16 a[24:27], a[36:39], a[84:87], a[24:27]\n"
"v_mfma_f32_16x16x32_bf16 a[24:27], a[40:43], a[80:83], a[24:27]\n"
"ds_read_b64_tr_b16 v[212:213], v111 offset:12320\n"
"ds_read_b64_tr_b16 v[214:215], v111 offset:13088\n"
"s_waitcnt lgkmcnt(6)\n"
"v_mfma_f32_16x16x32_bf16 a[0:3], a[32:35], a[64:67], a[0:3]\n"
"v_mfma_f32_16x16x32_bf16 a[4:7], a[32:35], a[68:71], a[4:7]\n"
"s_waitcnt lgkmcnt(0)\n"
"s_barrier\n"
"v_mfma_f32_16x16x32_bf16 a[4:7], a[36:39], a[64:67], a[4:7]\n"
"v_mfma_f32_16x16x32_bf16 a[8:11], a[32:35], a[72:75], a[8:11]\n"
"ds_read_b64_tr_b16 v[216:217], v111 offset:12352\n"
"ds_read_b64_tr_b16 v[218:219], v111 offset:13120\n"
"v_mfma_f32_16x16x32_bf16 a[8:11], a[36:39], a[68:71], a[8:11]\n"
"ds_write_b128 v91, a[0:3]\n"
"v_mfma_f32_16x16x32_bf16 a[8:11], a[40:43], a[64:67], a[8:11]\n"
"v_mfma_f32_16x16x32_bf16 a[12:15], a[32:35], a[76:79], a[12:15]\n"
"s_add_u32 m0, s73, 0x2400\n"
"s_add_u32 s27, s5, 0x40800\n"
"buffer_load_dwordx4 v114, s[12:15], s27 offen lds\n"
"buffer_load_dwordx4 v113, s[12:15], s27 offen offset:1024 lds\n"
"buffer_load_dwordx4 v112, s[12:15], s27 offen offset:2048 lds\n"
"v_mfma_f32_16x16x32_bf16 a[12:15], a[36:39], a[72:75], a[12:15]\n"
"ds_write_b128 v91, a[4:7] offset:1024\n"
"v_mfma_f32_16x16x32_bf16 a[12:15], a[40:43], a[68:71], a[12:15]\n"
"v_mfma_f32_16x16x32_bf16 a[12:15], a[44:47], a[64:67], a[12:15]\n"
"v_mfma_f32_16x16x32_bf16 a[16:19], a[32:35], a[80:83], a[16:19]\n"
"ds_write_b128 v91, a[8:11] offset:2048\n"
"s_waitcnt vmcnt(12)\n"
"ds_read_b64_tr_b16 a[200:201], v111 offset:15360\n"
"ds_read_b64_tr_b16 a[202:203], v111 offset:16128\n"
"v_mfma_f32_16x16x32_bf16 a[16:19], a[36:39], a[76:79], a[16:19]\n"
"v_mfma_f32_16x16x32_bf16 a[16:19], a[40:43], a[72:75], a[16:19]\n"
"v_mfma_f32_16x16x32_bf16 a[16:19], a[44:47], a[68:71], a[16:19]\n"
"ds_write_b128 v91, a[12:15] offset:3072\n"
"v_mfma_f32_16x16x32_bf16 a[16:19], a[48:51], a[64:67], a[16:19]\n"
"ds_read_b64_tr_b16 a[204:205], v111 offset:15392\n"
"ds_read_b64_tr_b16 a[206:207], v111 offset:16160\n"
"v_mfma_f32_16x16x32_bf16 a[20:23], a[32:35], a[84:87], a[20:23]\n"
"v_mfma_f32_16x16x32_bf16 a[20:23], a[36:39], a[80:83], a[20:23]\n"
"v_mfma_f32_16x16x32_bf16 a[20:23], a[40:43], a[76:79], a[20:23]\n"
"v_mfma_f32_16x16x32_bf16 a[20:23], a[44:47], a[72:75], a[20:23]\n"
"s_nop 1\n"
"ds_write_b128 v91, a[16:19] offset:4096\n"
"ds_read_b64_tr_b16 a[208:209], v111 offset:15424\n"
"ds_read_b64_tr_b16 a[210:211], v111 offset:16192\n"
"v_mfma_f32_16x16x32_bf16 a[20:23], a[48:51], a[68:71], a[20:23]\n"
"v_mfma_f32_16x16x32_bf16 a[20:23], a[52:55], a[64:67], a[20:23]\n"
"v_mfma_f32_16x16x32_bf16 a[24:27], a[44:47], a[76:79], a[24:27]\n"
"v_mfma_f32_16x16x32_bf16 a[24:27], a[48:51], a[72:75], a[24:27]\n"
"s_add_u32 m0, s73, 0x3000\n"
"s_add_u32 s27, s24, 0x2b000\n"
"s_waitcnt lgkmcnt(11)\n"
"buffer_load_dwordx4 v114, s[12:15], s27 offen lds\n"
"buffer_load_dwordx4 v113, s[12:15], s27 offen offset:1024 lds\n"
"buffer_load_dwordx4 v112, s[12:15], s27 offen offset:2048 lds\n"
"v_mfma_f32_16x16x32_bf16 a[24:27], a[52:55], a[68:71], a[24:27]\n"
"v_mfma_f32_16x16x32_bf16 a[24:27], a[56:59], a[64:67], a[24:27]\n"
"ds_write_b128 v91, a[20:23] offset:5120\n"
"v_mfma_f32_16x16x32_bf16 a[28:31], a[48:51], a[76:79], a[28:31]\n"
"v_mfma_f32_16x16x32_bf16 a[28:31], a[52:55], a[72:75], a[28:31]\n"
"v_mfma_f32_16x16x32_bf16 a[28:31], a[56:59], a[68:71], a[28:31]\n"
"v_mfma_f32_16x16x32_bf16 a[28:31], a[60:63], a[64:67], a[28:31]\n"
"ds_read_b128 v[68:71], v87\n"
"ds_read_b128 v[72:75], v87 offset:512\n"
"s_nop 0\n"
"ds_write_b128 v91, a[24:27] offset:6144\n"
"s_nop 3\n"
"ds_write_b128 v91, a[28:31] offset:7168\n"
"s_lshl_b32 s59, s68, 16\n"
"s_and_b32 s60, s68, s61\n"
"s_cmp_lt_u32 s7, 2\n"
"s_cselect_b32 s68, s59, s60\n"
"s_mov_b32 s60, 0xfffefffe\n"
"s_waitcnt lgkmcnt(0)\n"
"s_barrier\n"
"s_add_u32 s53, s32, 0x1c0000\n"
"buffer_store_dwordx2 v[100:101], v86, s[8:11], s53 offen sc1\n"
"ds_read_b128 v[0:3], v89\n"
"ds_read_b128 v[20:23], v89 offset:8192\n"
"ds_read_b128 v[24:27], v88\n"
"ds_read_b128 v[36:39], v88 offset:8192\n"
"ds_read_b128 v[40:43], v89 offset:2048\n"
"ds_read_b128 v[44:47], v89 offset:10240\n"
"ds_read_b128 v[60:63], v88 offset:2048\n"
"ds_read_b128 v[64:67], v88 offset:10240\n"
"v_and_b32 v76, s61, v71\n"
"v_and_b32 v78, s61, v70\n"
"v_and_b32 v116, s61, v69\n"
"v_and_b32 v118, s61, v68\n"
"v_lshlrev_b32 v77, 16, v71\n"
"v_lshlrev_b32 v79, 16, v70\n"
"v_lshlrev_b32 v117, 16, v69\n"
"v_lshlrev_b32 v119, 16, v68\n"
"s_waitcnt lgkmcnt(6)\n"
"v_pk_add_f32 v[0:1], v[0:1], v[20:21]\n"
"v_pk_add_f32 v[2:3], v[2:3], v[22:23]\n"
"v_mul_f32 v20, s68, v76\n"
"v_mul_f32 v21, s68, v77\n"
"v_mul_f32 v22, s68, v78\n"
"v_mul_f32 v23, s68, v79\n"
"v_cvt_pk_bf16_f32 v0, v0, v20\n"
"v_cvt_pk_bf16_f32 v1, v1, v21\n"
"v_cvt_pk_bf16_f32 v2, v2, v22\n"
"v_cvt_pk_bf16_f32 v3, v3, v23\n"
"v_dot2_f32_bf16 v0, v0, s94, 0\n"
"v_dot2_f32_bf16 v1, v1, s94, 0\n"
"v_dot2_f32_bf16 v2, v2, s94, 0\n"
"v_dot2_f32_bf16 v3, v3, s94, 0\n"
"s_nop 0\n"
"v_cvt_pk_bf16_f32 v68, v0, v1\n"
"s_nop 0\n"
"v_cvt_pk_bf16_f32 v69, v2, v3\n"
"v_pk_min_u16 v68, v68, s60\n"
"v_pk_min_u16 v69, v69, s60\n"
"s_waitcnt lgkmcnt(4)\n"
"v_pk_add_f32 v[24:25], v[24:25], v[36:37]\n"
"v_pk_add_f32 v[26:27], v[26:27], v[38:39]\n"
"v_mul_f32 v36, s68, v116\n"
"v_mul_f32 v37, s68, v117\n"
"v_mul_f32 v38, s68, v118\n"
"v_mul_f32 v39, s68, v119\n"
"v_cvt_pk_bf16_f32 v24, v24, v36\n"
"v_cvt_pk_bf16_f32 v25, v25, v37\n"
"v_cvt_pk_bf16_f32 v26, v26, v38\n"
"v_cvt_pk_bf16_f32 v27, v27, v39\n"
"v_dot2_f32_bf16 v24, v24, s94, 0\n"
"v_dot2_f32_bf16 v25, v25, s94, 0\n"
"v_dot2_f32_bf16 v26, v26, s94, 0\n"
"v_dot2_f32_bf16 v27, v27, s94, 0\n"
"s_nop 0\n"
"v_cvt_pk_bf16_f32 v70, v24, v25\n"
"s_nop 0\n"
"v_cvt_pk_bf16_f32 v71, v26, v27\n"
"v_pk_min_u16 v70, v70, s60\n"
"v_pk_min_u16 v71, v71, s60\n"
"buffer_store_dwordx4 v[68:71], v142, s[8:11], s28 offen sc1\n"
"v_and_b32 v76, s61, v75\n"
"v_and_b32 v78, s61, v74\n"
"v_and_b32 v116, s61, v73\n"
"v_and_b32 v118, s61, v72\n"
"v_lshlrev_b32 v77, 16, v75\n"
"v_lshlrev_b32 v79, 16, v74\n"
"v_lshlrev_b32 v117, 16, v73\n"
"v_lshlrev_b32 v119, 16, v72\n"
"s_waitcnt lgkmcnt(2)\n"
"v_pk_add_f32 v[40:41], v[40:41], v[44:45]\n"
"v_pk_add_f32 v[42:43], v[42:43], v[46:47]\n"
"v_mul_f32 v44, s68, v76\n"
"v_mul_f32 v45, s68, v77\n"
"v_mul_f32 v46, s68, v78\n"
"v_mul_f32 v47, s68, v79\n"
"v_cvt_pk_bf16_f32 v40, v40, v44\n"
"v_cvt_pk_bf16_f32 v41, v41, v45\n"
"v_cvt_pk_bf16_f32 v42, v42, v46\n"
"v_cvt_pk_bf16_f32 v43, v43, v47\n"
"v_dot2_f32_bf16 v40, v40, s94, 0\n"
"v_dot2_f32_bf16 v41, v41, s94, 0\n"
"v_dot2_f32_bf16 v42, v42, s94, 0\n"
"v_dot2_f32_bf16 v43, v43, s94, 0\n"
"s_nop 0\n"
"v_cvt_pk_bf16_f32 v72, v40, v41\n"
"s_nop 0\n"
"v_cvt_pk_bf16_f32 v73, v42, v43\n"
"v_pk_min_u16 v72, v72, s60\n"
"v_pk_min_u16 v73, v73, s60\n"
"s_waitcnt lgkmcnt(0)\n"
"v_pk_add_f32 v[60:61], v[60:61], v[64:65]\n"
"v_pk_add_f32 v[62:63], v[62:63], v[66:67]\n"
"v_mul_f32 v64, s68, v116\n"
"v_mul_f32 v65, s68, v117\n"
"v_mul_f32 v66, s68, v118\n"
"v_mul_f32 v67, s68, v119\n"
"v_cvt_pk_bf16_f32 v60, v60, v64\n"
"v_cvt_pk_bf16_f32 v61, v61, v65\n"
"v_cvt_pk_bf16_f32 v62, v62, v66\n"
"v_cvt_pk_bf16_f32 v63, v63, v67\n"
"v_dot2_f32_bf16 v60, v60, s94, 0\n"
"v_dot2_f32_bf16 v61, v61, s94, 0\n"
"v_dot2_f32_bf16 v62, v62, s94, 0\n"
"v_dot2_f32_bf16 v63, v63, s94, 0\n"
"s_nop 0\n"
"v_cvt_pk_bf16_f32 v74, v60, v61\n"
"s_nop 0\n"
"v_cvt_pk_bf16_f32 v75, v62, v63\n"
"v_pk_min_u16 v74, v74, s60\n"
"v_pk_min_u16 v75, v75, s60\n"
"buffer_store_dwordx4 v[72:75], v142, s[8:11], s28 offen offset:1024 sc1\n"
"s_add_u32 s53, s39, 0\n"
"buffer_store_dwordx2 v[28:29], v137, s[8:11], s53 offen sc1\n"
"s_add_u32 s53, s39, 0x800\n"
"buffer_store_dwordx2 v[32:33], v137, s[8:11], s53 offen sc1\n"
"s_barrier\n"
"s_lshl_b32 s58, s49, 7\n"
"v_lshl_add_u32 v125, v139, 10, s58\n"
"v_and_b32 v141, 1, v110\n"
"v_lshlrev_b32 v141, 2, v141\n"
"v_xor_b32 v141, v141, v138\n"
"v_lshl_add_u32 v125, v141, 4, v125\n"
"v_mov_b32 v141, 0x80000000\n"
"s_cmp_lt_u32 s7, 3\n"
"s_cselect_b64 s[86:87], -1, 0\n"
"s_cselect_b32 s59, 11, 10\n"
"s_lshl_b32 s59, s59, 14\n"
"v_cndmask_b32 v124, v141, v125, s[86:87]\n"
"s_mul_i32 s82, s7, 0x58000\n"
"s_add_u32 s83, s82, s59\n"
"s_sub_u32 s84, s59, 0x8000\n"
"s_mul_i32 s53, s7, 0x5800\n"
"s_add_u32 s53, s53, s29\n"
"s_add_u32 s91, s53, 0x4000\n"
"s_add_u32 s58, s53, 0x5000\n"
"s_cmp_lt_u32 s7, 3\n"
"s_cselect_b32 s92, s58, s53\n"
"s_and_b32 s62, s49, 0\n"
"s_xor_b32 s62, s62, s50\n"
"s_xor_b32 s58, s62, 0\n"
"s_lshl_b32 s60, s58, 12\n"
"s_add_u32 s85, s53, s60\n"
"s_and_b32 s60, s58, 1\n"
"s_cmp_eq_u32 s60, 0\n"
"s_cselect_b32 s78, s82, s83\n"
"s_lshr_b32 s58, s58, 1\n"
"s_lshl_b32 s58, s58, 14\n"
"s_add_u32 s78, s78, s58\n"
"s_xor_b32 s58, s62, 1\n"
"s_lshl_b32 s60, s58, 12\n"
"s_add_u32 s88, s53, s60\n"
"s_and_b32 s60, s58, 1\n"
"s_cmp_eq_u32 s60, 0\n"
"s_cselect_b32 s79, s82, s83\n"
"s_lshr_b32 s58, s58, 1\n"
"s_lshl_b32 s58, s58, 14\n"
"s_add_u32 s79, s79, s58\n"
"s_xor_b32 s58, s62, 2\n"
"s_lshl_b32 s60, s58, 12\n"
"s_add_u32 s89, s53, s60\n"
"s_and_b32 s60, s58, 1\n"
"s_cmp_eq_u32 s60, 0\n"
"s_cselect_b32 s80, s82, s83\n"
"s_lshr_b32 s58, s58, 1\n"
"s_lshl_b32 s58, s58, 14\n"
"s_add_u32 s80, s80, s58\n"
"s_xor_b32 s58, s62, 3\n"
"s_lshl_b32 s60, s58, 12\n"
"s_add_u32 s90, s53, s60\n"
"s_and_b32 s60, s58, 1\n"
"s_cmp_eq_u32 s60, 0\n"
"s_cselect_b32 s81, s82, s83\n"
"s_lshr_b32 s58, s58, 1\n"
"s_lshl_b32 s58, s58, 14\n"
"s_add_u32 s81, s81, s58\n"
"v_lshrrev_b32 v141, 5, v143\n"
"v_lshl_add_u32 v141, v141, 2, v109\n"
"v_lshlrev_b32 v123, 7, v141\n"
"v_and_b32 v141, 1, v110\n"
"v_lshlrev_b32 v141, 1, v141\n"
"v_lshrrev_b32 v140, 1, v108\n"
"v_add_u32 v141, v141, v140\n"
"v_and_b32 v140, 2, v109\n"
"v_lshlrev_b32 v140, 1, v140\n"
"v_xor_b32 v141, v141, v140\n"
"v_lshl_add_u32 v123, v141, 4, v123\n"
"v_and_b32 v141, 1, v108\n"
"v_lshl_add_u32 v123, v141, 3, v123\n"
"v_add_u32 v123, s73, v123\n"
"v_xor_b32 v122, 64, v123\n"
"v_lshlrev_b32 v141, 4, v138\n"
"v_lshl_add_u32 v85, v139, 12, v141\n"
"s_lshl_b32 s53, s49, 18\n"
"s_lshl_b32 s58, s7, 16\n"
"s_add_u32 s53, s53, s58\n"
"s_lshl_b32 s58, s48, 7\n"
"s_add_u32 s53, s53, s58\n"
"s_add_u32 s29, s53, 0x400000\n"
"s_add_u32 s4, s29, 0x8000\n"
"s_lshl_b32 s53, s7, 11\n"
"s_add_u32 s53, s53, 0x8000\n"
"v_add_u32 v84, s53, v142\n"
"v_lshrrev_b32 v141, 1, v108\n"
"v_lshl_add_u32 v141, v141, 2, v109\n"
"v_lshl_add_u32 v141, v110, 4, v141\n"
"v_lshlrev_b32 v83, 7, v141\n"
"v_and_b32 v141, 1, v108\n"
"v_lshl_add_u32 v83, v141, 3, v83\n"
"s_lshl_b32 s53, s7, 4\n"
"s_add_u32 s53, s53, 0x8000\n"
"v_add_u32 v83, s53, v83\n"
"s_mul_i32 s53, s7, 0xc0\n"
"s_add_u32 s53, s53, 0x26500\n"
"v_lshl_add_u32 v10, v110, 4, s53\n"
"v_lshlrev_b32 v11, 3, v107\n"
"v_lshl_add_u32 v11, v110, 8, v11\n"
"s_lshl_b32 s93, s48, 12\n"
"s_add_u32 s93, s93, 0x100000\n"
"s_lshl_b32 s53, s48, 16\n"
"s_lshl_b32 s87, s7, 12\n"
"s_add_u32 s86, s53, s87\n"
"s_add_u32 s86, s86, 0x600000\n"
"v_lshrrev_b32 v141, 2, v139\n"
"v_lshlrev_b32 v135, 12, v141\n"
"v_and_b32 v141, 2, v139\n"
"v_lshl_add_u32 v135, v141, 10, v135\n"
"v_and_b32 v141, 1, v139\n"
"v_lshl_add_u32 v135, v141, 9, v135\n"
"v_lshl_add_u32 v135, v138, 4, v135\n"
"s_lshl_b32 s53, s7, 7\n"
"v_add_u32 v135, s53, v135\n"
"s_and_b32 s53, s7, 1\n"
"s_lshl_b32 s53, s53, 5\n"
"v_lshl_add_u32 v12, v138, 2, s53\n"
"s_add_u32 s53, s50, 0\n"
"s_and_b32 s53, s53, 3\n"
"s_lshl_b32 s53, s53, 12\n"
"v_add_u32 v4, s53, v142\n"
"s_add_u32 s53, s50, 1\n"
"s_and_b32 s53, s53, 3\n"
"s_lshl_b32 s53, s53, 12\n"
"v_add_u32 v5, s53, v142\n"
"s_add_u32 s53, s50, 2\n"
"s_and_b32 s53, s53, 3\n"
"s_lshl_b32 s53, s53, 12\n"
"v_add_u32 v6, s53, v142\n"
"s_add_u32 s53, s50, 3\n"
"s_and_b32 s53, s53, 3\n"
"s_lshl_b32 s53, s53, 12\n"
"v_add_u32 v7, s53, v142\n"
"v_lshlrev_b32 v136, 3, v138\n"
"v_lshl_add_u32 v136, v139, 7, v136\n"
"s_waitcnt vmcnt(17)\n"
"ds_read_b64_tr_b16 a[236:237], v111\n"
"ds_read_b64_tr_b16 a[238:239], v111 offset:768\n"
"ds_read_b64_tr_b16 a[240:241], v111 offset:32\n"
"ds_read_b64_tr_b16 a[242:243], v111 offset:800\n"
"ds_read_b64_tr_b16 a[244:245], v111 offset:64\n"
"ds_read_b64_tr_b16 a[246:247], v111 offset:832\n"
"s_waitcnt vmcnt(14)\n"
"ds_read_b64_tr_b16 v[172:173], v111 offset:3072\n"
"ds_read_b64_tr_b16 v[174:175], v111 offset:3840\n"
"ds_read_b64_tr_b16 v[176:177], v111 offset:3104\n"
"ds_read_b64_tr_b16 v[178:179], v111 offset:3872\n"
"ds_read_b64_tr_b16 v[180:181], v111 offset:3136\n"
"ds_read_b64_tr_b16 v[182:183], v111 offset:3904\n"
"s_waitcnt vmcnt(11)\n"
"ds_read_b64_tr_b16 v[220:221], v111 offset:6144\n"
"ds_read_b64_tr_b16 v[222:223], v111 offset:6912\n"
"ds_read_b64_tr_b16 v[224:225], v111 offset:6176\n"
"ds_read_b64_tr_b16 v[226:227], v111 offset:6944\n"
"ds_read_b64_tr_b16 v[228:229], v111 offset:6208\n"
"ds_read_b64_tr_b16 v[230:231], v111 offset:6976\n"
"s_waitcnt vmcnt(8)\n"
"ds_read_b64_tr_b16 a[212:213], v111 offset:9216\n"
"ds_read_b64_tr_b16 a[214:215], v111 offset:9984\n"
"ds_read_b64_tr_b16 a[216:217], v111 offset:9248\n"
"ds_read_b64_tr_b16 a[218:219], v111 offset:10016\n"
"ds_read_b64_tr_b16 a[220:221], v111 offset:9280\n"
"ds_read_b64_tr_b16 a[222:223], v111 offset:10048\n"
"v_mfma_f32_16x16x32_bf16 a[168:171], a[176:179], v[16:19], 0\n"
"v_mfma_f32_16x16x32_bf16 a[172:175], a[180:183], v[16:19], 0\n"
"v_mfma_f32_16x16x32_bf16 a[160:163], a[184:187], v[16:19], 0\n"
"v_mfma_f32_16x16x32_bf16 a[168:171], a[188:191], v[16:19], a[168:171]\n"
"v_mfma_f32_16x16x32_bf16 a[172:175], a[192:195], v[16:19], a[172:175]\n"
"v_mfma_f32_16x16x32_bf16 a[160:163], a[196:199], v[16:19], a[160:163]\n"
"v_mfma_f32_16x16x32_bf16 a[168:171], a[224:227], v[16:19], a[168:171]\n"
"v_mfma_f32_16x16x32_bf16 a[172:175], a[228:231], v[16:19], a[172:175]\n"
"v_mfma_f32_16x16x32_bf16 a[160:163], a[232:235], v[16:19], a[160:163]\n"
"v_mfma_f32_16x16x32_bf16 a[168:171], v[160:163], v[16:19], a[168:171]\n"
"v_mfma_f32_16x16x32_bf16 a[172:175], v[164:167], v[16:19], a[172:175]\n"
"v_mfma_f32_16x16x32_bf16 a[160:163], v[168:171], v[16:19], a[160:163]\n"
"v_mfma_f32_16x16x32_bf16 a[168:171], v[208:211], v[16:19], a[168:171]\n"
"v_mfma_f32_16x16x32_bf16 a[172:175], v[212:215], v[16:19], a[172:175]\n"
"v_mfma_f32_16x16x32_bf16 a[160:163], v[216:219], v[16:19], a[160:163]\n"
"v_mfma_f32_16x16x32_bf16 a[168:171], a[200:203], v[16:19], a[168:171]\n"
"v_mfma_f32_16x16x32_bf16 a[172:175], a[204:207], v[16:19], a[172:175]\n"
"v_mfma_f32_16x16x32_bf16 a[160:163], a[208:211], v[16:19], a[160:163]\n"
"s_waitcnt lgkmcnt(14)\n"
"v_mfma_f32_16x16x32_bf16 a[168:171], a[236:239], v[16:19], a[168:171]\n"
"v_mfma_f32_16x16x32_bf16 a[172:175], a[240:243], v[16:19], a[172:175]\n"
"v_mfma_f32_16x16x32_bf16 a[160:163], a[244:247], v[16:19], a[160:163]\n"
"v_mfma_f32_16x16x32_bf16 a[168:171], v[172:175], v[16:19], a[168:171]\n"
"v_mfma_f32_16x16x32_bf16 a[172:175], v[176:179], v[16:19], a[172:175]\n"
"s_waitcnt lgkmcnt(12)\n"
"v_mfma_f32_16x16x32_bf16 a[160:163], v[180:183], v[16:19], a[160:163]\n"
"s_waitcnt lgkmcnt(10)\n"
"v_mfma_f32_16x16x32_bf16 a[168:171], v[220:223], v[16:19], a[168:171]\n"
"s_waitcnt lgkmcnt(8)\n"
"v_mfma_f32_16x16x32_bf16 a[172:175], v[224:227], v[16:19], a[172:175]\n"
"s_waitcnt lgkmcnt(6)\n"
"v_mfma_f32_16x16x32_bf16 a[160:163], v[228:231], v[16:19], a[160:163]\n"
"s_waitcnt lgkmcnt(4)\n"
"v_mfma_f32_16x16x32_bf16 a[168:171], a[212:215], v[16:19], a[168:171]\n"
"s_waitcnt lgkmcnt(2)\n"
"v_mfma_f32_16x16x32_bf16 a[172:175], a[216:219], v[16:19], a[172:175]\n"
"s_waitcnt lgkmcnt(0)\n"
"v_mfma_f32_16x16x32_bf16 a[160:163], a[220:223], v[16:19], a[160:163]\n"
"s_sleep 3\n"
"buffer_load_dwordx2 v[48:49], v102, s[8:11], s75 offen sc1\n"
"s_add_u32 m0, s73, 0x3c00\n"
"s_add_u32 s27, s25, 0x2b000\n"
"buffer_load_dwordx4 v114, s[12:15], s27 offen lds\n"
"buffer_load_dwordx4 v113, s[12:15], s27 offen offset:1024 lds\n"
"buffer_load_dwordx4 v112, s[12:15], s27 offen offset:2048 lds\n"
"s_add_u32 m0, s73, 0\n"
"s_add_u32 s27, s26, 0x2b000\n"
"buffer_load_dwordx4 v114, s[12:15], s27 offen lds\n"
"buffer_load_dwordx4 v113, s[12:15], s27 offen offset:1024 lds\n"
"buffer_load_dwordx4 v112, s[12:15], s27 offen offset:2048 lds\n"
"s_add_u32 m0, s73, 0xc00\n"
"s_add_u32 s27, s24, 0x40800\n"
"buffer_load_dwordx4 v114, s[12:15], s27 offen lds\n"
"buffer_load_dwordx4 v113, s[12:15], s27 offen offset:1024 lds\n"
"buffer_load_dwordx4 v112, s[12:15], s27 offen offset:2048 lds\n"
"s_add_u32 m0, s73, 0x1800\n"
"s_add_u32 s27, s25, 0x40800\n"
"buffer_load_dwordx4 v114, s[12:15], s27 offen lds\n"
"buffer_load_dwordx4 v113, s[12:15], s27 offen offset:1024 lds\n"
"buffer_load_dwordx4 v112, s[12:15], s27 offen offset:2048 lds\n"
"s_waitcnt vmcnt(12)\n"
"v_cmp_ne_u64 vcc, s[54:55], v[48:49]\n"
"s_cbranch_vccnz Lslow3_f2\n"
"Lback3_f2:\n"
"buffer_load_dwordx4 v[20:23], v85, s[8:11], s29 offen sc1\n"
"buffer_load_dwordx4 v[24:27], v85, s[8:11], s4 offen sc1\n"
"s_add_u32 s59, s74, 0\n"
"buffer_load_dwordx2 v[130:131], v136, s[8:11], s59 offen sc1\n"
"s_add_u32 s59, s74, 0x800\n"
"buffer_load_dwordx2 v[128:129], v136, s[8:11], s59 offen sc1\n"
"ds_read_b64_tr_b16 a[248:249], v111 offset:12288\n"
"ds_read_b64_tr_b16 a[250:251], v111 offset:13056\n"
"ds_read_b64_tr_b16 a[252:253], v111 offset:12320\n"
"ds_read_b64_tr_b16 a[254:255], v111 offset:13088\n"
"ds_read_b64_tr_b16 v[144:145], v111 offset:12352\n"
"ds_read_b64_tr_b16 v[146:147], v111 offset:13120\n"
"s_waitcnt vmcnt(13)\n"
"ds_read_b64_tr_b16 v[184:185], v111 offset:15360\n"
"ds_read_b64_tr_b16 v[186:187], v111 offset:16128\n"
"ds_read_b64_tr_b16 v[188:189], v111 offset:15392\n"
"ds_read_b64_tr_b16 v[190:191], v111 offset:16160\n"
"ds_read_b64_tr_b16 v[192:193], v111 offset:15424\n"
"ds_read_b64_tr_b16 v[194:195], v111 offset:16192\n"
"s_add_u32 m0, s73, 0x2400\n"
"s_add_u32 s27, s26, 0x40800\n"
"buffer_load_dwordx4 v114, s[12:15], s27 offen lds\n"
"buffer_load_dwordx4 v113, s[12:15], s27 offen offset:1024 lds\n"
"buffer_load_dwordx4 v112, s[12:15], s27 offen offset:2048 lds\n"
"s_add_u32 m0, s73, 0x3000\n"
"s_add_u32 s27, s78, 0\n"
"s_waitcnt lgkmcnt(6)\n"
"buffer_load_dwordx4 v125, s[16:19], s27 offen lds\n"
"s_add_u32 s27, s27, 0x1c00\n"
"buffer_load_dwordx4 v125, s[16:19], s27 offen offset:1024 lds\n"
"s_waitcnt vmcnt(5)\n"
"v_max3_u32 v134, v130, v131, v128\n"
"v_max_u32 v134, v134, v129\n"
"v_cmp_eq_u32 vcc, -1, v134\n"
"s_cbranch_vccnz Lslow4_s0\n"
"Lback4_s0:\n"
"v_add_f32_dpp v0, v130, v130 row_ror:8 row_mask:0xf bank_mask:0xf\n"
"v_add_f32_dpp v1, v131, v131 row_ror:8 row_mask:0xf bank_mask:0xf\n"
"s_nop 1\n"
"v_permlane16_swap_b32 v0, v1\n"
"v_add_f32 v0, v0, v1\n"
"v_mov_b32 v1, v0\n"
"s_nop 1\n"
"v_permlane32_swap_b32 v0, v1\n"
"v_add_f32 v130, v0, v1\n"
"v_mov_b32 v131, v130\n"
"s_nop 1\n"
"v_permlane16_swap_b32 v130, v131\n"
"v_mul_f32 v130, 0xbb000000, v130\n"
"v_mul_f32 v0, v130, v130\n"
"v_fma_f32 v131, v131, s37, -v0\n"
"v_max_f32 v131, 0, v131\n"
"v_cvt_pk_bf16_f32 v0, v130, v131\n"
"v_and_b32 v131, s61, v0\n"
"v_lshlrev_b32 v130, 16, v0\n"
"v_add_f32 v131, 0x3727c5ac, v131\n"
"v_cvt_pk_bf16_f32 v131, v131, v131\n"
"v_and_b32 v131, s61, v131\n"
"v_sqrt_f32 v131, v131\n"
"s_nop 0\n"
"v_cvt_pk_bf16_f32 v131, v131, v131\n"
"v_and_b32 v131, s61, v131\n"
"v_rcp_f32 v131, v131\n"
"v_add_f32_dpp v0, v128, v128 row_ror:8 row_mask:0xf bank_mask:0xf\n"
"v_add_f32_dpp v1, v129, v129 row_ror:8 row_mask:0xf bank_mask:0xf\n"
"s_nop 1\n"
"v_permlane16_swap_b32 v0, v1\n"
"v_add_f32 v0, v0, v1\n"
"v_mov_b32 v1, v0\n"
"s_nop 1\n"
"v_permlane32_swap_b32 v0, v1\n"
"v_add_f32 v128, v0, v1\n"
"v_mov_b32 v129, v128\n"
"s_nop 1\n"
"v_permlane16_swap_b32 v128, v129\n"
"v_mul_f32 v128, 0xbb000000, v128\n"
"v_mul_f32 v0, v128, v128\n"
"v_fma_f32 v129, v129, s37, -v0\n"
"v_max_f32 v129, 0, v129\n"
"v_cvt_pk_bf16_f32 v0, v128, v129\n"
"v_and_b32 v129, s61, v0\n"
"v_lshlrev_b32 v128, 16, v0\n"
"v_add_f32 v129, 0x3727c5ac, v129\n"
"v_cvt_pk_bf16_f32 v129, v129, v129\n"
"v_and_b32 v129, s61, v129\n"
"v_sqrt_f32 v129, v129\n"
"s_nop 0\n"
"v_cvt_pk_bf16_f32 v129, v129, v129\n"
"v_and_b32 v129, s61, v129\n"
"v_rcp_f32 v129, v129\n"
"v_max3_u32 v134, v20, v21, v22\n"
"v_max3_u32 v134, v134, v23, v24\n"
"v_max3_u32 v134, v134, v25, v26\n"
"v_max_u32 v134, v134, v27\n"
"v_cmp_eq_u32 vcc, -1, v134\n"
"s_cbranch_vccnz Lslow5_g\n"
"Lback5_g:\n"
"ds_write_b128 v84, v[20:23]\n"
"ds_write_b128 v84, v[24:27] offset:1024\n"
"s_waitcnt lgkmcnt(0)\n"
"s_barrier\n"
"ds_read_b64_tr_b16 v[20:21], v83\n"
"ds_read_b64_tr_b16 v[22:23], v83 offset:1024\n"
"ds_read_b64_tr_b16 v[24:25], v83 offset:64\n"
"ds_read_b64_tr_b16 v[26:27], v83 offset:1088\n"
"v_lshlrev_b32 v28, 16, v52\n"
"ds_read_b64_tr_b16 v[232:233], v111\n"
"ds_read_b64_tr_b16 v[234:235], v111 offset:768\n"
"v_and_b32 v29, s61, v52\n"
"v_lshlrev_b32 v30, 16, v53\n"
"v_and_b32 v31, s61, v53\n"
"v_lshlrev_b32 v32, 16, v54\n"
"v_and_b32 v33, s61, v54\n"
"v_lshlrev_b32 v34, 16, v55\n"
"v_and_b32 v35, s61, v55\n"
"v_pk_add_f32 v[44:45], v[28:29], v[130:131] op_sel_hi:[1,0]\n"
"v_pk_add_f32 v[46:47], v[30:31], v[130:131] op_sel_hi:[1,0]\n"
"v_pk_add_f32 v[60:61], v[32:33], v[130:131] op_sel_hi:[1,0]\n"
"ds_read_b64_tr_b16 v[236:237], v111 offset:32\n"
"ds_read_b64_tr_b16 v[238:239], v111 offset:800\n"
"v_pk_add_f32 v[62:63], v[34:35], v[130:131] op_sel_hi:[1,0]\n"
"v_cvt_pk_bf16_f32 v52, v44, v45\n"
"v_cvt_pk_bf16_f32 v53, v46, v47\n"
"v_cvt_pk_bf16_f32 v54, v60, v61\n"
"v_cvt_pk_bf16_f32 v55, v62, v63\n"
"v_lshlrev_b32 v44, 16, v52\n"
"v_and_b32 v45, s61, v52\n"
"v_lshlrev_b32 v46, 16, v53\n"
"v_and_b32 v47, s61, v53\n"
"v_lshlrev_b32 v60, 16, v54\n"
"v_and_b32 v61, s61, v54\n"
"ds_read_b64_tr_b16 v[240:241], v111 offset:64\n"
"ds_read_b64_tr_b16 v[242:243], v111 offset:832\n"
"v_lshlrev_b32 v62, 16, v55\n"
"v_and_b32 v63, s61, v55\n"
"v_pk_mul_f32 v[44:45], v[44:45], v[130:131] op_sel:[0,1] op_sel_hi:[1,1]\n"
"v_pk_mul_f32 v[46:47], v[46:47], v[130:131] op_sel:[0,1] op_sel_hi:[1,1]\n"
"v_pk_mul_f32 v[60:61], v[60:61], v[130:131] op_sel:[0,1] op_sel_hi:[1,1]\n"
"v_pk_mul_f32 v[62:63], v[62:63], v[130:131] op_sel:[0,1] op_sel_hi:[1,1]\n"
"v_cvt_pk_bf16_f32 v52, v44, v45\n"
"v_cvt_pk_bf16_f32 v53, v46, v47\n"
"v_cvt_pk_bf16_f32 v54, v60, v61\n"
"v_cvt_pk_bf16_f32 v55, v62, v63\n"
"s_waitcnt lgkmcnt(9)\n"
"v_and_b32 v36, 0xffff, v20\n"
"s_add_u32 m0, s73, 0x3800\n"
"s_add_u32 s27, s78, 0xc000\n"
"buffer_load_dwordx4 v125, s[16:19], s27 offen lds\n"
"s_add_u32 s27, s27, 0x1c00\n"
"buffer_load_dwordx4 v125, s[16:19], s27 offen offset:1024 lds\n"
"v_and_b32 v37, s61, v20\n"
"v_and_b32 v38, 0xffff, v21\n"
"v_and_b32 v39, s61, v21\n"
"s_waitcnt lgkmcnt(8)\n"
"v_and_b32 v40, 0xffff, v22\n"
"v_and_b32 v41, s61, v22\n"
"v_and_b32 v42, 0xffff, v23\n"
"v_and_b32 v43, s61, v23\n"
"v_dot2_f32_bf16 v44, v52, v36, 0\n"
"v_dot2_f32_bf16 v45, v52, v37, 0\n"
"v_dot2_f32_bf16 v46, v53, v38, 0\n"
"ds_read_b64_tr_b16 v[148:149], v111 offset:3072\n"
"ds_read_b64_tr_b16 v[150:151], v111 offset:3840\n"
"v_dot2_f32_bf16 v47, v53, v39, 0\n"
"v_dot2_f32_bf16 v60, v54, v40, 0\n"
"v_dot2_f32_bf16 v61, v54, v41, 0\n"
"v_dot2_f32_bf16 v62, v55, v42, 0\n"
"v_dot2_f32_bf16 v63, v55, v43, 0\n"
"v_cvt_pk_bf16_f32 v44, v44, v28\n"
"v_cvt_pk_bf16_f32 v45, v45, v29\n"
"v_cvt_pk_bf16_f32 v46, v46, v30\n"
"v_cvt_pk_bf16_f32 v47, v47, v31\n"
"v_cvt_pk_bf16_f32 v60, v60, v32\n"
"v_cvt_pk_bf16_f32 v61, v61, v33\n"
"ds_read_b64_tr_b16 v[152:153], v111 offset:3104\n"
"ds_read_b64_tr_b16 v[154:155], v111 offset:3872\n"
"v_cvt_pk_bf16_f32 v62, v62, v34\n"
"v_cvt_pk_bf16_f32 v63, v63, v35\n"
"v_dot2_f32_bf16 v28, v44, s94, 0\n"
"v_dot2_f32_bf16 v29, v45, s94, 0\n"
"v_dot2_f32_bf16 v30, v46, s94, 0\n"
"v_dot2_f32_bf16 v31, v47, s94, 0\n"
"v_dot2_f32_bf16 v32, v60, s94, 0\n"
"v_dot2_f32_bf16 v33, v61, s94, 0\n"
"v_dot2_f32_bf16 v34, v62, s94, 0\n"
"v_dot2_f32_bf16 v35, v63, s94, 0\n"
"v_cvt_pk_bf16_f32 v52, v28, v29\n"
"ds_read_b64_tr_b16 v[156:157], v111 offset:3136\n"
"ds_read_b64_tr_b16 v[158:159], v111 offset:3904\n"
"v_cvt_pk_bf16_f32 v53, v30, v31\n"
"v_cvt_pk_bf16_f32 v54, v32, v33\n"
"v_cvt_pk_bf16_f32 v55, v34, v35\n"
"v_min_u32 v52, -2, v52\n"
"v_min_u32 v53, -2, v53\n"
"v_min_u32 v54, -2, v54\n"
"v_min_u32 v55, -2, v55\n"
"s_add_u32 s53, s30, 0\n"
"buffer_store_dwordx4 v[52:55], v15, s[8:11], s53 offen sc1\n"
"v_add_f32 v36, v28, v29\n"
"v_mul_f32 v37, v28, v28\n"
"v_add_f32 v38, v30, v31\n"
"s_add_u32 m0, s73, 0x4000\n"
"s_add_u32 s27, s79, 0\n"
"buffer_load_dwordx4 v125, s[16:19], s27 offen lds\n"
"s_add_u32 s27, s27, 0x1c00\n"
"buffer_load_dwordx4 v125, s[16:19], s27 offen offset:1024 lds\n"
"v_mul_f32 v39, v29, v29\n"
"v_add_f32 v36, v36, v32\n"
"v_fmac_f32 v37, v30, v30\n"
"v_add_f32 v38, v38, v33\n"
"v_fmac_f32 v39, v31, v31\n"
"v_add_f32 v36, v36, v34\n"
"v_fmac_f32 v37, v32, v32\n"
"v_add_f32 v38, v38, v35\n"
"v_fmac_f32 v39, v33, v33\n"
"v_add_f32 v36, v36, v38\n"
"ds_read_b64_tr_b16 v[196:197], v111 offset:6144\n"
"ds_read_b64_tr_b16 v[198:199], v111 offset:6912\n"
"v_fmac_f32 v37, v34, v34\n"
"v_fmac_f32 v39, v35, v35\n"
"v_add_f32 v37, v37, v39\n"
"v_add_f32_dpp v38, v36, v36 row_ror:8 row_mask:0xf bank_mask:0xf\n"
"s_nop 0\n"
"v_add_f32_dpp v39, v37, v37 row_ror:8 row_mask:0xf bank_mask:0xf\n"
"s_nop 1\n"
"v_permlane16_swap_b32 v38, v39\n"
"v_add_f32 v38, v38, v39\n"
"v_mov_b32 v39, v38\n"
"s_nop 1\n"
"v_permlane32_swap_b32 v38, v39\n"
"v_add_f32 v36, v38, v39\n"
"v_mov_b32 v37, v36\n"
"ds_read_b64_tr_b16 v[200:201], v111 offset:6176\n"
"ds_read_b64_tr_b16 v[202:203], v111 offset:6944\n"
"v_mov_b32 v38, v36\n"
"s_nop 1\n"
"v_permlane16_swap_b32 v38, v37\n"
"v_min_u32 v36, -2, v36\n"
"v_min_u32 v37, -2, v37\n"
"s_add_u32 s53, s39, 0x20000\n"
"buffer_store_dwordx2 v[36:37], v137, s[8:11], s53 offen sc1\n"
"v_lshlrev_b32 v28, 16, v56\n"
"v_and_b32 v29, s61, v56\n"
"v_lshlrev_b32 v30, 16, v57\n"
"v_and_b32 v31, s61, v57\n"
"v_lshlrev_b32 v32, 16, v58\n"
"v_and_b32 v33, s61, v58\n"
"ds_read_b64_tr_b16 v[204:205], v111 offset:6208\n"
"ds_read_b64_tr_b16 v[206:207], v111 offset:6976\n"
"v_lshlrev_b32 v34, 16, v59\n"
"v_and_b32 v35, s61, v59\n"
"v_pk_add_f32 v[44:45], v[28:29], v[128:129] op_sel_hi:[1,0]\n"
"v_pk_add_f32 v[46:47], v[30:31], v[128:129] op_sel_hi:[1,0]\n"
"v_pk_add_f32 v[60:61], v[32:33], v[128:129] op_sel_hi:[1,0]\n"
"v_pk_add_f32 v[62:63], v[34:35], v[128:129] op_sel_hi:[1,0]\n"
"v_cvt_pk_bf16_f32 v56, v44, v45\n"
"v_cvt_pk_bf16_f32 v57, v46, v47\n"
"v_cvt_pk_bf16_f32 v58, v60, v61\n"
"v_cvt_pk_bf16_f32 v59, v62, v63\n"
"v_lshlrev_b32 v44, 16, v56\n"
"s_add_u32 m0, s73, 0x4800\n"
"s_add_u32 s27, s80, 0\n"
"buffer_load_dwordx4 v125, s[16:19], s27 offen lds\n"
"s_add_u32 s27, s27, 0x1c00\n"
"buffer_load_dwordx4 v125, s[16:19], s27 offen offset:1024 lds\n"
"v_and_b32 v45, s61, v56\n"
"v_lshlrev_b32 v46, 16, v57\n"
"v_and_b32 v47, s61, v57\n"
"v_lshlrev_b32 v60, 16, v58\n"
"v_and_b32 v61, s61, v58\n"
"v_lshlrev_b32 v62, 16, v59\n"
"v_and_b32 v63, s61, v59\n"
"v_pk_mul_f32 v[44:45], v[44:45], v[128:129] op_sel:[0,1] op_sel_hi:[1,1]\n"
"v_pk_mul_f32 v[46:47], v[46:47], v[128:129] op_sel:[0,1] op_sel_hi:[1,1]\n"
"v_pk_mul_f32 v[60:61], v[60:61], v[128:129] op_sel:[0,1] op_sel_hi:[1,1]\n"
"s_waitcnt vmcnt(10)\n"
"ds_read_b64_tr_b16 v[244:245], v111 offset:9216\n"
"ds_read_b64_tr_b16 v[246:247], v111 offset:9984\n"
"v_pk_mul_f32 v[62:63], v[62:63], v[128:129] op_sel:[0,1] op_sel_hi:[1,1]\n"
"v_cvt_pk_bf16_f32 v56, v44, v45\n"
"v_cvt_pk_bf16_f32 v57, v46, v47\n"
"v_cvt_pk_bf16_f32 v58, v60, v61\n"
"v_cvt_pk_bf16_f32 v59, v62, v63\n"
"s_waitcnt lgkmcnt(14)\n"
"v_and_b32 v36, 0xffff, v24\n"
"v_and_b32 v37, s61, v24\n"
"v_and_b32 v38, 0xffff, v25\n"
"v_and_b32 v39, s61, v25\n"
"v_and_b32 v40, 0xffff, v26\n"
"v_and_b32 v41, s61, v26\n"
"ds_read_b64_tr_b16 v[248:249], v111 offset:9248\n"
"ds_read_b64_tr_b16 v[250:251], v111 offset:10016\n"
"v_and_b32 v42, 0xffff, v27\n"
"v_and_b32 v43, s61, v27\n"
"v_dot2_f32_bf16 v44, v56, v36, 0\n"
"v_dot2_f32_bf16 v45, v56, v37, 0\n"
"v_dot2_f32_bf16 v46, v57, v38, 0\n"
"v_dot2_f32_bf16 v47, v57, v39, 0\n"
"v_dot2_f32_bf16 v60, v58, v40, 0\n"
"v_dot2_f32_bf16 v61, v58, v41, 0\n"
"v_dot2_f32_bf16 v62, v59, v42, 0\n"
"v_dot2_f32_bf16 v63, v59, v43, 0\n"
"v_cvt_pk_bf16_f32 v44, v44, v28\n"
"ds_read_b64_tr_b16 v[252:253], v111 offset:9280\n"
"ds_read_b64_tr_b16 v[254:255], v111 offset:10048\n"
"v_cvt_pk_bf16_f32 v45, v45, v29\n"
"v_cvt_pk_bf16_f32 v46, v46, v30\n"
"v_cvt_pk_bf16_f32 v47, v47, v31\n"
"v_cvt_pk_bf16_f32 v60, v60, v32\n"
"v_cvt_pk_bf16_f32 v61, v61, v33\n"
"v_cvt_pk_bf16_f32 v62, v62, v34\n"
"v_cvt_pk_bf16_f32 v63, v63, v35\n"
"v_dot2_f32_bf16 v28, v44, s94, 0\n"
"v_dot2_f32_bf16 v29, v45, s94, 0\n"
"v_dot2_f32_bf16 v30, v46, s94, 0\n"
"v_dot2_f32_bf16 v31, v47, s94, 0\n"
"s_add_u32 m0, s73, 0\n"
"s_add_u32 s27, s81, 0\n"
"buffer_load_dwordx4 v125, s[16:19], s27 offen lds\n"
"s_add_u32 s27, s27, 0x1c00\n"
"buffer_load_dwordx4 v125, s[16:19], s27 offen offset:1024 lds\n"
"v_dot2_f32_bf16 v32, v60, s94, 0\n"
"v_dot2_f32_bf16 v33, v61, s94, 0\n"
"v_dot2_f32_bf16 v34, v62, s94, 0\n"
"v_dot2_f32_bf16 v35, v63, s94, 0\n"
"v_cvt_pk_bf16_f32 v56, v28, v29\n"
"v_cvt_pk_bf16_f32 v57, v30, v31\n"
"v_cvt_pk_bf16_f32 v58, v32, v33\n"
"v_cvt_pk_bf16_f32 v59, v34, v35\n"
"v_min_u32 v56, -2, v56\n"
"v_min_u32 v57, -2, v57\n"
"s_waitcnt vmcnt(10)\n"
"ds_read_b64_tr_b16 a[0:1], v123 offset:12288\n"
"ds_read_b64_tr_b16 a[2:3], v123 offset:13312\n"
"v_min_u32 v58, -2, v58\n"
"v_min_u32 v59, -2, v59\n"
"s_add_u32 s53, s30, 0x8000\n"
"buffer_store_dwordx4 v[56:59], v15, s[8:11], s53 offen sc1\n"
"v_add_f32 v36, v28, v29\n"
"v_mul_f32 v37, v28, v28\n"
"v_add_f32 v38, v30, v31\n"
"v_mul_f32 v39, v29, v29\n"
"v_add_f32 v36, v36, v32\n"
"v_fmac_f32 v37, v30, v30\n"
"v_add_f32 v38, v38, v33\n"
"v_fmac_f32 v39, v31, v31\n"
"ds_read_b64_tr_b16 a[4:5], v122 offset:12288\n"
"ds_read_b64_tr_b16 a[6:7], v122 offset:13312\n"
"v_add_f32 v36, v36, v34\n"
"v_fmac_f32 v37, v32, v32\n"
"v_add_f32 v38, v38, v35\n"
"v_fmac_f32 v39, v33, v33\n"
"v_add_f32 v36, v36, v38\n"
"v_fmac_f32 v37, v34, v34\n"
"v_fmac_f32 v39, v35, v35\n"
"v_add_f32 v37, v37, v39\n"
"v_add_f32_dpp v38, v36, v36 row_ror:8 row_mask:0xf bank_mask:0xf\n"
"s_nop 0\n"
"v_add_f32_dpp v39, v37, v37 row_ror:8 row_mask:0xf bank_mask:0xf\n"
"s_nop 1\n"
"v_permlane16_swap_b32 v38, v39\n"
"s_add_u32 m0, s73, 0x800\n"
"s_add_u32 s27, s78, 0x18000\n"
"s_waitcnt lgkmcnt(14)\n"
"buffer_load_dwordx4 v125, s[16:19], s27 offen lds\n"
"s_add_u32 s27, s27, 0x1c00\n"
"buffer_load_dwordx4 v125, s[16:19], s27 offen offset:1024 lds\n"
"v_add_f32 v38, v38, v39\n"
"v_mov_b32 v39, v38\n"
"s_nop 1\n"
"v_permlane32_swap_b32 v38, v39\n"
"v_add_f32 v36, v38, v39\n"
"v_mov_b32 v37, v36\n"
"v_mov_b32 v38, v36\n"
"s_nop 1\n"
"v_permlane16_swap_b32 v38, v37\n"
"v_min_u32 v36, -2, v36\n"
"v_min_u32 v37, -2, v37\n"
"s_add_u32 s53, s39, 0x20800\n"
"buffer_store_dwordx2 v[36:37], v137, s[8:11], s53 offen sc1\n"
"s_waitcnt vmcnt(12)\n"
"ds_read_b64_tr_b16 a[8:9], v123 offset:14336\n"
"ds_read_b64_tr_b16 a[10:11], v123 offset:15360\n"
"ds_read_b64_tr_b16 a[12:13], v122 offset:14336\n"
"ds_read_b64_tr_b16 a[14:15], v122 offset:15360\n"
"s_add_u32 m0, s73, 0x1000\n"
"s_add_u32 s27, s79, 0xc000\n"
"buffer_load_dwordx4 v125, s[16:19], s27 offen lds\n"
"s_add_u32 s27, s27, 0x1c00\n"
"buffer_load_dwordx4 v125, s[16:19], s27 offen offset:1024 lds\n"
"s_waitcnt vmcnt(11)\n"
"ds_read_b64_tr_b16 a[32:33], v123 offset:16384\n"
"ds_read_b64_tr_b16 a[34:35], v123 offset:17408\n"
"ds_read_b64_tr_b16 a[36:37], v122 offset:16384\n"
"ds_read_b64_tr_b16 a[38:39], v122 offset:17408\n"
"s_add_u32 m0, s73, 0x1800\n"
"s_add_u32 s27, s80, 0xc000\n"
"s_waitcnt lgkmcnt(14)\n"
"buffer_load_dwordx4 v125, s[16:19], s27 offen lds\n"
"s_add_u32 s27, s27, 0x1c00\n"
"buffer_load_dwordx4 v125, s[16:19], s27 offen offset:1024 lds\n"
"s_waitcnt vmcnt(10)\n"
"ds_read_b64_tr_b16 a[64:65], v123 offset:18432\n"
"ds_read_b64_tr_b16 a[66:67], v123 offset:19456\n"
"ds_read_b64_tr_b16 a[68:69], v122 offset:18432\n"
"ds_read_b64_tr_b16 a[70:71], v122 offset:19456\n"
"s_add_u32 m0, s73, 0x2000\n"
"s_add_u32 s27, s81, 0xc000\n"
"s_waitcnt lgkmcnt(14)\n"
"buffer_load_dwordx4 v125, s[16:19], s27 offen lds\n"
"s_add_u32 s27, s27, 0x1c00\n"
"buffer_load_dwordx4 v125, s[16:19], s27 offen offset:1024 lds\n"
"v_mfma_f32_16x16x32_bf16 a[168:171], a[248:251], v[16:19], a[168:171]\n"
"v_mfma_f32_16x16x32_bf16 a[172:175], a[252:255], v[16:19], a[172:175]\n"
"v_mfma_f32_16x16x32_bf16 a[160:163], v[144:147], v[16:19], a[160:163]\n"
"v_mfma_f32_16x16x32_bf16 a[168:171], v[184:187], v[16:19], a[168:171]\n"
"v_mfma_f32_16x16x32_bf16 a[172:175], v[188:191], v[16:19], a[172:175]\n"
"v_mfma_f32_16x16x32_bf16 a[160:163], v[192:195], v[16:19], a[160:163]\n"
"v_mfma_f32_16x16x32_bf16 a[168:171], v[232:235], v[16:19], a[168:171]\n"
"v_mfma_f32_16x16x32_bf16 a[172:175], v[236:239], v[16:19], a[172:175]\n"
"v_mfma_f32_16x16x32_bf16 a[160:163], v[240:243], v[16:19], a[160:163]\n"
"v_mfma_f32_16x16x32_bf16 a[168:171], v[148:151], v[16:19], a[168:171]\n"
"v_mfma_f32_16x16x32_bf16 a[172:175], v[152:155], v[16:19], a[172:175]\n"
"v_mfma_f32_16x16x32_bf16 a[160:163], v[156:159], v[16:19], a[160:163]\n"
"v_mfma_f32_16x16x32_bf16 a[168:171], v[196:199], v[16:19], a[168:171]\n"
"v_mfma_f32_16x16x32_bf16 a[172:175], v[200:203], v[16:19], a[172:175]\n"
"v_mfma_f32_16x16x32_bf16 a[160:163], v[204:207], v[16:19], a[160:163]\n"
"v_mfma_f32_16x16x32_bf16 a[168:171], v[244:247], v[16:19], a[168:171]\n"
"v_mfma_f32_16x16x32_bf16 a[172:175], v[248:251], v[16:19], a[172:175]\n"
"v_mfma_f32_16x16x32_bf16 a[160:163], v[252:255], v[16:19], a[160:163]\n"
"v_cmp_eq_u32 vcc, 0, v107\n"
"v_mov_b32 v141, 0x40000\n"
"s_nop 0\n"
"v_cndmask_b32 v141, v141, v10, vcc\n"
"s_nop 1\n"
"ds_write_b128 v141, a[168:171]\n"
"ds_write_b128 v141, a[172:175] offset:64\n"
"ds_write_b128 v141, a[160:163] offset:128\n"
"s_sleep 6\n"
"s_add_u32 s59, s93, 0x20000\n"
"buffer_load_dwordx2 v[0:1], v11, s[8:11], s59 offen sc1\n"
"s_add_u32 s59, s93, 0x20000\n"
"buffer_load_dwordx2 v[2:3], v11, s[8:11], s59 offen offset:128 sc1\n"
"s_add_u32 s59, s93, 0x20400\n"
"buffer_load_dwordx2 v[28:29], v11, s[8:11], s59 offen sc1\n"
"s_add_u32 s59, s93, 0x20400\n"
"buffer_load_dwordx2 v[30:31], v11, s[8:11], s59 offen offset:128 sc1\n"
"s_add_u32 m0, s87, 0\n"
"s_add_u32 s27, s86, 0\n"
"buffer_load_dwordx4 v142, s[8:11], s27 offen sc1 lds\n"
"buffer_load_dwordx4 v142, s[8:11], s27 offen offset:1024 sc1 lds\n"
"buffer_load_dwordx4 v142, s[8:11], s27 offen offset:2048 sc1 lds\n"
"buffer_load_dwordx4 v142, s[8:11], s27 offen offset:3072 sc1 lds\n"
"s_add_u32 m0, s87, 0x4000\n"
"s_add_u32 s27, s86, 0x4000\n"
"buffer_load_dwordx4 v142, s[8:11], s27 offen sc1 lds\n"
"buffer_load_dwordx4 v142, s[8:11], s27 offen offset:1024 sc1 lds\n"
"buffer_load_dwordx4 v142, s[8:11], s27 offen offset:2048 sc1 lds\n"
"buffer_load_dwordx4 v142, s[8:11], s27 offen offset:3072 sc1 lds\n"
"s_waitcnt vmcnt(22)\n"
"ds_read_b64_tr_b16 a[96:97], v123\n"
"ds_read_b64_tr_b16 a[98:99], v123 offset:1024\n"
"ds_read_b64_tr_b16 a[100:101], v122\n"
"ds_read_b64_tr_b16 a[102:103], v122 offset:1024\n"
"s_waitcnt vmcnt(19)\n"
"ds_read_b64_tr_b16 a[16:17], v123 offset:2048\n"
"ds_read_b64_tr_b16 a[18:19], v123 offset:3072\n"
"ds_read_b64_tr_b16 a[20:21], v122 offset:2048\n"
"ds_read_b64_tr_b16 a[22:23], v122 offset:3072\n"
"s_waitcnt vmcnt(16)\n"
"ds_read_b64_tr_b16 a[40:41], v123 offset:4096\n"
"ds_read_b64_tr_b16 a[42:43], v123 offset:5120\n"
"ds_read_b64_tr_b16 a[44:45], v122 offset:4096\n"
"ds_read_b64_tr_b16 a[46:47], v122 offset:5120\n"
"s_add_u32 m0, s73, 0x2800\n"
"s_add_u32 s27, s78, s84\n"
"buffer_load_dwordx4 v125, s[16:19], s27 offen lds\n"
"s_add_u32 s27, s27, 0x1c00\n"
"buffer_load_dwordx4 v125, s[16:19], s27 offen offset:1024 lds\n"
"s_add_u32 m0, s73, 0x3000\n"
"s_add_u32 s27, s79, 0x18000\n"
"s_waitcnt lgkmcnt(14)\n"
"buffer_load_dwordx4 v125, s[16:19], s27 offen lds\n"
"s_add_u32 s27, s27, 0x1c00\n"
"buffer_load_dwordx4 v125, s[16:19], s27 offen offset:1024 lds\n"
"s_add_u32 m0, s73, 0x3800\n"
"s_add_u32 s27, s80, 0x18000\n"
"buffer_load_dwordx4 v125, s[16:19], s27 offen lds\n"
"s_add_u32 s27, s27, 0x1c00\n"
"buffer_load_dwordx4 v125, s[16:19], s27 offen offset:1024 lds\n"
"s_waitcnt vmcnt(16)\n"
"v_max3_u32 v134, v0, v1, v2\n"
"s_waitcnt vmcnt(15)\n"
"v_max3_u32 v134, v134, v3, v28\n"
"s_waitcnt vmcnt(14)\n"
"v_max3_u32 v134, v134, v29, v30\n"
"v_max_u32 v134, v134, v31\n"
"v_cmp_eq_u32 vcc, -1, v134\n"
"s_cbranch_vccnz Lslow6_xs00\n"
"Lback6_xs00:\n"
"s_waitcnt vmcnt(6)\n"
"s_barrier\n"
"ds_read_b128 v[72:75], v4\n"
"ds_read_b128 v[76:79], v4 offset:16384\n"
"ds_read_b128 v[80:83], v4 offset:1024\n"
"ds_read_b128 v[84:87], v4 offset:17408\n"
"s_waitcnt lgkmcnt(2)\n"
"v_mfma_f32_16x16x32_bf16 v[40:43], a[176:179], v[72:75], 0\n"
"ds_read_b128 v[88:91], v5\n"
"v_mfma_f32_16x16x32_bf16 v[48:51], a[180:183], v[72:75], 0\n"
"ds_read_b128 v[92:95], v5 offset:16384\n"
"v_mfma_f32_16x16x32_bf16 v[64:67], a[184:187], v[72:75], 0\n"
"v_add_f32 v0, v0, v2\n"
"v_mfma_f32_16x16x32_bf16 v[44:47], a[176:179], v[76:79], 0\n"
"v_add_f32 v1, v1, v3\n"
"v_mfma_f32_16x16x32_bf16 v[60:63], a[180:183], v[76:79], 0\n"
"v_mfma_f32_16x16x32_bf16 v[68:71], a[184:187], v[76:79], 0\n"
"v_permlane16_swap_b32 v0, v1\n"
"ds_read_b64_tr_b16 a[72:73], v123 offset:6144\n"
"ds_read_b64_tr_b16 a[74:75], v123 offset:7168\n"
"s_waitcnt lgkmcnt(4)\n"
"v_mfma_f32_16x16x32_bf16 v[40:43], a[188:191], v[80:83], v[40:43]\n"
"ds_read_b128 v[72:75], v6\n"
"v_add_f32 v0, v0, v1\n"
"v_mfma_f32_16x16x32_bf16 v[48:51], a[192:195], v[80:83], v[48:51]\n"
"ds_read_b128 v[76:79], v6 offset:16384\n"
"ds_read_b64_tr_b16 a[76:77], v122 offset:6144\n"
"ds_read_b64_tr_b16 a[78:79], v122 offset:7168\n"
"v_mfma_f32_16x16x32_bf16 v[64:67], a[196:199], v[80:83], v[64:67]\n"
"v_mov_b32 v1, v0\n"
"v_mfma_f32_16x16x32_bf16 v[44:47], a[188:191], v[84:87], v[44:47]\n"
"s_nop 0\n"
"v_permlane32_swap_b32 v0, v1\n"
"s_add_u32 m0, s73, 0x4000\n"
"s_add_u32 s27, s81, 0x18000\n"
"buffer_load_dwordx4 v125, s[16:19], s27 offen lds\n"
"s_add_u32 s27, s27, 0x1c00\n"
"buffer_load_dwordx4 v125, s[16:19], s27 offen offset:1024 lds\n"
"v_mfma_f32_16x16x32_bf16 v[60:63], a[192:195], v[84:87], v[60:63]\n"
"v_mfma_f32_16x16x32_bf16 v[68:71], a[196:199], v[84:87], v[68:71]\n"
"v_add_f32 v0, v0, v1\n"
"ds_read_b64_tr_b16 a[104:105], v123 offset:8192\n"
"ds_read_b64_tr_b16 a[106:107], v123 offset:9216\n"
"s_waitcnt lgkmcnt(8)\n"
"v_mfma_f32_16x16x32_bf16 v[40:43], a[224:227], v[88:91], v[40:43]\n"
"ds_read_b128 v[80:83], v7\n"
"v_mov_b32 v1, v0\n"
"v_mfma_f32_16x16x32_bf16 v[48:51], a[228:231], v[88:91], v[48:51]\n"
"ds_read_b128 v[84:87], v7 offset:16384\n"
"v_mfma_f32_16x16x32_bf16 v[64:67], a[232:235], v[88:91], v[64:67]\n"
"v_permlane16_swap_b32 v0, v1\n"
"ds_read_b64_tr_b16 a[108:109], v122 offset:8192\n"
"ds_read_b64_tr_b16 a[110:111], v122 offset:9216\n"
"v_mfma_f32_16x16x32_bf16 v[44:47], a[224:227], v[92:95], v[44:47]\n"
"v_mul_f32 v8, 0xbb000000, v0\n"
"v_mfma_f32_16x16x32_bf16 v[60:63], a[228:231], v[92:95], v[60:63]\n"
"s_add_u32 m0, s73, 0x4800\n"
"s_add_u32 s27, s79, s84\n"
"buffer_load_dwordx4 v125, s[16:19], s27 offen lds\n"
"s_add_u32 s27, s27, 0x1c00\n"
"buffer_load_dwordx4 v125, s[16:19], s27 offen offset:1024 lds\n"
"v_mfma_f32_16x16x32_bf16 v[68:71], a[232:235], v[92:95], v[68:71]\n"
"v_mul_f32 v2, v8, v8\n"
"s_waitcnt lgkmcnt(8)\n"
"v_mfma_f32_16x16x32_bf16 v[40:43], v[160:163], v[72:75], v[40:43]\n"
"ds_read_b128 v[88:91], v4 offset:2048\n"
"v_fma_f32 v1, v1, s37, -v2\n"
"s_waitcnt vmcnt(8)\n"
"ds_read_b64_tr_b16 a[24:25], v123 offset:10240\n"
"ds_read_b64_tr_b16 a[26:27], v123 offset:11264\n"
"v_mfma_f32_16x16x32_bf16 v[48:51], v[164:167], v[72:75], v[48:51]\n"
"ds_read_b128 v[92:95], v4 offset:18432\n"
"v_mfma_f32_16x16x32_bf16 v[64:67], v[168:171], v[72:75], v[64:67]\n"
"v_max_f32 v1, 0, v1\n"
"ds_read_b64_tr_b16 a[28:29], v122 offset:10240\n"
"ds_read_b64_tr_b16 a[30:31], v122 offset:11264\n"
"v_mfma_f32_16x16x32_bf16 v[44:47], v[160:163], v[76:79], v[44:47]\n"
"v_add_f32 v1, 0x3727c5ac, v1\n"
"v_mfma_f32_16x16x32_bf16 v[60:63], v[164:167], v[76:79], v[60:63]\n"
"v_mfma_f32_16x16x32_bf16 v[68:71], v[168:171], v[76:79], v[68:71]\n"
"v_rsq_f32 v1, v1\n"
"s_add_u32 m0, s73, 0\n"
"s_add_u32 s27, s80, s84\n"
"buffer_load_dwordx4 v125, s[16:19], s27 offen lds\n"
"s_add_u32 s27, s27, 0x1c00\n"
"buffer_load_dwordx4 v125, s[16:19], s27 offen offset:1024 lds\n"
"s_waitcnt lgkmcnt(8)\n"
"v_mfma_f32_16x16x32_bf16 v[40:43], v[208:211], v[80:83], v[40:43]\n"
"ds_read_b128 v[72:75], v5 offset:1024\n"
"v_add_f32 v28, v28, v30\n"
"v_mfma_f32_16x16x32_bf16 v[48:51], v[212:215], v[80:83], v[48:51]\n"
"ds_read_b128 v[76:79], v5 offset:17408\n"
"s_waitcnt vmcnt(8)\n"
"ds_read_b64_tr_b16 a[48:49], v123 offset:12288\n"
"ds_read_b64_tr_b16 a[50:51], v123 offset:13312\n"
"v_mfma_f32_16x16x32_bf16 v[64:67], v[216:219], v[80:83], v[64:67]\n"
"v_add_f32 v29, v29, v31\n"
"v_mfma_f32_16x16x32_bf16 v[44:47], v[208:211], v[84:87], v[44:47]\n"
"ds_read_b64_tr_b16 a[52:53], v122 offset:12288\n"
"ds_read_b64_tr_b16 a[54:55], v122 offset:13312\n"
"v_mfma_f32_16x16x32_bf16 v[60:63], v[212:215], v[84:87], v[60:63]\n"
"v_permlane16_swap_b32 v28, v29\n"
"v_mfma_f32_16x16x32_bf16 v[68:71], v[216:219], v[84:87], v[68:71]\n"
"v_add_f32 v28, v28, v29\n"
"s_add_u32 m0, s73, 0x800\n"
"s_add_u32 s27, s81, s84\n"
"buffer_load_dwordx4 v125, s[16:19], s27 offen lds\n"
"s_add_u32 s27, s27, 0x1c00\n"
"buffer_load_dwordx4 v125, s[16:19], s27 offen offset:1024 lds\n"
"s_waitcnt lgkmcnt(8)\n"
"v_mfma_f32_16x16x32_bf16 v[40:43], a[200:203], v[88:91], v[40:43]\n"
"ds_read_b128 v[80:83], v6 offset:1024\n"
"s_add_u32 s59, s93, 0x20800\n"
"buffer_load_dwordx2 v[32:33], v11, s[8:11], s59 offen sc1\n"
"v_mfma_f32_16x16x32_bf16 v[48:51], a[204:207], v[88:91], v[48:51]\n"
"ds_read_b128 v[84:87], v6 offset:17408\n"
"v_mov_b32 v29, v28\n"
"v_mfma_f32_16x16x32_bf16 v[64:67], a[208:211], v[88:91], v[64:67]\n"
"s_nop 0\n"
"v_permlane32_swap_b32 v28, v29\n"
"s_waitcnt vmcnt(9)\n"
"ds_read_b64_tr_b16 a[80:81], v123 offset:14336\n"
"ds_read_b64_tr_b16 a[82:83], v123 offset:15360\n"
"v_mfma_f32_16x16x32_bf16 v[44:47], a[200:203], v[92:95], v[44:47]\n"
"v_mfma_f32_16x16x32_bf16 v[60:63], a[204:207], v[92:95], v[60:63]\n"
"v_add_f32 v28, v28, v29\n"
"ds_read_b64_tr_b16 a[84:85], v122 offset:14336\n"
"ds_read_b64_tr_b16 a[86:87], v122 offset:15360\n"
"s_add_u32 s59, s93, 0x20800\n"
"buffer_load_dwordx2 v[34:35], v11, s[8:11], s59 offen offset:128 sc1\n"
"v_mfma_f32_16x16x32_bf16 v[68:71], a[208:211], v[92:95], v[68:71]\n"
"v_mov_b32 v29, v28\n"
"s_waitcnt lgkmcnt(10)\n"
"v_mfma_f32_16x16x32_bf16 v[40:43], a[236:239], v[72:75], v[40:43]\n"
"ds_read_b128 v[88:91], v7 offset:1024\n"
"s_add_u32 m0, s73, 0x1000\n"
"s_add_u32 s27, s82, 0x8000\n"
"buffer_load_dwordx4 v125, s[16:19], s27 offen lds\n"
"s_add_u32 s27, s27, 0x1c00\n"
"buffer_load_dwordx4 v125, s[16:19], s27 offen offset:1024 lds\n"
"v_mfma_f32_16x16x32_bf16 v[48:51], a[240:243], v[72:75], v[48:51]\n"
"ds_read_b128 v[92:95], v7 offset:17408\n"
"v_permlane16_swap_b32 v28, v29\n"
"v_mfma_f32_16x16x32_bf16 v[64:67], a[244:247], v[72:75], v[64:67]\n"
"v_mul_f32 v9, 0xbb000000, v28\n"
"s_waitcnt vmcnt(10)\n"
"ds_read_b64_tr_b16 a[112:113], v123 offset:16384\n"
"ds_read_b64_tr_b16 a[114:115], v123 offset:17408\n"
"s_add_u32 s59, s93, 0x20c00\n"
"buffer_load_dwordx2 v[36:37], v11, s[8:11], s59 offen sc1\n"
"v_mfma_f32_16x16x32_bf16 v[44:47], a[236:239], v[76:79], v[44:47]\n"
"v_mfma_f32_16x16x32_bf16 v[60:63], a[240:243], v[76:79], v[60:63]\n"
"v_mul_f32 v30, v9, v9\n"
"v_mfma_f32_16x16x32_bf16 v[68:71], a[244:247], v[76:79], v[68:71]\n"
"v_fma_f32 v29, v29, s37, -v30\n"
"ds_read_b64_tr_b16 a[116:117], v122 offset:16384\n"
"ds_read_b64_tr_b16 a[118:119], v122 offset:17408\n"
"s_waitcnt lgkmcnt(10)\n"
"v_mfma_f32_16x16x32_bf16 v[40:43], v[172:175], v[80:83], v[40:43]\n"
"ds_read_b128 v[72:75], v4 offset:3072\n"
"s_add_u32 s59, s93, 0x20c00\n"
"buffer_load_dwordx2 v[38:39], v11, s[8:11], s59 offen offset:128 sc1\n"
"v_mfma_f32_16x16x32_bf16 v[48:51], v[176:179], v[80:83], v[48:51]\n"
"ds_read_b128 v[76:79], v4 offset:19456\n"
"v_max_f32 v29, 0, v29\n"
"s_add_u32 m0, s73, 0x1800\n"
"s_add_u32 s27, s82, 0x14000\n"
"buffer_load_dwordx4 v125, s[16:19], s27 offen lds\n"
"s_add_u32 s27, s27, 0x1c00\n"
"buffer_load_dwordx4 v125, s[16:19], s27 offen offset:1024 lds\n"
"v_mfma_f32_16x16x32_bf16 v[64:67], v[180:183], v[80:83], v[64:67]\n"
"v_add_f32 v29, 0x3727c5ac, v29\n"
"v_mfma_f32_16x16x32_bf16 v[44:47], v[172:175], v[84:87], v[44:47]\n"
"s_waitcnt vmcnt(12)\n"
"ds_read_b64_tr_b16 a[56:57], v123 offset:18432\n"
"ds_read_b64_tr_b16 a[58:59], v123 offset:19456\n"
"v_mfma_f32_16x16x32_bf16 v[60:63], v[176:179], v[84:87], v[60:63]\n"
"v_rsq_f32 v29, v29\n"
"v_mfma_f32_16x16x32_bf16 v[68:71], v[180:183], v[84:87], v[68:71]\n"
"s_cmp_lt_u32 s7, 2\n"
"s_cselect_b64 vcc, -1, 0\n"
"v_cndmask_b32 v1, v29, v1, vcc\n"
"ds_read_b64_tr_b16 a[60:61], v122 offset:18432\n"
"ds_read_b64_tr_b16 a[62:63], v122 offset:19456\n"
"s_add_u32 m0, s87, 0x8000\n"
"s_add_u32 s27, s86, 0x8000\n"
"buffer_load_dwordx4 v142, s[8:11], s27 offen sc1 lds\n"
"s_waitcnt lgkmcnt(10)\n"
"v_mfma_f32_16x16x32_bf16 v[40:43], v[220:223], v[88:91], v[40:43]\n"
"ds_read_b128 v[80:83], v5 offset:2048\n"
"v_mfma_f32_16x16x32_bf16 v[48:51], v[224:227], v[88:91], v[48:51]\n"
"ds_read_b128 v[84:87], v5 offset:18432\n"
"ds_bpermute_b32 v127, v12, v1\n"
"v_mfma_f32_16x16x32_bf16 v[64:67], v[228:231], v[88:91], v[64:67]\n"
"s_add_u32 m0, s73, 0x2000\n"
"s_add_u32 s27, s82, 0x20000\n"
"s_add_u32 s60, s83, 0x8000\n"
"s_cmp_lt_u32 s7, 3\n"
"s_cselect_b32 s27, s27, s60\n"
"buffer_load_dwordx4 v125, s[16:19], s27 offen lds\n"
"s_add_u32 s27, s27, 0x1c00\n"
"buffer_load_dwordx4 v125, s[16:19], s27 offen offset:1024 lds\n"
"v_mfma_f32_16x16x32_bf16 v[44:47], v[220:223], v[92:95], v[44:47]\n"
"s_add_u32 m0, s87, 0x8000\n"
"s_add_u32 s27, s86, 0x8000\n"
"buffer_load_dwordx4 v142, s[8:11], s27 offen offset:1024 sc1 lds\n"
"v_mfma_f32_16x16x32_bf16 v[60:63], v[224:227], v[92:95], v[60:63]\n"
"s_waitcnt vmcnt(14)\n"
"ds_read_b64_tr_b16 a[88:89], v123\n"
"ds_read_b64_tr_b16 a[90:91], v123 offset:1024\n"
"v_mfma_f32_16x16x32_bf16 v[68:71], v[228:231], v[92:95], v[68:71]\n"
"s_waitcnt lgkmcnt(9)\n"
"v_mfma_f32_16x16x32_bf16 v[40:43], a[212:215], v[72:75], v[40:43]\n"
"ds_read_b128 v[88:91], v6 offset:2048\n"
"ds_read_b64_tr_b16 a[92:93], v122\n"
"ds_read_b64_tr_b16 a[94:95], v122 offset:1024\n"
"v_mfma_f32_16x16x32_bf16 v[48:51], a[216:219], v[72:75], v[48:51]\n"
"ds_read_b128 v[92:95], v6 offset:18432\n"
"s_add_u32 m0, s87, 0x8000\n"
"s_add_u32 s27, s86, 0x8000\n"
"buffer_load_dwordx4 v142, s[8:11], s27 offen offset:2048 sc1 lds\n"
"v_mfma_f32_16x16x32_bf16 v[64:67], a[220:223], v[72:75], v[64:67]\n"
"s_add_u32 m0, s73, 0x2800\n"
"s_add_u32 s27, s83, 0x8000\n"
"s_add_u32 s60, s83, 0x14000\n"
"s_cmp_lt_u32 s7, 3\n"
"s_cselect_b32 s27, s27, s60\n"
"buffer_load_dwordx4 v125, s[16:19], s27 offen lds\n"
"s_add_u32 s27, s27, 0x1c00\n"
"buffer_load_dwordx4 v125, s[16:19], s27 offen offset:1024 lds\n"
"v_mfma_f32_16x16x32_bf16 v[44:47], a[212:215], v[76:79], v[44:47]\n"
"v_mfma_f32_16x16x32_bf16 v[60:63], a[216:219], v[76:79], v[60:63]\n"
"v_mfma_f32_16x16x32_bf16 v[68:71], a[220:223], v[76:79], v[68:71]\n"
"s_waitcnt vmcnt(15)\n"
"ds_read_b64_tr_b16 a[120:121], v123 offset:2048\n"
"ds_read_b64_tr_b16 a[122:123], v123 offset:3072\n"
"s_add_u32 m0, s87, 0x8000\n"
"s_add_u32 s27, s86, 0x8000\n"
"buffer_load_dwordx4 v142, s[8:11], s27 offen offset:3072 sc1 lds\n"
"s_waitcnt lgkmcnt(9)\n"
"v_mfma_f32_16x16x32_bf16 v[40:43], a[248:251], v[80:83], v[40:43]\n"
"ds_read_b128 v[72:75], v7 offset:2048\n"
"v_mfma_f32_16x16x32_bf16 v[48:51], a[252:255], v[80:83], v[48:51]\n"
"ds_read_b128 v[76:79], v7 offset:18432\n"
"ds_read_b64_tr_b16 a[124:125], v122 offset:2048\n"
"ds_read_b64_tr_b16 a[126:127], v122 offset:3072\n"
"v_mfma_f32_16x16x32_bf16 v[64:67], v[144:147], v[80:83], v[64:67]\n"
"v_mfma_f32_16x16x32_bf16 v[44:47], a[248:251], v[84:87], v[44:47]\n"
"s_add_u32 m0, s73, 0x3000\n"
"s_add_u32 s27, s83, 0x14000\n"
"buffer_load_dwordx4 v124, s[16:19], s27 offen lds\n"
"s_add_u32 s27, s27, 0x1c00\n"
"buffer_load_dwordx4 v124, s[16:19], s27 offen offset:1024 lds\n"
"v_mfma_f32_16x16x32_bf16 v[60:63], a[252:255], v[84:87], v[60:63]\n"
"s_add_u32 m0, s87, 0xc000\n"
"s_add_u32 s27, s86, 0xc000\n"
"buffer_load_dwordx4 v142, s[8:11], s27 offen sc1 lds\n"
"v_mfma_f32_16x16x32_bf16 v[68:71], v[144:147], v[84:87], v[68:71]\n"
"s_waitcnt vmcnt(15)\n"
"ds_read_b64_tr_b16 a[128:129], v123 offset:4096\n"
"ds_read_b64_tr_b16 a[130:131], v123 offset:5120\n"
"s_waitcnt lgkmcnt(8)\n"
"v_mfma_f32_16x16x32_bf16 v[40:43], v[184:187], v[88:91], v[40:43]\n"
"ds_read_b128 v[80:83], v5 offset:3072\n"
"v_mfma_f32_16x16x32_bf16 v[48:51], v[188:191], v[88:91], v[48:51]\n"
"ds_read_b128 v[84:87], v5 offset:19456\n"
"v_mfma_f32_16x16x32_bf16 v[64:67], v[192:195], v[88:91], v[64:67]\n"
"ds_read_b64_tr_b16 a[132:133], v122 offset:4096\n"
"ds_read_b64_tr_b16 a[134:135], v122 offset:5120\n"
"s_add_u32 m0, s87, 0xc000\n"
"s_add_u32 s27, s86, 0xc000\n"
"buffer_load_dwordx4 v142, s[8:11], s27 offen offset:1024 sc1 lds\n"
"v_mfma_f32_16x16x32_bf16 v[44:47], v[184:187], v[92:95], v[44:47]\n"
"v_mfma_f32_16x16x32_bf16 v[60:63], v[188:191], v[92:95], v[60:63]\n"
"s_add_u32 m0, s73, 0x3800\n"
"s_add_u32 s27, s83, 0x20000\n"
"buffer_load_dwordx4 v124, s[16:19], s27 offen lds\n"
"s_add_u32 s27, s27, 0x1c00\n"
"buffer_load_dwordx4 v124, s[16:19], s27 offen offset:1024 lds\n"
"v_mfma_f32_16x16x32_bf16 v[68:71], v[192:195], v[92:95], v[68:71]\n"
"s_waitcnt lgkmcnt(8)\n"
"v_mfma_f32_16x16x32_bf16 v[40:43], v[232:235], v[72:75], v[40:43]\n"
"ds_read_b128 v[88:91], v6 offset:3072\n"
"s_waitcnt vmcnt(14)\n"
"ds_read_b64_tr_b16 a[136:137], v123 offset:6144\n"
"ds_read_b64_tr_b16 a[138:139], v123 offset:7168\n"
"s_add_u32 m0, s87, 0xc000\n"
"s_add_u32 s27, s86, 0xc000\n"
"buffer_load_dwordx4 v142, s[8:11], s27 offen offset:2048 sc1 lds\n"
"v_mfma_f32_16x16x32_bf16 v[48:51], v[236:239], v[72:75], v[48:51]\n"
"ds_read_b128 v[92:95], v6 offset:19456\n"
"v_mfma_f32_16x16x32_bf16 v[64:67], v[240:243], v[72:75], v[64:67]\n"
"ds_read_b64_tr_b16 a[140:141], v122 offset:6144\n"
"ds_read_b64_tr_b16 a[142:143], v122 offset:7168\n"
"v_mfma_f32_16x16x32_bf16 v[44:47], v[232:235], v[76:79], v[44:47]\n"
"v_mfma_f32_16x16x32_bf16 v[60:63], v[236:239], v[76:79], v[60:63]\n"
"s_add_u32 m0, s87, 0xc000\n"
"s_add_u32 s27, s86, 0xc000\n"
"buffer_load_dwordx4 v142, s[8:11], s27 offen offset:3072 sc1 lds\n"
"v_mfma_f32_16x16x32_bf16 v[68:71], v[240:243], v[76:79], v[68:71]\n"
"s_waitcnt lgkmcnt(8)\n"
"v_mfma_f32_16x16x32_bf16 v[40:43], v[148:151], v[80:83], v[40:43]\n"
"ds_read_b128 v[72:75], v7 offset:3072\n"
"v_mfma_f32_16x16x32_bf16 v[48:51], v[152:155], v[80:83], v[48:51]\n"
"ds_read_b128 v[76:79], v7 offset:19456\n"
"s_waitcnt vmcnt(13)\n"
"ds_read_b64_tr_b16 a[144:145], v123 offset:8192\n"
"ds_read_b64_tr_b16 a[146:147], v123 offset:9216\n"
"v_mfma_f32_16x16x32_bf16 v[64:67], v[156:159], v[80:83], v[64:67]\n"
"v_mfma_f32_16x16x32_bf16 v[44:47], v[148:151], v[84:87], v[44:47]\n"
"ds_read_b64_tr_b16 a[148:149], v122 offset:8192\n"
"ds_read_b64_tr_b16 a[150:151], v122 offset:9216\n"
"v_mfma_f32_16x16x32_bf16 v[60:63], v[152:155], v[84:87], v[60:63]\n"
"v_mfma_f32_16x16x32_bf16 v[68:71], v[156:159], v[84:87], v[68:71]\n"
"s_waitcnt lgkmcnt(8)\n"
"v_mfma_f32_16x16x32_bf16 v[40:43], v[196:199], v[88:91], v[40:43]\n"
"v_mfma_f32_16x16x32_bf16 v[48:51], v[200:203], v[88:91], v[48:51]\n"
"ds_read_b128 v[80:83], v10\n"
"v_mfma_f32_16x16x32_bf16 v[64:67], v[204:207], v[88:91], v[64:67]\n"
"s_waitcnt vmcnt(9)\n"
"ds_read_b64_tr_b16 a[152:153], v123 offset:10240\n"
"ds_read_b64_tr_b16 a[154:155], v123 offset:11264\n"
"v_mfma_f32_16x16x32_bf16 v[44:47], v[196:199], v[92:95], v[44:47]\n"
"ds_read_b128 v[84:87], v10 offset:64\n"
"v_mfma_f32_16x16x32_bf16 v[60:63], v[200:203], v[92:95], v[60:63]\n"
"ds_read_b64_tr_b16 a[156:157], v122 offset:10240\n"
"ds_read_b64_tr_b16 a[158:159], v122 offset:11264\n"
"v_mfma_f32_16x16x32_bf16 v[68:71], v[204:207], v[92:95], v[68:71]\n"
"s_waitcnt lgkmcnt(10)\n"
"v_mfma_f32_16x16x32_bf16 v[40:43], v[244:247], v[72:75], v[40:43]\n"
"ds_read_b128 v[88:91], v10 offset:128\n"
"v_mfma_f32_16x16x32_bf16 v[48:51], v[248:251], v[72:75], v[48:51]\n"
"v_mfma_f32_16x16x32_bf16 v[64:67], v[252:255], v[72:75], v[64:67]\n"
"v_mfma_f32_16x16x32_bf16 v[44:47], v[244:247], v[76:79], v[44:47]\n"
"v_mfma_f32_16x16x32_bf16 v[60:63], v[248:251], v[76:79], v[60:63]\n"
"v_mfma_f32_16x16x32_bf16 v[68:71], v[252:255], v[76:79], v[68:71]\n"
"s_nop 5\n"
"v_cmp_u_f32 vcc, v40, v44\n"
"s_cbranch_vccnz Lslowx00\n"
"Lbackx00:\n"
"s_waitcnt lgkmcnt(6)\n"
"v_pk_fma_f32 v[40:41], v[80:81], v[8:9], v[40:41] op_sel:[0,0,0] op_sel_hi:[1,0,1]\n"
"v_pk_fma_f32 v[42:43], v[82:83], v[8:9], v[42:43] op_sel:[0,0,0] op_sel_hi:[1,0,1]\n"
"v_pk_fma_f32 v[44:45], v[80:81], v[8:9], v[44:45] op_sel:[0,1,0] op_sel_hi:[1,1,1]\n"
"v_pk_fma_f32 v[46:47], v[82:83], v[8:9], v[46:47] op_sel:[0,1,0] op_sel_hi:[1,1,1]\n"
"v_cvt_pk_bf16_f32 v92, v40, v41\n"
"v_cvt_pk_bf16_f32 v93, v42, v43\n"
"v_cvt_pk_bf16_f32 v94, v44, v45\n"
"v_cvt_pk_bf16_f32 v95, v46, v47\n"
"v_pk_max_i16 v92, v92, 0\n"
"v_pk_max_i16 v94, v94, 0\n"
"v_pk_max_i16 v93, v93, 0\n"
"v_pk_max_i16 v95, v95, 0\n"
"s_add_u32 s53, s33, 0\n"
"v_permlane32_swap_b32 v92, v94\n"
"v_permlane32_swap_b32 v93, v95\n"
"v_xor_b32 v92, v14, v92\n"
"buffer_store_dwordx4 v[92:95], v13, s[8:11], s53 offen sc1\n"
"s_waitcnt lgkmcnt(3)\n"
"v_pk_fma_f32 v[48:49], v[84:85], v[8:9], v[48:49] op_sel:[0,0,0] op_sel_hi:[1,0,1]\n"
"v_pk_fma_f32 v[50:51], v[86:87], v[8:9], v[50:51] op_sel:[0,0,0] op_sel_hi:[1,0,1]\n"
"v_pk_fma_f32 v[60:61], v[84:85], v[8:9], v[60:61] op_sel:[0,1,0] op_sel_hi:[1,1,1]\n"
"v_pk_fma_f32 v[62:63], v[86:87], v[8:9], v[62:63] op_sel:[0,1,0] op_sel_hi:[1,1,1]\n"
"v_cvt_pk_bf16_f32 v72, v48, v49\n"
"v_cvt_pk_bf16_f32 v73, v50, v51\n"
"v_cvt_pk_bf16_f32 v74, v60, v61\n"
"v_cvt_pk_bf16_f32 v75, v62, v63\n"
"v_pk_max_i16 v72, v72, 0\n"
"v_pk_max_i16 v74, v74, 0\n"
"v_pk_max_i16 v73, v73, 0\n"
"v_pk_max_i16 v75, v75, 0\n"
"s_add_u32 s53, s33, 0x2000\n"
"v_permlane32_swap_b32 v72, v74\n"
"v_permlane32_swap_b32 v73, v75\n"
"v_xor_b32 v72, v14, v72\n"
"buffer_store_dwordx4 v[72:75], v13, s[8:11], s53 offen sc1\n"
"s_waitcnt lgkmcnt(0)\n"
"v_pk_fma_f32 v[64:65], v[88:89], v[8:9], v[64:65] op_sel:[0,0,0] op_sel_hi:[1,0,1]\n"
"v_pk_fma_f32 v[66:67], v[90:91], v[8:9], v[66:67] op_sel:[0,0,0] op_sel_hi:[1,0,1]\n"
"v_pk_fma_f32 v[68:69], v[88:89], v[8:9], v[68:69] op_sel:[0,1,0] op_sel_hi:[1,1,1]\n"
"v_pk_fma_f32 v[70:71], v[90:91], v[8:9], v[70:71] op_sel:[0,1,0] op_sel_hi:[1,1,1]\n"
"v_cvt_pk_bf16_f32 v92, v64, v65\n"
"v_cvt_pk_bf16_f32 v93, v66, v67\n"
"v_cvt_pk_bf16_f32 v94, v68, v69\n"
"v_cvt_pk_bf16_f32 v95, v70, v71\n"
"v_pk_max_i16 v92, v92, 0\n"
"v_pk_max_i16 v94, v94, 0\n"
"v_pk_max_i16 v93, v93, 0\n"
"v_pk_max_i16 v95, v95, 0\n"
"s_add_u32 s53, s34, 0\n"
"v_permlane32_swap_b32 v92, v94\n"
"v_permlane32_swap_b32 v93, v95\n"
"v_xor_b32 v92, v14, v92\n"
"buffer_store_dwordx4 v[92:95], v13, s[8:11], s53 offen sc1\n"
"s_add_u32 s56, s56, 1\n"
"s_addc_u32 s57, s57, 0\n"
"v_mov_b32 v130, s56\n"
"v_mov_b32 v131, s57\n"
"s_lshl_b32 s53, s6, 2\n"
"s_add_u32 s53, s53, s7\n"
"s_cmp_eq_u32 s53, 0\n"
"s_cselect_b32 s53, 0, 0x80000000\n"
"v_or_b32 v141, s53, v99\n"
"buffer_store_dwordx2 v[130:131], v141, s[8:11], 0 offen sc1\n"
"v_mov_b32 v0, -1\n"
"v_mov_b32 v1, -1\n"
"v_mov_b32 v2, -1\n"
"v_mov_b32 v3, -1\n"
"s_add_u32 s59, s30, 0x200000\n"
"buffer_store_dwordx4 v[0:3], v15, s[8:11], s59 offen sc1\n"
"s_add_u32 s59, s30, 0x208000\n"
"buffer_store_dwordx4 v[0:3], v15, s[8:11], s59 offen sc1\n"
"v_max3_u32 v134, v32, v33, v34\n"
"v_max3_u32 v134, v134, v35, v36\n"
"v_max3_u32 v134, v134, v37, v38\n"
"v_max_u32 v134, v134, v39\n"
"v_cmp_eq_u32 vcc, -1, v134\n"
"s_cbranch_vccnz Lslow7_xs01\n"
"Lback7_xs01:\n"
"s_waitcnt vmcnt(6)\n"
"s_barrier\n"
"ds_read_b128 v[72:75], v4 offset:32768\n"
"ds_read_b128 v[76:79], v4 offset:49152\n"
"ds_read_b128 v[80:83], v4 offset:33792\n"
"ds_read_b128 v[84:87], v4 offset:50176\n"
"s_waitcnt lgkmcnt(2)\n"
"v_mfma_f32_16x16x32_bf16 v[40:43], a[176:179], v[72:75], 0\n"
"ds_read_b128 v[88:91], v5 offset:32768\n"
"ds_read_b64_tr_b16 a[160:161], v123 offset:12288\n"
"ds_read_b64_tr_b16 a[162:163], v123 offset:13312\n"
"v_mfma_f32_16x16x32_bf16 v[48:51], a[180:183], v[72:75], 0\n"
"ds_read_b128 v[92:95], v5 offset:49152\n"
"v_mfma_f32_16x16x32_bf16 v[64:67], a[184:187], v[72:75], 0\n"
"v_add_f32 v32, v32, v34\n"
"v_mfma_f32_16x16x32_bf16 v[44:47], a[176:179], v[76:79], 0\n"
"v_add_f32 v33, v33, v35\n"
"v_mfma_f32_16x16x32_bf16 v[60:63], a[180:183], v[76:79], 0\n"
"s_nop 0\n"
"v_permlane16_swap_b32 v32, v33\n"
"v_mfma_f32_16x16x32_bf16 v[68:71], a[184:187], v[76:79], 0\n"
"v_add_f32 v32, v32, v33\n"
"s_waitcnt lgkmcnt(4)\n"
"v_mfma_f32_16x16x32_bf16 v[40:43], a[188:191], v[80:83], v[40:43]\n"
"ds_read_b128 v[72:75], v6 offset:32768\n"
"ds_read_b64_tr_b16 a[164:165], v122 offset:12288\n"
"ds_read_b64_tr_b16 a[166:167], v122 offset:13312\n"
"v_mov_b32 v33, v32\n"
"v_mfma_f32_16x16x32_bf16 v[48:51], a[192:195], v[80:83], v[48:51]\n"
"ds_read_b128 v[76:79], v6 offset:49152\n"
"v_permlane32_swap_b32 v32, v33\n"
"v_mfma_f32_16x16x32_bf16 v[64:67], a[196:199], v[80:83], v[64:67]\n"
"v_add_f32 v32, v32, v33\n"
"v_mfma_f32_16x16x32_bf16 v[44:47], a[188:191], v[84:87], v[44:47]\n"
"v_mov_b32 v33, v32\n"
"v_mfma_f32_16x16x32_bf16 v[60:63], a[192:195], v[84:87], v[60:63]\n"
"s_nop 0\n"
"v_permlane16_swap_b32 v32, v33\n"
"v_mfma_f32_16x16x32_bf16 v[68:71], a[196:199], v[84:87], v[68:71]\n"
"v_mul_f32 v8, 0xbb000000, v32\n"
"s_waitcnt lgkmcnt(4)\n"
"v_mfma_f32_16x16x32_bf16 v[40:43], a[224:227], v[88:91], v[40:43]\n"
"ds_read_b128 v[80:83], v7 offset:32768\n"
"v_mul_f32 v34, v8, v8\n"
"v_mfma_f32_16x16x32_bf16 v[48:51], a[228:231], v[88:91], v[48:51]\n"
"ds_read_b128 v[84:87], v7 offset:49152\n"
"v_fma_f32 v33, v33, s37, -v34\n"
"v_mfma_f32_16x16x32_bf16 v[64:67], a[232:235], v[88:91], v[64:67]\n"
"v_max_f32 v33, 0, v33\n"
"v_mfma_f32_16x16x32_bf16 v[44:47], a[224:227], v[92:95], v[44:47]\n"
"v_add_f32 v33, 0x3727c5ac, v33\n"
"v_mfma_f32_16x16x32_bf16 v[60:63], a[228:231], v[92:95], v[60:63]\n"
"v_rsq_f32 v33, v33\n"
"v_mfma_f32_16x16x32_bf16 v[68:71], a[232:235], v[92:95], v[68:71]\n"
"v_add_f32 v36, v36, v38\n"
"s_waitcnt lgkmcnt(2)\n"
"v_mfma_f32_16x16x32_bf16 v[40:43], v[160:163], v[72:75], v[40:43]\n"
"ds_read_b128 v[88:91], v4 offset:34816\n"
"v_add_f32 v37, v37, v39\n"
"v_mfma_f32_16x16x32_bf16 v[48:51], v[164:167], v[72:75], v[48:51]\n"
"ds_read_b128 v[92:95], v4 offset:51200\n"
"v_permlane16_swap_b32 v36, v37\n"
"v_mfma_f32_16x16x32_bf16 v[64:67], v[168:171], v[72:75], v[64:67]\n"
"ds_read_b64_tr_b16 a[168:169], v123 offset:14336\n"
"ds_read_b64_tr_b16 a[170:171], v123 offset:15360\n"
"v_add_f32 v36, v36, v37\n"
"v_mfma_f32_16x16x32_bf16 v[44:47], v[160:163], v[76:79], v[44:47]\n"
"v_mov_b32 v37, v36\n"
"v_mfma_f32_16x16x32_bf16 v[60:63], v[164:167], v[76:79], v[60:63]\n"
"s_nop 0\n"
"v_permlane32_swap_b32 v36, v37\n"
"v_mfma_f32_16x16x32_bf16 v[68:71], v[168:171], v[76:79], v[68:71]\n"
"v_add_f32 v36, v36, v37\n"
"s_waitcnt lgkmcnt(4)\n"
"v_mfma_f32_16x16x32_bf16 v[40:43], v[208:211], v[80:83], v[40:43]\n"
"ds_read_b128 v[72:75], v5 offset:33792\n"
"v_mov_b32 v37, v36\n"
"v_mfma_f32_16x16x32_bf16 v[48:51], v[212:215], v[80:83], v[48:51]\n"
"ds_read_b128 v[76:79], v5 offset:50176\n"
"v_permlane16_swap_b32 v36, v37\n"
"v_mfma_f32_16x16x32_bf16 v[64:67], v[216:219], v[80:83], v[64:67]\n"
"v_mul_f32 v9, 0xbb000000, v36\n"
"v_mfma_f32_16x16x32_bf16 v[44:47], v[208:211], v[84:87], v[44:47]\n"
"ds_read_b64_tr_b16 a[172:173], v122 offset:14336\n"
"ds_read_b64_tr_b16 a[174:175], v122 offset:15360\n"
"v_mul_f32 v38, v9, v9\n"
"v_mfma_f32_16x16x32_bf16 v[60:63], v[212:215], v[84:87], v[60:63]\n"
"v_fma_f32 v37, v37, s37, -v38\n"
"v_mfma_f32_16x16x32_bf16 v[68:71], v[216:219], v[84:87], v[68:71]\n"
"v_max_f32 v37, 0, v37\n"
"s_waitcnt lgkmcnt(6)\n"
"v_mfma_f32_16x16x32_bf16 v[40:43], a[200:203], v[88:91], v[40:43]\n"
"ds_read_b128 v[80:83], v6 offset:33792\n"
"v_add_f32 v37, 0x3727c5ac, v37\n"
"v_mfma_f32_16x16x32_bf16 v[48:51], a[204:207], v[88:91], v[48:51]\n"
"ds_read_b128 v[84:87], v6 offset:50176\n"
"v_rsq_f32 v37, v37\n"
"v_mfma_f32_16x16x32_bf16 v[64:67], a[208:211], v[88:91], v[64:67]\n"
"s_cmp_lt_u32 s7, 2\n"
"s_cselect_b64 vcc, -1, 0\n"
"v_cndmask_b32 v33, v37, v33, vcc\n"
"v_mfma_f32_16x16x32_bf16 v[44:47], a[200:203], v[92:95], v[44:47]\n"
"ds_bpermute_b32 v126, v12, v33\n"
"v_mfma_f32_16x16x32_bf16 v[60:63], a[204:207], v[92:95], v[60:63]\n"
"v_mfma_f32_16x16x32_bf16 v[68:71], a[208:211], v[92:95], v[68:71]\n"
"s_waitcnt lgkmcnt(5)\n"
"v_mfma_f32_16x16x32_bf16 v[40:43], a[236:239], v[72:75], v[40:43]\n"
"ds_read_b128 v[88:91], v7 offset:33792\n"
"v_mfma_f32_16x16x32_bf16 v[48:51], a[240:243], v[72:75], v[48:51]\n"
"ds_read_b128 v[92:95], v7 offset:50176\n"
"v_mfma_f32_16x16x32_bf16 v[64:67], a[244:247], v[72:75], v[64:67]\n"
"v_mfma_f32_16x16x32_bf16 v[44:47], a[236:239], v[76:79], v[44:47]\n"
"v_mfma_f32_16x16x32_bf16 v[60:63], a[240:243], v[76:79], v[60:63]\n"
"v_mfma_f32_16x16x32_bf16 v[68:71], a[244:247], v[76:79], v[68:71]\n"
"s_waitcnt lgkmcnt(3)\n"
"v_mfma_f32_16x16x32_bf16 v[40:43], v[172:175], v[80:83], v[40:43]\n"
"ds_read_b128 v[72:75], v4 offset:35840\n"
"v_mfma_f32_16x16x32_bf16 v[48:51], v[176:179], v[80:83], v[48:51]\n"
"ds_read_b128 v[76:79], v4 offset:52224\n"
"v_add_u32 v141, s73, v142\n"
"ds_write_b128 v141, v[20:23] offset:16384\n"
"ds_write_b128 v141, v[24:27] offset:17408\n"
"ds_write_b128 v141, v[56:59] offset:18432\n"
"v_mfma_f32_16x16x32_bf16 v[64:67], v[180:183], v[80:83], v[64:67]\n"
"buffer_load_dwordx4 v[0:3], v142, s[8:11], s85 offen sc1\n"
"v_mfma_f32_16x16x32_bf16 v[44:47], v[172:175], v[84:87], v[44:47]\n"
"v_mfma_f32_16x16x32_bf16 v[60:63], v[176:179], v[84:87], v[60:63]\n"
"v_mfma_f32_16x16x32_bf16 v[68:71], v[180:183], v[84:87], v[68:71]\n"
"buffer_load_dwordx4 v[16:19], v142, s[8:11], s85 offen offset:1024 sc1\n"
"s_waitcnt lgkmcnt(5)\n"
"v_mfma_f32_16x16x32_bf16 v[40:43], v[220:223], v[88:91], v[40:43]\n"
"ds_read_b128 v[80:83], v5 offset:34816\n"
"v_mfma_f32_16x16x32_bf16 v[48:51], v[224:227], v[88:91], v[48:51]\n"
"ds_read_b128 v[84:87], v5 offset:51200\n"
"v_mfma_f32_16x16x32_bf16 v[64:67], v[228:231], v[88:91], v[64:67]\n"
"v_mfma_f32_16x16x32_bf16 v[44:47], v[220:223], v[92:95], v[44:47]\n"
"buffer_load_dwordx4 v[28:31], v142, s[8:11], s85 offen offset:2048 sc1\n"
"v_mfma_f32_16x16x32_bf16 v[60:63], v[224:227], v[92:95], v[60:63]\n"
"v_mfma_f32_16x16x32_bf16 v[68:71], v[228:231], v[92:95], v[68:71]\n"
"s_waitcnt lgkmcnt(5)\n"
"v_mfma_f32_16x16x32_bf16 v[40:43], a[212:215], v[72:75], v[40:43]\n"
"ds_read_b128 v[88:91], v6 offset:34816\n"
"v_mfma_f32_16x16x32_bf16 v[48:51], a[216:219], v[72:75], v[48:51]\n"
"ds_read_b128 v[92:95], v6 offset:51200\n"
"buffer_load_dwordx4 v[96:99], v142, s[8:11], s85 offen offset:3072 sc1\n"
"v_mfma_f32_16x16x32_bf16 v[64:67], a[220:223], v[72:75], v[64:67]\n"
"v_mfma_f32_16x16x32_bf16 v[44:47], a[212:215], v[76:79], v[44:47]\n"
"v_mfma_f32_16x16x32_bf16 v[60:63], a[216:219], v[76:79], v[60:63]\n"
"buffer_load_dwordx4 v[100:103], v142, s[8:11], s88 offen sc1\n"
"v_mfma_f32_16x16x32_bf16 v[68:71], a[220:223], v[76:79], v[68:71]\n"
"s_waitcnt lgkmcnt(2)\n"
"v_mfma_f32_16x16x32_bf16 v[40:43], a[248:251], v[80:83], v[40:43]\n"
"ds_read_b128 v[72:75], v7 offset:34816\n"
"v_mfma_f32_16x16x32_bf16 v[48:51], a[252:255], v[80:83], v[48:51]\n"
"ds_read_b128 v[76:79], v7 offset:51200\n"
"v_mfma_f32_16x16x32_bf16 v[64:67], v[144:147], v[80:83], v[64:67]\n"
"buffer_load_dwordx4 v[104:107], v142, s[8:11], s88 offen offset:1024 sc1\n"
"v_mfma_f32_16x16x32_bf16 v[44:47], a[248:251], v[84:87], v[44:47]\n"
"v_mfma_f32_16x16x32_bf16 v[60:63], a[252:255], v[84:87], v[60:63]\n"
"v_mfma_f32_16x16x32_bf16 v[68:71], v[144:147], v[84:87], v[68:71]\n"
"s_waitcnt lgkmcnt(2)\n"
"v_mfma_f32_16x16x32_bf16 v[40:43], v[184:187], v[88:91], v[40:43]\n"
"ds_read_b128 v[80:83], v5 offset:35840\n"
"buffer_load_dwordx4 v[108:111], v142, s[8:11], s88 offen offset:2048 sc1\n"
"v_mfma_f32_16x16x32_bf16 v[48:51], v[188:191], v[88:91], v[48:51]\n"
"ds_read_b128 v[84:87], v5 offset:52224\n"
"v_mfma_f32_16x16x32_bf16 v[64:67], v[192:195], v[88:91], v[64:67]\n"
"v_mfma_f32_16x16x32_bf16 v[44:47], v[184:187], v[92:95], v[44:47]\n"
"v_mfma_f32_16x16x32_bf16 v[60:63], v[188:191], v[92:95], v[60:63]\n"
"buffer_load_dwordx4 v[112:115], v142, s[8:11], s88 offen offset:3072 sc1\n"
"v_mfma_f32_16x16x32_bf16 v[68:71], v[192:195], v[92:95], v[68:71]\n"
"s_waitcnt lgkmcnt(2)\n"
"v_mfma_f32_16x16x32_bf16 v[40:43], v[232:235], v[72:75], v[40:43]\n"
"ds_read_b128 v[88:91], v6 offset:35840\n"
"v_mfma_f32_16x16x32_bf16 v[48:51], v[236:239], v[72:75], v[48:51]\n"
"ds_read_b128 v[92:95], v6 offset:52224\n"
"buffer_load_dwordx4 v[116:119], v142, s[8:11], s89 offen sc1\n"
"v_mfma_f32_16x16x32_bf16 v[64:67], v[240:243], v[72:75], v[64:67]\n"
"v_mfma_f32_16x16x32_bf16 v[44:47], v[232:235], v[76:79], v[44:47]\n"
"v_mfma_f32_16x16x32_bf16 v[60:63], v[236:239], v[76:79], v[60:63]\n"
"v_mfma_f32_16x16x32_bf16 v[68:71], v[240:243], v[76:79], v[68:71]\n"
"buffer_load_dwordx4 v[20:23], v142, s[8:11], s89 offen offset:1024 sc1\n"
"s_waitcnt lgkmcnt(2)\n"
"v_mfma_f32_16x16x32_bf16 v[40:43], v[148:151], v[80:83], v[40:43]\n"
"ds_read_b128 v[72:75], v7 offset:35840\n"
"v_mfma_f32_16x16x32_bf16 v[48:51], v[152:155], v[80:83], v[48:51]\n"
"ds_read_b128 v[76:79], v7 offset:52224\n"
"v_mfma_f32_16x16x32_bf16 v[64:67], v[156:159], v[80:83], v[64:67]\n"
"v_mfma_f32_16x16x32_bf16 v[44:47], v[148:151], v[84:87], v[44:47]\n"
"buffer_load_dwordx4 v[24:27], v142, s[8:11], s89 offen offset:2048 sc1\n"
"v_mfma_f32_16x16x32_bf16 v[60:63], v[152:155], v[84:87], v[60:63]\n"
"v_mfma_f32_16x16x32_bf16 v[68:71], v[156:159], v[84:87], v[68:71]\n"
"s_waitcnt lgkmcnt(2)\n"
"v_mfma_f32_16x16x32_bf16 v[40:43], v[196:199], v[88:91], v[40:43]\n"
"buffer_load_dwordx4 v[56:59], v142, s[8:11], s89 offen offset:3072 sc1\n"
"v_mfma_f32_16x16x32_bf16 v[48:51], v[200:203], v[88:91], v[48:51]\n"
"ds_read_b128 v[80:83], v10\n"
"v_mfma_f32_16x16x32_bf16 v[64:67], v[204:207], v[88:91], v[64:67]\n"
"v_mfma_f32_16x16x32_bf16 v[44:47], v[196:199], v[92:95], v[44:47]\n"
"ds_read_b128 v[84:87], v10 offset:64\n"
"v_mfma_f32_16x16x32_bf16 v[60:63], v[200:203], v[92:95], v[60:63]\n"
"buffer_load_dwordx4 v[32:35], v142, s[8:11], s90 offen sc1\n"
"v_mfma_f32_16x16x32_bf16 v[68:71], v[204:207], v[92:95], v[68:71]\n"
"s_waitcnt lgkmcnt(2)\n"
"v_mfma_f32_16x16x32_bf16 v[40:43], v[244:247], v[72:75], v[40:43]\n"
"ds_read_b128 v[88:91], v10 offset:128\n"
"v_mfma_f32_16x16x32_bf16 v[48:51], v[248:251], v[72:75], v[48:51]\n"
"v_mfma_f32_16x16x32_bf16 v[64:67], v[252:255], v[72:75], v[64:67]\n"
"buffer_load_dwordx4 v[36:39], v142, s[8:11], s90 offen offset:1024 sc1\n"
"v_mfma_f32_16x16x32_bf16 v[44:47], v[244:247], v[76:79], v[44:47]\n"
"v_mfma_f32_16x16x32_bf16 v[60:63], v[248:251], v[76:79], v[60:63]\n"
"v_mfma_f32_16x16x32_bf16 v[68:71], v[252:255], v[76:79], v[68:71]\n"
"s_nop 5\n"
"v_cmp_u_f32 vcc, v40, v44\n"
"s_cbranch_vccnz Lslowx01\n"
"Lbackx01:\n"
"s_waitcnt lgkmcnt(2)\n"
"v_pk_fma_f32 v[40:41], v[80:81], v[8:9], v[40:41] op_sel:[0,0,0] op_sel_hi:[1,0,1]\n"
"v_pk_fma_f32 v[42:43], v[82:83], v[8:9], v[42:43] op_sel:[0,0,0] op_sel_hi:[1,0,1]\n"
"v_pk_fma_f32 v[44:45], v[80:81], v[8:9], v[44:45] op_sel:[0,1,0] op_sel_hi:[1,1,1]\n"
"v_pk_fma_f32 v[46:47], v[82:83], v[8:9], v[46:47] op_sel:[0,1,0] op_sel_hi:[1,1,1]\n"
"v_cvt_pk_bf16_f32 v92, v40, v41\n"
"v_cvt_pk_bf16_f32 v93, v42, v43\n"
"v_cvt_pk_bf16_f32 v94, v44, v45\n"
"v_cvt_pk_bf16_f32 v95, v46, v47\n"
"v_pk_max_i16 v92, v92, 0\n"
"v_pk_max_i16 v94, v94, 0\n"
"v_pk_max_i16 v93, v93, 0\n"
"v_pk_max_i16 v95, v95, 0\n"
"s_add_u32 s53, s33, 0x16000\n"
"v_permlane32_swap_b32 v92, v94\n"
"v_permlane32_swap_b32 v93, v95\n"
"v_xor_b32 v92, v14, v92\n"
"buffer_store_dwordx4 v[92:95], v13, s[8:11], s53 offen sc1\n"
"s_waitcnt lgkmcnt(1)\n"
"v_pk_fma_f32 v[48:49], v[84:85], v[8:9], v[48:49] op_sel:[0,0,0] op_sel_hi:[1,0,1]\n"
"v_pk_fma_f32 v[50:51], v[86:87], v[8:9], v[50:51] op_sel:[0,0,0] op_sel_hi:[1,0,1]\n"
"v_pk_fma_f32 v[60:61], v[84:85], v[8:9], v[60:61] op_sel:[0,1,0] op_sel_hi:[1,1,1]\n"
"v_pk_fma_f32 v[62:63], v[86:87], v[8:9], v[62:63] op_sel:[0,1,0] op_sel_hi:[1,1,1]\n"
"v_cvt_pk_bf16_f32 v72, v48, v49\n"
"v_cvt_pk_bf16_f32 v73, v50, v51\n"
"v_cvt_pk_bf16_f32 v74, v60, v61\n"
"v_cvt_pk_bf16_f32 v75, v62, v63\n"
"v_pk_max_i16 v72, v72, 0\n"
"v_pk_max_i16 v74, v74, 0\n"
"v_pk_max_i16 v73, v73, 0\n"
"v_pk_max_i16 v75, v75, 0\n"
"s_add_u32 s53, s33, 0x18000\n"
"v_permlane32_swap_b32 v72, v74\n"
"v_permlane32_swap_b32 v73, v75\n"
"v_xor_b32 v72, v14, v72\n"
"buffer_store_dwordx4 v[72:75], v13, s[8:11], s53 offen sc1\n"
"s_waitcnt lgkmcnt(0)\n"
"v_pk_fma_f32 v[64:65], v[88:89], v[8:9], v[64:65] op_sel:[0,0,0] op_sel_hi:[1,0,1]\n"
"v_pk_fma_f32 v[66:67], v[90:91], v[8:9], v[66:67] op_sel:[0,0,0] op_sel_hi:[1,0,1]\n"
"v_pk_fma_f32 v[68:69], v[88:89], v[8:9], v[68:69] op_sel:[0,1,0] op_sel_hi:[1,1,1]\n"
"v_pk_fma_f32 v[70:71], v[90:91], v[8:9], v[70:71] op_sel:[0,1,0] op_sel_hi:[1,1,1]\n"
"v_cvt_pk_bf16_f32 v92, v64, v65\n"
"v_cvt_pk_bf16_f32 v93, v66, v67\n"
"v_cvt_pk_bf16_f32 v94, v68, v69\n"
"v_cvt_pk_bf16_f32 v95, v70, v71\n"
"v_pk_max_i16 v92, v92, 0\n"
"v_pk_max_i16 v94, v94, 0\n"
"v_pk_max_i16 v93, v93, 0\n"
"v_pk_max_i16 v95, v95, 0\n"
"s_add_u32 s53, s34, s35\n"
"v_permlane32_swap_b32 v92, v94\n"
"v_permlane32_swap_b32 v93, v95\n"
"v_xor_b32 v92, v14, v92\n"
"buffer_store_dwordx4 v[92:95], v13, s[8:11], s53 offen sc1\n"
"v_add_u32 v141, s73, v142\n"
"ds_write_b128 v141, v[4:7] offset:12288\n"
"ds_write_b128 v141, v[8:11] offset:13312\n"
"ds_write_b128 v141, v[12:15] offset:14336\n"
"s_lshl_b32 s53, s7, 13\n"
"v_add_u32 v125, 0x16000, v142\n"
"v_add_u32 v133, s53, v142\n"
"s_waitcnt vmcnt(16)\n"
"v_xor_b32 v0, v132, v0\n"
"s_waitcnt vmcnt(15)\n"
"v_xor_b32 v16, v132, v16\n"
"s_waitcnt vmcnt(14)\n"
"v_xor_b32 v28, v132, v28\n"
"s_waitcnt vmcnt(13)\n"
"v_xor_b32 v96, v132, v96\n"
"v_or3_b32 v134, v0, v1, v2\n"
"v_or3_b32 v134, v134, v3, v16\n"
"v_or3_b32 v134, v134, v17, v18\n"
"v_or3_b32 v134, v134, v19, v28\n"
"v_or3_b32 v134, v134, v29, v30\n"
"v_or3_b32 v134, v134, v31, v96\n"
"v_or3_b32 v134, v134, v97, v98\n"
"v_or3_b32 v134, v134, v99, v99\n"
"v_and_b32 v134, s38, v134\n"
"v_cmp_ne_u32 vcc, 0, v134\n"
"s_cbranch_vccnz Lslow8_h00\n"
"Lback8_h00:\n"
"v_mfma_f32_32x32x16_bf16 v[60:75], a[0:3], v[0:3], 0\n"
"buffer_load_dwordx4 v[40:43], v142, s[8:11], s90 offen offset:2048 sc1\n"
"v_mfma_f32_32x32x16_bf16 v[76:91], a[4:7], v[0:3], 0\n"
"v_mfma_f32_32x32x16_bf16 v[60:75], a[8:11], v[16:19], v[60:75]\n"
"buffer_load_dwordx4 v[44:47], v142, s[8:11], s90 offen offset:3072 sc1\n"
"v_mfma_f32_32x32x16_bf16 v[76:91], a[12:15], v[16:19], v[76:91]\n"
"s_waitcnt vmcnt(14)\n"
"v_xor_b32 v100, v132, v100\n"
"s_waitcnt vmcnt(13)\n"
"v_xor_b32 v104, v132, v104\n"
"s_waitcnt vmcnt(12)\n"
"v_xor_b32 v108, v132, v108\n"
"s_waitcnt vmcnt(11)\n"
"v_xor_b32 v112, v132, v112\n"
"v_mfma_f32_32x32x16_bf16 v[60:75], a[16:19], v[28:31], v[60:75]\n"
"buffer_load_dwordx4 v[48:51], v142, s[8:11], s91 offen sc1\n"
"v_or3_b32 v134, v100, v101, v102\n"
"v_or3_b32 v134, v134, v103, v104\n"
"v_or3_b32 v134, v134, v105, v106\n"
"v_mfma_f32_32x32x16_bf16 v[76:91], a[20:23], v[28:31], v[76:91]\n"
"v_or3_b32 v134, v134, v107, v108\n"
"v_or3_b32 v134, v134, v109, v110\n"
"v_or3_b32 v134, v134, v111, v112\n"
"v_or3_b32 v134, v134, v113, v114\n"
"v_mfma_f32_32x32x16_bf16 v[60:75], a[24:27], v[96:99], v[60:75]\n"
"buffer_load_dwordx4 v[92:95], v142, s[8:11], s91 offen offset:1024 sc1\n"
"v_or3_b32 v134, v134, v115, v115\n"
"v_and_b32 v134, s38, v134\n"
"v_cmp_ne_u32 s[66:67], 0, v134\n"
"v_mfma_f32_32x32x16_bf16 v[76:91], a[28:31], v[96:99], v[76:91]\n"
"s_cmp_lg_u64 s[66:67], 0\n"
"s_cbranch_scc1 Lslow9_h01\n"
"Lback9_h01:\n"
"v_mfma_f32_32x32x16_bf16 v[60:75], a[32:35], v[100:103], v[60:75]\n"
"buffer_load_dwordx4 v[120:123], v142, s[8:11], s91 offen offset:2048 sc1\n"
"v_mfma_f32_32x32x16_bf16 v[76:91], a[36:39], v[100:103], v[76:91]\n"
"v_mfma_f32_32x32x16_bf16 v[60:75], a[40:43], v[104:107], v[60:75]\n"
"buffer_load_dwordx4 v[4:7], v142, s[8:11], s91 offen offset:3072 sc1\n"
"v_mfma_f32_32x32x16_bf16 v[76:91], a[44:47], v[104:107], v[76:91]\n"
"s_waitcnt vmcnt(14)\n"
"v_xor_b32 v116, v132, v116\n"
"s_waitcnt vmcnt(13)\n"
"v_xor_b32 v20, v132, v20\n"
"s_waitcnt vmcnt(12)\n"
"v_xor_b32 v24, v132, v24\n"
"s_waitcnt vmcnt(11)\n"
"v_xor_b32 v56, v132, v56\n"
"v_mfma_f32_32x32x16_bf16 v[60:75], a[48:51], v[108:111], v[60:75]\n"
"buffer_load_dwordx4 v[8:11], v142, s[8:11], s92 offen sc1\n"
"v_or3_b32 v134, v116, v117, v118\n"
"v_or3_b32 v134, v134, v119, v20\n"
"v_or3_b32 v134, v134, v21, v22\n"
"v_mfma_f32_32x32x16_bf16 v[76:91], a[52:55], v[108:111], v[76:91]\n"
"v_or3_b32 v134, v134, v23, v24\n"
"v_or3_b32 v134, v134, v25, v26\n"
"v_or3_b32 v134, v134, v27, v56\n"
"v_or3_b32 v134, v134, v57, v58\n"
"v_mfma_f32_32x32x16_bf16 v[60:75], a[56:59], v[112:115], v[60:75]\n"
"buffer_load_dwordx4 v[12:15], v142, s[8:11], s92 offen offset:1024 sc1\n"
"v_or3_b32 v134, v134, v59, v59\n"
"v_and_b32 v134, s38, v134\n"
"v_cmp_ne_u32 s[66:67], 0, v134\n"
"v_mfma_f32_32x32x16_bf16 v[76:91], a[60:63], v[112:115], v[76:91]\n"
"s_cmp_lg_u64 s[66:67], 0\n"
"s_cbranch_scc1 Lslow10_h02\n"
"Lback10_h02:\n"
"v_mfma_f32_32x32x16_bf16 v[60:75], a[64:67], v[116:119], v[60:75]\n"
"v_mfma_f32_32x32x16_bf16 v[76:91], a[68:71], v[116:119], v[76:91]\n"
"v_mfma_f32_32x32x16_bf16 v[60:75], a[72:75], v[20:23], v[60:75]\n"
"v_mfma_f32_32x32x16_bf16 v[76:91], a[76:79], v[20:23], v[76:91]\n"
"s_waitcnt vmcnt(12)\n"
"v_xor_b32 v32, v132, v32\n"
"s_waitcnt vmcnt(11)\n"
"v_xor_b32 v36, v132, v36\n"
"s_waitcnt vmcnt(7)\n"
"v_xor_b32 v40, v132, v40\n"
"s_waitcnt vmcnt(6)\n"
"v_xor_b32 v44, v132, v44\n"
"v_mfma_f32_32x32x16_bf16 v[60:75], a[80:83], v[24:27], v[60:75]\n"
"v_or3_b32 v134, v32, v33, v34\n"
"v_or3_b32 v134, v134, v35, v36\n"
"v_or3_b32 v134, v134, v37, v38\n"
"v_mfma_f32_32x32x16_bf16 v[76:91], a[84:87], v[24:27], v[76:91]\n"
"v_or3_b32 v134, v134, v39, v40\n"
"v_or3_b32 v134, v134, v41, v42\n"
"v_or3_b32 v134, v134, v43, v44\n"
"v_or3_b32 v134, v134, v45, v46\n"
"v_mfma_f32_32x32x16_bf16 v[60:75], a[88:91], v[56:59], v[60:75]\n"
"v_or3_b32 v134, v134, v47, v47\n"
"v_and_b32 v134, s38, v134\n"
"v_cmp_ne_u32 s[66:67], 0, v134\n"
"v_mfma_f32_32x32x16_bf16 v[76:91], a[92:95], v[56:59], v[76:91]\n"
"s_cmp_lg_u64 s[66:67], 0\n"
"s_cbranch_scc1 Lslow11_h03\n"
"Lback11_h03:\n"
"v_mfma_f32_32x32x16_bf16 v[60:75], a[96:99], v[32:35], v[60:75]\n"
"buffer_load_dwordx4 v[0:3], v125, s[8:11], s85 offen sc1\n"
"v_mfma_f32_32x32x16_bf16 v[76:91], a[100:103], v[32:35], v[76:91]\n"
"v_mfma_f32_32x32x16_bf16 v[60:75], a[104:107], v[36:39], v[60:75]\n"
"buffer_load_dwordx4 v[16:19], v125, s[8:11], s85 offen offset:1024 sc1\n"
"v_mfma_f32_32x32x16_bf16 v[76:91], a[108:111], v[36:39], v[76:91]\n"
"s_waitcnt vmcnt(7)\n"
"v_xor_b32 v48, v132, v48\n"
"s_waitcnt vmcnt(6)\n"
"v_xor_b32 v92, v132, v92\n"
"s_waitcnt vmcnt(5)\n"
"v_xor_b32 v120, v132, v120\n"
"s_waitcnt vmcnt(4)\n"
"v_xor_b32 v4, v132, v4\n"
"s_waitcnt vmcnt(3)\n"
"v_xor_b32 v8, v132, v8\n"
"v_mfma_f32_32x32x16_bf16 v[60:75], a[112:115], v[40:43], v[60:75]\n"
"buffer_load_dwordx4 v[28:31], v125, s[8:11], s85 offen offset:2048 sc1\n"
"s_waitcnt vmcnt(3)\n"
"v_xor_b32 v12, v132, v12\n"
"v_or3_b32 v134, v48, v49, v50\n"
"v_or3_b32 v134, v134, v51, v92\n"
"v_or3_b32 v134, v134, v93, v94\n"
"v_or3_b32 v134, v134, v95, v120\n"
"v_mfma_f32_32x32x16_bf16 v[76:91], a[116:119], v[40:43], v[76:91]\n"
"v_or3_b32 v134, v134, v121, v122\n"
"v_or3_b32 v134, v134, v123, v4\n"
"v_or3_b32 v134, v134, v5, v6\n"
"v_or3_b32 v134, v134, v7, v8\n"
"v_or3_b32 v134, v134, v9, v10\n"
"v_mfma_f32_32x32x16_bf16 v[60:75], a[120:123], v[44:47], v[60:75]\n"
"buffer_load_dwordx4 v[96:99], v125, s[8:11], s85 offen offset:3072 sc1\n"
"v_or3_b32 v134, v134, v11, v12\n"
"v_or3_b32 v134, v134, v13, v14\n"
"v_or3_b32 v134, v134, v15, v15\n"
"v_and_b32 v134, s38, v134\n"
"v_cmp_ne_u32 s[66:67], 0, v134\n"
"v_mfma_f32_32x32x16_bf16 v[76:91], a[124:127], v[44:47], v[76:91]\n"
"s_cmp_lg_u64 s[66:67], 0\n"
"s_cbranch_scc1 Lslow12_h04\n"
"Lback12_h04:\n"
"v_mfma_f32_32x32x16_bf16 v[60:75], a[128:131], v[48:51], v[60:75]\n"
"buffer_load_dwordx4 v[100:103], v125, s[8:11], s88 offen sc1\n"
"v_mfma_f32_32x32x16_bf16 v[60:75], a[136:139], v[92:95], v[60:75]\n"
"v_mfma_f32_32x32x16_bf16 v[60:75], a[144:147], v[120:123], v[60:75]\n"
"buffer_load_dwordx4 v[104:107], v125, s[8:11], s88 offen offset:1024 sc1\n"
"v_mfma_f32_32x32x16_bf16 v[60:75], a[152:155], v[4:7], v[60:75]\n"
"v_mfma_f32_32x32x16_bf16 v[60:75], a[160:163], v[8:11], v[60:75]\n"
"buffer_load_dwordx4 v[108:111], v125, s[8:11], s88 offen offset:2048 sc1\n"
"v_mfma_f32_32x32x16_bf16 v[60:75], a[168:171], v[12:15], v[60:75]\n"
"v_mfma_f32_32x32x16_bf16 v[76:91], a[132:135], v[48:51], v[76:91]\n"
"buffer_load_dwordx4 v[40:43], v125, s[8:11], s88 offen offset:3072 sc1\n"
"v_mfma_f32_32x32x16_bf16 v[76:91], a[140:143], v[92:95], v[76:91]\n"
"s_waitcnt vmcnt(7)\n"
"v_xor_b32 v0, v132, v0\n"
"s_waitcnt vmcnt(6)\n"
"v_xor_b32 v16, v132, v16\n"
"s_waitcnt vmcnt(5)\n"
"v_xor_b32 v28, v132, v28\n"
"s_waitcnt vmcnt(4)\n"
"v_xor_b32 v96, v132, v96\n"
"v_mfma_f32_32x32x16_bf16 v[76:91], a[148:151], v[120:123], v[76:91]\n"
"ds_write_b128 v133, v[60:63]\n"
"buffer_load_dwordx4 v[44:47], v125, s[8:11], s89 offen sc1\n"
"v_or3_b32 v134, v0, v1, v2\n"
"v_or3_b32 v134, v134, v3, v16\n"
"v_or3_b32 v134, v134, v17, v18\n"
"v_mfma_f32_32x32x16_bf16 v[76:91], a[156:159], v[4:7], v[76:91]\n"
"ds_write_b128 v133, v[64:67] offset:1024\n"
"v_or3_b32 v134, v134, v19, v28\n"
"v_or3_b32 v134, v134, v29, v30\n"
"v_or3_b32 v134, v134, v31, v96\n"
"v_or3_b32 v134, v134, v97, v98\n"
"v_mfma_f32_32x32x16_bf16 v[76:91], a[164:167], v[8:11], v[76:91]\n"
"ds_write_b128 v133, v[68:71] offset:2048\n"
"buffer_load_dwordx4 v[48:51], v125, s[8:11], s89 offen offset:1024 sc1\n"
"v_or3_b32 v134, v134, v99, v99\n"
"v_and_b32 v134, s38, v134\n"
"v_cmp_ne_u32 s[66:67], 0, v134\n"
"v_mfma_f32_32x32x16_bf16 v[76:91], a[172:175], v[12:15], v[76:91]\n"
"ds_write_b128 v133, v[72:75] offset:3072\n"
"s_cmp_lg_u64 s[66:67], 0\n"
"s_cbranch_scc1 Lslow13_h05\n"
"Lback13_h05:\n"
"v_mfma_f32_32x32x16_bf16 v[60:75], a[0:3], v[0:3], 0\n"
"s_nop 7\n"
"ds_write_b128 v133, v[76:79] offset:4096\n"
"buffer_load_dwordx4 v[92:95], v125, s[8:11], s89 offen offset:2048 sc1\n"
"v_mfma_f32_32x32x16_bf16 v[60:75], a[8:11], v[16:19], v[60:75]\n"
"ds_write_b128 v133, v[80:83] offset:5120\n"
"buffer_load_dwordx4 v[120:123], v125, s[8:11], s89 offen offset:3072 sc1\n"
"v_mfma_f32_32x32x16_bf16 v[60:75], a[16:19], v[28:31], v[60:75]\n"
"ds_write_b128 v133, v[84:87] offset:6144\n"
"buffer_load_dwordx4 v[4:7], v125, s[8:11], s90 offen sc1\n"
"v_mfma_f32_32x32x16_bf16 v[60:75], a[24:27], v[96:99], v[60:75]\n"
"ds_write_b128 v133, v[88:91] offset:7168\n"
"buffer_load_dwordx4 v[8:11], v125, s[8:11], s90 offen offset:1024 sc1\n"
"s_waitcnt vmcnt(9)\n"
"v_xor_b32 v100, v132, v100\n"
"s_waitcnt vmcnt(8)\n"
"v_xor_b32 v104, v132, v104\n"
"s_waitcnt vmcnt(7)\n"
"v_xor_b32 v108, v132, v108\n"
"s_waitcnt vmcnt(6)\n"
"v_xor_b32 v40, v132, v40\n"
"v_mfma_f32_32x32x16_bf16 v[76:91], a[4:7], v[0:3], 0\n"
"buffer_load_dwordx4 v[12:15], v125, s[8:11], s90 offen offset:2048 sc1\n"
"v_or3_b32 v134, v100, v101, v102\n"
"v_or3_b32 v134, v134, v103, v104\n"
"v_or3_b32 v134, v134, v105, v106\n"
"v_add_u32 v141, s73, v142\n"
"ds_read_b128 v[20:23], v141 offset:16384\n"
"ds_read_b128 v[24:27], v141 offset:17408\n"
"ds_read_b128 v[56:59], v141 offset:18432\n"
"v_mfma_f32_32x32x16_bf16 v[76:91], a[12:15], v[16:19], v[76:91]\n"
"buffer_load_dwordx4 v[0:3], v125, s[8:11], s90 offen offset:3072 sc1\n"
"v_or3_b32 v134, v134, v107, v108\n"
"v_or3_b32 v134, v134, v109, v110\n"
"v_or3_b32 v134, v134, v111, v40\n"
"v_or3_b32 v134, v134, v41, v42\n"
"s_waitcnt lgkmcnt(0)\n"
"s_barrier\n"
"v_mfma_f32_32x32x16_bf16 v[76:91], a[20:23], v[28:31], v[76:91]\n"
"buffer_load_dwordx4 v[16:19], v125, s[8:11], s91 offen sc1\n"
"v_or3_b32 v134, v134, v43, v43\n"
"v_and_b32 v134, s38, v134\n"
"v_cmp_ne_u32 s[66:67], 0, v134\n"
"ds_read_b128 v[112:115], v135\n"
"ds_read_b128 v[116:119], v135 offset:1024\n"
"ds_read_b128 v[32:35], v135 offset:8192\n"
"ds_read_b128 v[36:39], v135 offset:9216\n"
"v_mfma_f32_32x32x16_bf16 v[76:91], a[28:31], v[96:99], v[76:91]\n"
"buffer_load_dwordx4 v[28:31], v125, s[8:11], s91 offen offset:1024 sc1\n"
"s_cmp_lg_u64 s[66:67], 0\n"
"s_cbranch_scc1 Lslow14_h06\n"
"Lback14_h06:\n"
"v_mfma_f32_32x32x16_bf16 v[60:75], a[32:35], v[100:103], v[60:75]\n"
"buffer_load_dwordx4 v[96:99], v125, s[8:11], s91 offen offset:2048 sc1\n"
"s_waitcnt lgkmcnt(1)\n"
"v_add_f32 v112, v112, v32\n"
"v_add_f32 v113, v113, v33\n"
"v_add_f32 v114, v114, v34\n"
"v_add_f32 v115, v115, v35\n"
"s_waitcnt lgkmcnt(0)\n"
"v_add_f32 v116, v116, v36\n"
"v_mfma_f32_32x32x16_bf16 v[76:91], a[36:39], v[100:103], v[76:91]\n"
"buffer_load_dwordx4 v[100:103], v125, s[8:11], s91 offen offset:3072 sc1\n"
"v_add_f32 v117, v117, v37\n"
"v_add_f32 v118, v118, v38\n"
"v_add_f32 v119, v119, v39\n"
"ds_read_b128 v[32:35], v135 offset:16384\n"
"ds_read_b128 v[36:39], v135 offset:17408\n"
"v_mfma_f32_32x32x16_bf16 v[60:75], a[40:43], v[104:107], v[60:75]\n"
"v_mfma_f32_32x32x16_bf16 v[76:91], a[44:47], v[104:107], v[76:91]\n"
"buffer_load_dwordx4 v[104:107], v125, s[8:11], s92 offen sc1\n"
"s_waitcnt vmcnt(12)\n"
"v_xor_b32 v44, v132, v44\n"
"s_waitcnt vmcnt(11)\n"
"v_xor_b32 v48, v132, v48\n"
"s_waitcnt vmcnt(10)\n"
"v_xor_b32 v92, v132, v92\n"
"s_waitcnt vmcnt(9)\n"
"v_xor_b32 v120, v132, v120\n"
"s_waitcnt lgkmcnt(1)\n"
"v_add_f32 v112, v112, v32\n"
"v_add_f32 v113, v113, v33\n"
"v_mfma_f32_32x32x16_bf16 v[60:75], a[48:51], v[108:111], v[60:75]\n"
"v_or3_b32 v134, v44, v45, v46\n"
"v_or3_b32 v134, v134, v47, v48\n"
"v_or3_b32 v134, v134, v49, v50\n"
"v_add_f32 v114, v114, v34\n"
"v_add_f32 v115, v115, v35\n"
"s_waitcnt lgkmcnt(0)\n"
"v_add_f32 v116, v116, v36\n"
"v_add_f32 v117, v117, v37\n"
"v_add_f32 v118, v118, v38\n"
"v_mfma_f32_32x32x16_bf16 v[76:91], a[52:55], v[108:111], v[76:91]\n"
"buffer_load_dwordx4 v[108:111], v125, s[8:11], s92 offen offset:1024 sc1\n"
"v_or3_b32 v134, v134, v51, v92\n"
"v_or3_b32 v134, v134, v93, v94\n"
"v_or3_b32 v134, v134, v95, v120\n"
"v_or3_b32 v134, v134, v121, v122\n"
"v_add_f32 v119, v119, v39\n"
"ds_read_b128 v[32:35], v135 offset:24576\n"
"ds_read_b128 v[36:39], v135 offset:25600\n"
"v_mfma_f32_32x32x16_bf16 v[60:75], a[56:59], v[40:43], v[60:75]\n"
"v_or3_b32 v134, v134, v123, v123\n"
"v_and_b32 v134, s38, v134\n"
"v_cmp_ne_u32 s[66:67], 0, v134\n"
"v_mfma_f32_32x32x16_bf16 v[76:91], a[60:63], v[40:43], v[76:91]\n"
"s_cmp_lg_u64 s[66:67], 0\n"
"s_cbranch_scc1 Lslow15_h07\n"
"Lback15_h07:\n"
"s_waitcnt lgkmcnt(1)\n"
"v_add_f32 v112, v112, v32\n"
"v_add_f32 v113, v113, v33\n"
"v_add_f32 v114, v114, v34\n"
"v_add_f32 v115, v115, v35\n"
"s_waitcnt lgkmcnt(0)\n"
"v_add_f32 v116, v116, v36\n"
"v_mfma_f32_32x32x16_bf16 v[60:75], a[64:67], v[44:47], v[60:75]\n"
"v_add_f32 v117, v117, v37\n"
"v_add_f32 v118, v118, v38\n"
"v_add_f32 v119, v119, v39\n"
"v_lshlrev_b32 v32, 16, v52\n"
"v_mfma_f32_32x32x16_bf16 v[76:91], a[68:71], v[44:47], v[76:91]\n"
"v_and_b32 v33, s61, v52\n"
"v_lshlrev_b32 v34, 16, v53\n"
"v_and_b32 v35, s61, v53\n"
"v_lshlrev_b32 v36, 16, v54\n"
"v_and_b32 v37, s61, v54\n"
"v_mfma_f32_32x32x16_bf16 v[60:75], a[72:75], v[48:51], v[60:75]\n"
"v_lshlrev_b32 v38, 16, v55\n"
"v_and_b32 v39, s61, v55\n"
"v_fma_f32 v32, v112, v127, v32\n"
"v_fma_f32 v33, v113, v127, v33\n"
"v_fma_f32 v34, v114, v127, v34\n"
"v_mfma_f32_32x32x16_bf16 v[76:91], a[76:79], v[48:51], v[76:91]\n"
"s_waitcnt vmcnt(9)\n"
"v_xor_b32 v4, v132, v4\n"
"s_waitcnt vmcnt(8)\n"
"v_xor_b32 v8, v132, v8\n"
"s_waitcnt vmcnt(7)\n"
"v_xor_b32 v12, v132, v12\n"
"s_waitcnt vmcnt(6)\n"
"v_xor_b32 v0, v132, v0\n"
"v_fma_f32 v35, v115, v127, v35\n"
"v_fma_f32 v36, v116, v127, v36\n"
"v_fma_f32 v37, v117, v127, v37\n"
"v_fma_f32 v38, v118, v127, v38\n"
"v_fma_f32 v39, v119, v127, v39\n"
"v_mfma_f32_32x32x16_bf16 v[60:75], a[80:83], v[92:95], v[60:75]\n"
"v_or3_b32 v134, v4, v5, v6\n"
"v_or3_b32 v134, v134, v7, v8\n"
"v_or3_b32 v134, v134, v9, v10\n"
"v_add_f32 v112, v32, v33\n"
"v_mul_f32 v113, v32, v32\n"
"v_add_f32 v114, v34, v35\n"
"v_mul_f32 v115, v33, v33\n"
"v_add_f32 v112, v112, v36\n"
"v_mfma_f32_32x32x16_bf16 v[76:91], a[84:87], v[92:95], v[76:91]\n"
"v_or3_b32 v134, v134, v11, v12\n"
"v_or3_b32 v134, v134, v13, v14\n"
"v_or3_b32 v134, v134, v15, v0\n"
"v_or3_b32 v134, v134, v1, v2\n"
"v_fmac_f32 v113, v34, v34\n"
"v_add_f32 v114, v114, v37\n"
"v_fmac_f32 v115, v35, v35\n"
"v_add_f32 v112, v112, v38\n"
"v_mfma_f32_32x32x16_bf16 v[60:75], a[88:91], v[120:123], v[60:75]\n"
"v_or3_b32 v134, v134, v3, v3\n"
"v_and_b32 v134, s38, v134\n"
"v_cmp_ne_u32 s[66:67], 0, v134\n"
"v_fmac_f32 v113, v36, v36\n"
"v_add_f32 v114, v114, v39\n"
"v_fmac_f32 v115, v37, v37\n"
"v_add_f32 v112, v112, v114\n"
"v_fmac_f32 v113, v38, v38\n"
"v_mfma_f32_32x32x16_bf16 v[76:91], a[92:95], v[120:123], v[76:91]\n"
"s_cmp_lg_u64 s[66:67], 0\n"
"s_cbranch_scc1 Lslow16_h08\n"
"Lback16_h08:\n"
"v_fmac_f32 v115, v39, v39\n"
"v_add_f32 v113, v113, v115\n"
"v_add_f32_dpp v114, v112, v112 row_ror:8 row_mask:0xf bank_mask:0xf\n"
"s_nop 0\n"
"v_add_f32_dpp v115, v113, v113 row_ror:8 row_mask:0xf bank_mask:0xf\n"
"s_nop 1\n"
"v_permlane16_swap_b32 v114, v115\n"
"v_mfma_f32_32x32x16_bf16 v[60:75], a[96:99], v[4:7], v[60:75]\n"
"v_add_f32 v114, v114, v115\n"
"v_mov_b32 v115, v114\n"
"s_nop 1\n"
"v_permlane32_swap_b32 v114, v115\n"
"v_add_f32 v112, v114, v115\n"
"v_mov_b32 v113, v112\n"
"v_mfma_f32_32x32x16_bf16 v[76:91], a[100:103], v[4:7], v[76:91]\n"
"v_mov_b32 v114, v112\n"
"s_nop 1\n"
"v_permlane16_swap_b32 v114, v113\n"
"v_min_u32 v112, -2, v112\n"
"v_min_u32 v113, -2, v113\n"
"s_add_u32 s53, s39, 0x40000\n"
"buffer_store_dwordx2 v[112:113], v137, s[8:11], s53 offen sc1\n"
"v_mfma_f32_32x32x16_bf16 v[60:75], a[104:107], v[8:11], v[60:75]\n"
"v_cvt_pk_bf16_f32 v52, v32, v33\n"
"v_cvt_pk_bf16_f32 v53, v34, v35\n"
"v_cvt_pk_bf16_f32 v54, v36, v37\n"
"v_cvt_pk_bf16_f32 v55, v38, v39\n"
"v_mfma_f32_32x32x16_bf16 v[76:91], a[108:111], v[8:11], v[76:91]\n"
"s_waitcnt vmcnt(6)\n"
"v_xor_b32 v16, v132, v16\n"
"s_waitcnt vmcnt(5)\n"
"v_xor_b32 v28, v132, v28\n"
"s_waitcnt vmcnt(4)\n"
"v_xor_b32 v96, v132, v96\n"
"s_waitcnt vmcnt(3)\n"
"v_xor_b32 v100, v132, v100\n"
"s_waitcnt vmcnt(2)\n"
"v_xor_b32 v104, v132, v104\n"
"v_mfma_f32_32x32x16_bf16 v[60:75], a[112:115], v[12:15], v[60:75]\n"
"s_waitcnt vmcnt(1)\n"
"v_xor_b32 v108, v132, v108\n"
"v_or3_b32 v134, v16, v17, v18\n"
"v_or3_b32 v134, v134, v19, v28\n"
"v_or3_b32 v134, v134, v29, v30\n"
"v_or3_b32 v134, v134, v31, v96\n"
"v_mfma_f32_32x32x16_bf16 v[76:91], a[116:119], v[12:15], v[76:91]\n"
"v_or3_b32 v134, v134, v97, v98\n"
"v_or3_b32 v134, v134, v99, v100\n"
"v_or3_b32 v134, v134, v101, v102\n"
"v_or3_b32 v134, v134, v103, v104\n"
"v_or3_b32 v134, v134, v105, v106\n"
"v_mfma_f32_32x32x16_bf16 v[60:75], a[120:123], v[0:3], v[60:75]\n"
"v_or3_b32 v134, v134, v107, v108\n"
"v_or3_b32 v134, v134, v109, v110\n"
"v_or3_b32 v134, v134, v111, v111\n"
"v_and_b32 v134, s38, v134\n"
"v_cmp_ne_u32 s[66:67], 0, v134\n"
"v_mfma_f32_32x32x16_bf16 v[76:91], a[124:127], v[0:3], v[76:91]\n"
"s_cmp_lg_u64 s[66:67], 0\n"
"s_cbranch_scc1 Lslow17_h09\n"
"Lback17_h09:\n"
"v_mfma_f32_32x32x16_bf16 v[60:75], a[128:131], v[16:19], v[60:75]\n"
"v_mfma_f32_32x32x16_bf16 v[60:75], a[136:139], v[28:31], v[60:75]\n"
"v_mfma_f32_32x32x16_bf16 v[60:75], a[144:147], v[96:99], v[60:75]\n"
"v_mfma_f32_32x32x16_bf16 v[60:75], a[152:155], v[100:103], v[60:75]\n"
"v_mfma_f32_32x32x16_bf16 v[60:75], a[160:163], v[104:107], v[60:75]\n"
"v_mfma_f32_32x32x16_bf16 v[60:75], a[168:171], v[108:111], v[60:75]\n"
"v_mfma_f32_32x32x16_bf16 v[76:91], a[132:135], v[16:19], v[76:91]\n"
"v_mfma_f32_32x32x16_bf16 v[76:91], a[140:143], v[28:31], v[76:91]\n"
"v_mfma_f32_32x32x16_bf16 v[76:91], a[148:151], v[96:99], v[76:91]\n"
"s_nop 7\n"
"s_nop 0\n"
"ds_write_b128 v133, v[60:63] offset:32768\n"
"v_mfma_f32_32x32x16_bf16 v[76:91], a[156:159], v[100:103], v[76:91]\n"
"ds_write_b128 v133, v[64:67] offset:33792\n"
"v_mfma_f32_32x32x16_bf16 v[76:91], a[164:167], v[104:107], v[76:91]\n"
"ds_write_b128 v133, v[68:71] offset:34816\n"
"v_mfma_f32_32x32x16_bf16 v[76:91], a[172:175], v[108:111], v[76:91]\n"
"ds_write_b128 v133, v[72:75] offset:35840\n"
"s_nop 7\n"
"s_nop 2\n"
"ds_write_b128 v133, v[76:79] offset:36864\n"
"ds_write_b128 v133, v[80:83] offset:37888\n"
"ds_write_b128 v133, v[84:87] offset:38912\n"
"ds_write_b128 v133, v[88:91] offset:39936\n"
"v_add_u32 v141, s73, v142\n"
"ds_read_b128 v[4:7], v141 offset:12288\n"
"ds_read_b128 v[8:11], v141 offset:13312\n"
"ds_read_b128 v[12:15], v141 offset:14336\n"
"s_waitcnt lgkmcnt(0)\n"
"s_barrier\n"
"ds_read_b128 v[28:31], v135 offset:32768\n"
"ds_read_b128 v[36:39], v135 offset:40960\n"
"ds_read_b128 v[32:35], v135 offset:33792\n"
"ds_read_b128 v[40:43], v135 offset:41984\n"
"ds_read_b128 v[44:47], v135 offset:49152\n"
"ds_read_b128 v[48:51], v135 offset:50176\n"
"ds_read_b128 v[0:3], v135 offset:57344\n"
"ds_read_b128 v[16:19], v135 offset:58368\n"
"v_lshlrev_b32 v60, 16, v56\n"
"v_and_b32 v61, s61, v56\n"
"v_lshlrev_b32 v62, 16, v57\n"
"v_and_b32 v63, s61, v57\n"
"v_lshlrev_b32 v64, 16, v58\n"
"v_and_b32 v65, s61, v58\n"
"v_lshlrev_b32 v66, 16, v59\n"
"v_and_b32 v67, s61, v59\n"
"s_waitcnt lgkmcnt(6)\n"
"v_pk_add_f32 v[28:29], v[28:29], v[36:37]\n"
"v_pk_add_f32 v[30:31], v[30:31], v[38:39]\n"
"s_waitcnt lgkmcnt(4)\n"
"v_pk_add_f32 v[32:33], v[32:33], v[40:41]\n"
"s_add_u32 s59, s74, 0x40000\n"
"buffer_load_dwordx2 v[130:131], v136, s[8:11], s59 offen sc1\n"
"v_pk_add_f32 v[34:35], v[34:35], v[42:43]\n"
"s_waitcnt lgkmcnt(1)\n"
"v_pk_add_f32 v[44:45], v[44:45], v[0:1]\n"
"v_pk_add_f32 v[46:47], v[46:47], v[2:3]\n"
"s_waitcnt lgkmcnt(0)\n"
"v_pk_add_f32 v[48:49], v[48:49], v[16:17]\n"
"v_pk_add_f32 v[50:51], v[50:51], v[18:19]\n"
"v_pk_add_f32 v[28:29], v[28:29], v[44:45]\n"
"v_pk_add_f32 v[30:31], v[30:31], v[46:47]\n"
"v_pk_add_f32 v[32:33], v[32:33], v[48:49]\n"
"v_pk_add_f32 v[34:35], v[34:35], v[50:51]\n"
"v_fma_f32 v60, v28, v126, v60\n"
"v_fma_f32 v61, v29, v126, v61\n"
"v_fma_f32 v62, v30, v126, v62\n"
"v_fma_f32 v63, v31, v126, v63\n"
"v_fma_f32 v64, v32, v126, v64\n"
"v_fma_f32 v65, v33, v126, v65\n"
"v_fma_f32 v66, v34, v126, v66\n"
"v_fma_f32 v67, v35, v126, v67\n"
"v_add_f32 v28, v60, v61\n"
"v_mul_f32 v29, v60, v60\n"
"v_add_f32 v30, v62, v63\n"
"v_mul_f32 v31, v61, v61\n"
"v_add_f32 v28, v28, v64\n"
"v_fmac_f32 v29, v62, v62\n"
"v_add_f32 v30, v30, v65\n"
"v_fmac_f32 v31, v63, v63\n"
"v_add_f32 v28, v28, v66\n"
"v_fmac_f32 v29, v64, v64\n"
"v_add_f32 v30, v30, v67\n"
"v_fmac_f32 v31, v65, v65\n"
"v_add_f32 v28, v28, v30\n"
"v_fmac_f32 v29, v66, v66\n"
"v_fmac_f32 v31, v67, v67\n"
"v_add_f32 v29, v29, v31\n"
"v_add_f32_dpp v30, v28, v28 row_ror:8 row_mask:0xf bank_mask:0xf\n"
"s_nop 0\n"
"v_add_f32_dpp v31, v29, v29 row_ror:8 row_mask:0xf bank_mask:0xf\n"
"s_nop 1\n"
"v_permlane16_swap_b32 v30, v31\n"
"v_add_f32 v30, v30, v31\n"
"v_mov_b32 v31, v30\n"
"s_nop 1\n"
"v_permlane32_swap_b32 v30, v31\n"
"v_add_f32 v28, v30, v31\n"
"v_mov_b32 v29, v28\n"
"v_mov_b32 v30, v28\n"
"s_nop 1\n"
"v_permlane16_swap_b32 v30, v29\n"
"v_min_u32 v28, -2, v28\n"
"v_min_u32 v29, -2, v29\n"
"s_add_u32 s53, s39, 0x40800\n"
"buffer_store_dwordx2 v[28:29], v137, s[8:11], s53 offen sc1\n"
"v_cvt_pk_bf16_f32 v56, v60, v61\n"
"v_cvt_pk_bf16_f32 v57, v62, v63\n"
"v_cvt_pk_bf16_f32 v58, v64, v65\n"
"v_cvt_pk_bf16_f32 v59, v66, v67\n"
"s_waitcnt vmcnt(1)\n"
"v_max_u32 v134, v130, v131\n"
"v_cmp_eq_u32 vcc, -1, v134\n"
"s_cbranch_vccnz Lslow18_sa\n"
"Lback18_sa:\n"
"v_add_f32_dpp v124, v130, v130 row_ror:8 row_mask:0xf bank_mask:0xf\n"
"v_add_f32_dpp v125, v131, v131 row_ror:8 row_mask:0xf bank_mask:0xf\n"
"s_nop 1\n"
"v_permlane16_swap_b32 v124, v125\n"
"v_add_f32 v124, v124, v125\n"
"v_mov_b32 v125, v124\n"
"s_nop 1\n"
"v_permlane32_swap_b32 v124, v125\n"
"v_add_f32 v130, v124, v125\n"
"v_mov_b32 v131, v130\n"
"s_nop 1\n"
"v_permlane16_swap_b32 v130, v131\n"
"v_mul_f32 v130, 0xbb000000, v130\n"
"v_mul_f32 v124, v130, v130\n"
"v_fma_f32 v131, v131, s37, -v124\n"
"v_max_f32 v131, 0, v131\n"
"v_cvt_pk_bf16_f32 v124, v130, v131\n"
"v_and_b32 v131, s61, v124\n"
"v_lshlrev_b32 v130, 16, v124\n"
"v_add_f32 v131, 0x3727c5ac, v131\n"
"v_cvt_pk_bf16_f32 v131, v131, v131\n"
"v_and_b32 v131, s61, v131\n"
"v_sqrt_f32 v131, v131\n"
"s_nop 0\n"
"v_cvt_pk_bf16_f32 v131, v131, v131\n"
"v_and_b32 v131, s61, v131\n"
"v_rcp_f32 v131, v131\n"
"v_lshlrev_b32 v0, 16, v52\n"
"v_and_b32 v1, s61, v52\n"
"v_lshlrev_b32 v2, 16, v53\n"
"v_and_b32 v3, s61, v53\n"
"v_lshlrev_b32 v16, 16, v54\n"
"v_and_b32 v17, s61, v54\n"
"v_lshlrev_b32 v18, 16, v55\n"
"v_and_b32 v19, s61, v55\n"
"v_pk_add_f32 v[36:37], v[0:1], v[130:131] op_sel_hi:[1,0]\n"
"v_pk_add_f32 v[38:39], v[2:3], v[130:131] op_sel_hi:[1,0]\n"
"v_pk_add_f32 v[40:41], v[16:17], v[130:131] op_sel_hi:[1,0]\n"
"v_pk_add_f32 v[42:43], v[18:19], v[130:131] op_sel_hi:[1,0]\n"
"v_cvt_pk_bf16_f32 v52, v36, v37\n"
"v_cvt_pk_bf16_f32 v53, v38, v39\n"
"v_cvt_pk_bf16_f32 v54, v40, v41\n"
"v_cvt_pk_bf16_f32 v55, v42, v43\n"
"v_lshlrev_b32 v36, 16, v52\n"
"v_and_b32 v37, s61, v52\n"
"v_lshlrev_b32 v38, 16, v53\n"
"v_and_b32 v39, s61, v53\n"
"v_lshlrev_b32 v40, 16, v54\n"
"v_and_b32 v41, s61, v54\n"
"v_lshlrev_b32 v42, 16, v55\n"
"v_and_b32 v43, s61, v55\n"
"v_pk_mul_f32 v[36:37], v[36:37], v[130:131] op_sel:[0,1] op_sel_hi:[1,1]\n"
"v_pk_mul_f32 v[38:39], v[38:39], v[130:131] op_sel:[0,1] op_sel_hi:[1,1]\n"
"v_pk_mul_f32 v[40:41], v[40:41], v[130:131] op_sel:[0,1] op_sel_hi:[1,1]\n"
"v_pk_mul_f32 v[42:43], v[42:43], v[130:131] op_sel:[0,1] op_sel_hi:[1,1]\n"
"v_cvt_pk_bf16_f32 v52, v36, v37\n"
"v_cvt_pk_bf16_f32 v53, v38, v39\n"
"v_cvt_pk_bf16_f32 v54, v40, v41\n"
"v_cvt_pk_bf16_f32 v55, v42, v43\n"
"v_and_b32 v28, 0xffff, v20\n"
"v_and_b32 v29, s61, v20\n"
"v_and_b32 v30, 0xffff, v21\n"
"v_and_b32 v31, s61, v21\n"
"v_and_b32 v32, 0xffff, v22\n"
"v_and_b32 v33, s61, v22\n"
"v_and_b32 v34, 0xffff, v23\n"
"v_and_b32 v35, s61, v23\n"
"v_dot2_f32_bf16 v36, v52, v28, 0\n"
"v_dot2_f32_bf16 v37, v52, v29, 0\n"
"v_dot2_f32_bf16 v38, v53, v30, 0\n"
"v_dot2_f32_bf16 v39, v53, v31, 0\n"
"v_dot2_f32_bf16 v40, v54, v32, 0\n"
"v_dot2_f32_bf16 v41, v54, v33, 0\n"
"v_dot2_f32_bf16 v42, v55, v34, 0\n"
"v_dot2_f32_bf16 v43, v55, v35, 0\n"
"v_cvt_pk_bf16_f32 v36, v36, v0\n"
"v_cvt_pk_bf16_f32 v37, v37, v1\n"
"v_cvt_pk_bf16_f32 v38, v38, v2\n"
"v_cvt_pk_bf16_f32 v39, v39, v3\n"
"v_cvt_pk_bf16_f32 v40, v40, v16\n"
"v_cvt_pk_bf16_f32 v41, v41, v17\n"
"v_cvt_pk_bf16_f32 v42, v42, v18\n"
"v_cvt_pk_bf16_f32 v43, v43, v19\n"
"v_dot2_f32_bf16 v0, v36, s94, 0\n"
"v_dot2_f32_bf16 v1, v37, s94, 0\n"
"v_dot2_f32_bf16 v2, v38, s94, 0\n"
"v_dot2_f32_bf16 v3, v39, s94, 0\n"
"v_dot2_f32_bf16 v16, v40, s94, 0\n"
"v_dot2_f32_bf16 v17, v41, s94, 0\n"
"v_dot2_f32_bf16 v18, v42, s94, 0\n"
"v_dot2_f32_bf16 v19, v43, s94, 0\n"
"v_cvt_pk_bf16_f32 v52, v0, v1\n"
"v_cvt_pk_bf16_f32 v53, v2, v3\n"
"v_cvt_pk_bf16_f32 v54, v16, v17\n"
"v_cvt_pk_bf16_f32 v55, v18, v19\n"
"v_min_u32 v52, -2, v52\n"
"v_min_u32 v53, -2, v53\n"
"v_min_u32 v54, -2, v54\n"
"v_min_u32 v55, -2, v55\n"
"s_add_u32 s53, s30, 0x200000\n"
"buffer_store_dwordx4 v[52:55], v15, s[8:11], s53 offen sc1\n"
"v_add_f32 v28, v0, v1\n"
"v_mul_f32 v29, v0, v0\n"
"v_add_f32 v30, v2, v3\n"
"v_mul_f32 v31, v1, v1\n"
"v_add_f32 v28, v28, v16\n"
"v_fmac_f32 v29, v2, v2\n"
"v_add_f32 v30, v30, v17\n"
"v_fmac_f32 v31, v3, v3\n"
"v_add_f32 v28, v28, v18\n"
"v_fmac_f32 v29, v16, v16\n"
"v_add_f32 v30, v30, v19\n"
"v_fmac_f32 v31, v17, v17\n"
"v_add_f32 v28, v28, v30\n"
"v_fmac_f32 v29, v18, v18\n"
"v_fmac_f32 v31, v19, v19\n"
"v_add_f32 v29, v29, v31\n"
"v_add_f32_dpp v30, v28, v28 row_ror:8 row_mask:0xf bank_mask:0xf\n"
"s_nop 0\n"
"v_add_f32_dpp v31, v29, v29 row_ror:8 row_mask:0xf bank_mask:0xf\n"
"s_nop 1\n"
"v_permlane16_swap_b32 v30, v31\n"
"v_add_f32 v30, v30, v31\n"
"v_mov_b32 v31, v30\n"
"s_nop 1\n"
"v_permlane32_swap_b32 v30, v31\n"
"v_add_f32 v28, v30, v31\n"
"v_mov_b32 v29, v28\n"
"v_mov_b32 v30, v28\n"
"s_nop 1\n"
"v_permlane16_swap_b32 v30, v29\n"
"v_min_u32 v28, -2, v28\n"
"v_min_u32 v29, -2, v29\n"
"s_add_u32 s53, s39, 0x60000\n"
"buffer_store_dwordx2 v[28:29], v137, s[8:11], s53 offen sc1\n"
"s_sleep 10\n"
"s_add_u32 s59, s74, 0x40800\n"
"buffer_load_dwordx2 v[128:129], v136, s[8:11], s59 offen sc1\n"
"s_sleep 6\n"
"s_add_u32 s59, s93, 0x60000\n"
"buffer_load_dwordx2 v[0:1], v11, s[8:11], s59 offen sc1\n"
"s_add_u32 s59, s93, 0x60000\n"
"buffer_load_dwordx2 v[2:3], v11, s[8:11], s59 offen offset:128 sc1\n"
"s_add_u32 s59, s93, 0x60400\n"
"buffer_load_dwordx2 v[16:17], v11, s[8:11], s59 offen sc1\n"
"s_add_u32 s59, s93, 0x60400\n"
"buffer_load_dwordx2 v[18:19], v11, s[8:11], s59 offen offset:128 sc1\n"
"s_add_u32 m0, s87, 0\n"
"s_add_u32 s27, s86, 0x200000\n"
"buffer_load_dwordx4 v142, s[8:11], s27 offen sc1 lds\n"
"buffer_load_dwordx4 v142, s[8:11], s27 offen offset:1024 sc1 lds\n"
"buffer_load_dwordx4 v142, s[8:11], s27 offen offset:2048 sc1 lds\n"
"buffer_load_dwordx4 v142, s[8:11], s27 offen offset:3072 sc1 lds\n"
"s_add_u32 m0, s87, 0x4000\n"
"s_add_u32 s27, s86, 0x204000\n"
"buffer_load_dwordx4 v142, s[8:11], s27 offen sc1 lds\n"
"buffer_load_dwordx4 v142, s[8:11], s27 offen offset:1024 sc1 lds\n"
"buffer_load_dwordx4 v142, s[8:11], s27 offen offset:2048 sc1 lds\n"
"buffer_load_dwordx4 v142, s[8:11], s27 offen offset:3072 sc1 lds\n"
"s_waitcnt vmcnt(12)\n"
"v_max_u32 v134, v128, v129\n"
"v_cmp_eq_u32 vcc, -1, v134\n"
"s_cbranch_vccnz Lslow19_sb\n"
"Lback19_sb:\n"
"v_add_f32_dpp v124, v128, v128 row_ror:8 row_mask:0xf bank_mask:0xf\n"
"v_add_f32_dpp v125, v129, v129 row_ror:8 row_mask:0xf bank_mask:0xf\n"
"s_nop 1\n"
"v_permlane16_swap_b32 v124, v125\n"
"v_add_f32 v124, v124, v125\n"
"v_mov_b32 v125, v124\n"
"s_nop 1\n"
"v_permlane32_swap_b32 v124, v125\n"
"v_add_f32 v128, v124, v125\n"
"v_mov_b32 v129, v128\n"
"s_nop 1\n"
"v_permlane16_swap_b32 v128, v129\n"
"v_mul_f32 v128, 0xbb000000, v128\n"
"v_mul_f32 v124, v128, v128\n"
"v_fma_f32 v129, v129, s37, -v124\n"
"v_max_f32 v129, 0, v129\n"
"v_cvt_pk_bf16_f32 v124, v128, v129\n"
"v_and_b32 v129, s61, v124\n"
"v_lshlrev_b32 v128, 16, v124\n"
"v_add_f32 v129, 0x3727c5ac, v129\n"
"v_cvt_pk_bf16_f32 v129, v129, v129\n"
"v_and_b32 v129, s61, v129\n"
"v_sqrt_f32 v129, v129\n"
"s_nop 0\n"
"v_cvt_pk_bf16_f32 v129, v129, v129\n"
"v_and_b32 v129, s61, v129\n"
"v_rcp_f32 v129, v129\n"
"v_lshlrev_b32 v36, 16, v56\n"
"v_and_b32 v37, s61, v56\n"
"v_lshlrev_b32 v38, 16, v57\n"
"v_and_b32 v39, s61, v57\n"
"v_lshlrev_b32 v40, 16, v58\n"
"v_and_b32 v41, s61, v58\n"
"v_lshlrev_b32 v42, 16, v59\n"
"v_and_b32 v43, s61, v59\n"
"v_pk_add_f32 v[60:61], v[36:37], v[128:129] op_sel_hi:[1,0]\n"
"v_pk_add_f32 v[62:63], v[38:39], v[128:129] op_sel_hi:[1,0]\n"
"v_pk_add_f32 v[64:65], v[40:41], v[128:129] op_sel_hi:[1,0]\n"
"v_pk_add_f32 v[66:67], v[42:43], v[128:129] op_sel_hi:[1,0]\n"
"v_cvt_pk_bf16_f32 v56, v60, v61\n"
"v_cvt_pk_bf16_f32 v57, v62, v63\n"
"v_cvt_pk_bf16_f32 v58, v64, v65\n"
"v_cvt_pk_bf16_f32 v59, v66, v67\n"
"v_lshlrev_b32 v60, 16, v56\n"
"v_and_b32 v61, s61, v56\n"
"v_lshlrev_b32 v62, 16, v57\n"
"v_and_b32 v63, s61, v57\n"
"v_lshlrev_b32 v64, 16, v58\n"
"v_and_b32 v65, s61, v58\n"
"v_lshlrev_b32 v66, 16, v59\n"
"v_and_b32 v67, s61, v59\n"
"v_pk_mul_f32 v[60:61], v[60:61], v[128:129] op_sel:[0,1] op_sel_hi:[1,1]\n"
"v_pk_mul_f32 v[62:63], v[62:63], v[128:129] op_sel:[0,1] op_sel_hi:[1,1]\n"
"v_pk_mul_f32 v[64:65], v[64:65], v[128:129] op_sel:[0,1] op_sel_hi:[1,1]\n"
"v_pk_mul_f32 v[66:67], v[66:67], v[128:129] op_sel:[0,1] op_sel_hi:[1,1]\n"
"v_cvt_pk_bf16_f32 v56, v60, v61\n"
"v_cvt_pk_bf16_f32 v57, v62, v63\n"
"v_cvt_pk_bf16_f32 v58, v64, v65\n"
"v_cvt_pk_bf16_f32 v59, v66, v67\n"
"v_and_b32 v44, 0xffff, v24\n"
"v_and_b32 v45, s61, v24\n"
"v_and_b32 v46, 0xffff, v25\n"
"v_and_b32 v47, s61, v25\n"
"v_and_b32 v48, 0xffff, v26\n"
"v_and_b32 v49, s61, v26\n"
"v_and_b32 v50, 0xffff, v27\n"
"v_and_b32 v51, s61, v27\n"
"v_dot2_f32_bf16 v60, v56, v44, 0\n"
"v_dot2_f32_bf16 v61, v56, v45, 0\n"
"v_dot2_f32_bf16 v62, v57, v46, 0\n"
"v_dot2_f32_bf16 v63, v57, v47, 0\n"
"v_dot2_f32_bf16 v64, v58, v48, 0\n"
"v_dot2_f32_bf16 v65, v58, v49, 0\n"
"v_dot2_f32_bf16 v66, v59, v50, 0\n"
"v_dot2_f32_bf16 v67, v59, v51, 0\n"
"v_cvt_pk_bf16_f32 v60, v60, v36\n"
"v_cvt_pk_bf16_f32 v61, v61, v37\n"
"v_cvt_pk_bf16_f32 v62, v62, v38\n"
"v_cvt_pk_bf16_f32 v63, v63, v39\n"
"v_cvt_pk_bf16_f32 v64, v64, v40\n"
"v_cvt_pk_bf16_f32 v65, v65, v41\n"
"v_cvt_pk_bf16_f32 v66, v66, v42\n"
"v_cvt_pk_bf16_f32 v67, v67, v43\n"
"v_dot2_f32_bf16 v36, v60, s94, 0\n"
"v_dot2_f32_bf16 v37, v61, s94, 0\n"
"v_dot2_f32_bf16 v38, v62, s94, 0\n"
"v_dot2_f32_bf16 v39, v63, s94, 0\n"
"v_dot2_f32_bf16 v40, v64, s94, 0\n"
"v_dot2_f32_bf16 v41, v65, s94, 0\n"
"v_dot2_f32_bf16 v42, v66, s94, 0\n"
"v_dot2_f32_bf16 v43, v67, s94, 0\n"
"v_cvt_pk_bf16_f32 v56, v36, v37\n"
"v_cvt_pk_bf16_f32 v57, v38, v39\n"
"v_cvt_pk_bf16_f32 v58, v40, v41\n"
"v_cvt_pk_bf16_f32 v59, v42, v43\n"
"v_min_u32 v56, -2, v56\n"
"v_min_u32 v57, -2, v57\n"
"v_min_u32 v58, -2, v58\n"
"v_min_u32 v59, -2, v59\n"
"s_add_u32 s53, s30, 0x208000\n"
"buffer_store_dwordx4 v[56:59], v15, s[8:11], s53 offen sc1\n"
"v_add_f32 v44, v36, v37\n"
"v_mul_f32 v45, v36, v36\n"
"v_add_f32 v46, v38, v39\n"
"v_mul_f32 v47, v37, v37\n"
"v_add_f32 v44, v44, v40\n"
"v_fmac_f32 v45, v38, v38\n"
"v_add_f32 v46, v46, v41\n"
"v_fmac_f32 v47, v39, v39\n"
"v_add_f32 v44, v44, v42\n"
"v_fmac_f32 v45, v40, v40\n"
"v_add_f32 v46, v46, v43\n"
"v_fmac_f32 v47, v41, v41\n"
"v_add_f32 v44, v44, v46\n"
"v_fmac_f32 v45, v42, v42\n"
"v_fmac_f32 v47, v43, v43\n"
"v_add_f32 v45, v45, v47\n"
"v_add_f32_dpp v46, v44, v44 row_ror:8 row_mask:0xf bank_mask:0xf\n"
"s_nop 0\n"
"v_add_f32_dpp v47, v45, v45 row_ror:8 row_mask:0xf bank_mask:0xf\n"
"s_nop 1\n"
"v_permlane16_swap_b32 v46, v47\n"
"v_add_f32 v46, v46, v47\n"
"v_mov_b32 v47, v46\n"
"s_nop 1\n"
"v_permlane32_swap_b32 v46, v47\n"
"v_add_f32 v44, v46, v47\n"
"v_mov_b32 v45, v44\n"
"v_mov_b32 v46, v44\n"
"s_nop 1\n"
"v_permlane16_swap_b32 v46, v45\n"
"v_min_u32 v44, -2, v44\n"
"v_min_u32 v45, -2, v45\n"
"s_add_u32 s53, s39, 0x60800\n"
"buffer_store_dwordx2 v[44:45], v137, s[8:11], s53 offen sc1\n"
"s_waitcnt vmcnt(12)\n"
"v_max3_u32 v134, v0, v1, v2\n"
"s_waitcnt vmcnt(11)\n"
"v_max3_u32 v134, v134, v3, v16\n"
"s_waitcnt vmcnt(10)\n"
"v_max3_u32 v134, v134, v17, v18\n"
"v_max_u32 v134, v134, v19\n"
"v_cmp_eq_u32 vcc, -1, v134\n"
"s_cbranch_vccnz Lslow20_xs10\n"
"Lback20_xs10:\n"
"s_waitcnt vmcnt(2)\n"
"s_barrier\n"
"ds_read_b128 v[68:71], v4\n"
"ds_read_b128 v[72:75], v4 offset:16384\n"
"ds_read_b128 v[76:79], v4 offset:1024\n"
"ds_read_b128 v[80:83], v4 offset:17408\n"
"s_waitcnt lgkmcnt(2)\n"
"v_mfma_f32_16x16x32_bf16 v[36:39], a[176:179], v[68:71], 0\n"
"ds_read_b128 v[84:87], v5\n"
"v_mfma_f32_16x16x32_bf16 v[44:47], a[180:183], v[68:71], 0\n"
"ds_read_b128 v[88:91], v5 offset:16384\n"
"v_mfma_f32_16x16x32_bf16 v[60:63], a[184:187], v[68:71], 0\n"
"v_add_f32 v0, v0, v2\n"
"v_mfma_f32_16x16x32_bf16 v[40:43], a[176:179], v[72:75], 0\n"
"v_add_f32 v1, v1, v3\n"
"v_mfma_f32_16x16x32_bf16 v[48:51], a[180:183], v[72:75], 0\n"
"v_mfma_f32_16x16x32_bf16 v[64:67], a[184:187], v[72:75], 0\n"
"v_permlane16_swap_b32 v0, v1\n"
"s_waitcnt lgkmcnt(2)\n"
"v_mfma_f32_16x16x32_bf16 v[36:39], a[188:191], v[76:79], v[36:39]\n"
"ds_read_b128 v[68:71], v6\n"
"v_add_f32 v0, v0, v1\n"
"v_mfma_f32_16x16x32_bf16 v[44:47], a[192:195], v[76:79], v[44:47]\n"
"ds_read_b128 v[72:75], v6 offset:16384\n"
"v_mfma_f32_16x16x32_bf16 v[60:63], a[196:199], v[76:79], v[60:63]\n"
"v_mov_b32 v1, v0\n"
"v_mfma_f32_16x16x32_bf16 v[40:43], a[188:191], v[80:83], v[40:43]\n"
"s_nop 0\n"
"v_permlane32_swap_b32 v0, v1\n"
"v_mfma_f32_16x16x32_bf16 v[48:51], a[192:195], v[80:83], v[48:51]\n"
"v_mfma_f32_16x16x32_bf16 v[64:67], a[196:199], v[80:83], v[64:67]\n"
"v_add_f32 v0, v0, v1\n"
"s_waitcnt lgkmcnt(2)\n"
"v_mfma_f32_16x16x32_bf16 v[36:39], a[224:227], v[84:87], v[36:39]\n"
"ds_read_b128 v[76:79], v7\n"
"v_mov_b32 v1, v0\n"
"s_add_u32 s59, s93, 0x60800\n"
"buffer_load_dwordx2 v[28:29], v11, s[8:11], s59 offen sc1\n"
"v_mfma_f32_16x16x32_bf16 v[44:47], a[228:231], v[84:87], v[44:47]\n"
"ds_read_b128 v[80:83], v7 offset:16384\n"
"v_mfma_f32_16x16x32_bf16 v[60:63], a[232:235], v[84:87], v[60:63]\n"
"v_permlane16_swap_b32 v0, v1\n"
"v_mfma_f32_16x16x32_bf16 v[40:43], a[224:227], v[88:91], v[40:43]\n"
"v_mul_f32 v8, 0xbb000000, v0\n"
"v_mfma_f32_16x16x32_bf16 v[48:51], a[228:231], v[88:91], v[48:51]\n"
"s_add_u32 s59, s93, 0x60800\n"
"buffer_load_dwordx2 v[30:31], v11, s[8:11], s59 offen offset:128 sc1\n"
"v_mfma_f32_16x16x32_bf16 v[64:67], a[232:235], v[88:91], v[64:67]\n"
"v_mul_f32 v2, v8, v8\n"
"s_waitcnt lgkmcnt(2)\n"
"v_mfma_f32_16x16x32_bf16 v[36:39], v[160:163], v[68:71], v[36:39]\n"
"ds_read_b128 v[84:87], v4 offset:2048\n"
"v_fma_f32 v1, v1, s37, -v2\n"
"v_mfma_f32_16x16x32_bf16 v[44:47], v[164:167], v[68:71], v[44:47]\n"
"ds_read_b128 v[88:91], v4 offset:18432\n"
"v_mfma_f32_16x16x32_bf16 v[60:63], v[168:171], v[68:71], v[60:63]\n"
"v_max_f32 v1, 0, v1\n"
"s_add_u32 s59, s93, 0x60c00\n"
"buffer_load_dwordx2 v[32:33], v11, s[8:11], s59 offen sc1\n"
"v_mfma_f32_16x16x32_bf16 v[40:43], v[160:163], v[72:75], v[40:43]\n"
"v_add_f32 v1, 0x3727c5ac, v1\n"
"v_mfma_f32_16x16x32_bf16 v[48:51], v[164:167], v[72:75], v[48:51]\n"
"v_mfma_f32_16x16x32_bf16 v[64:67], v[168:171], v[72:75], v[64:67]\n"
"v_rsq_f32 v1, v1\n"
"s_waitcnt lgkmcnt(2)\n"
"v_mfma_f32_16x16x32_bf16 v[36:39], v[208:211], v[76:79], v[36:39]\n"
"ds_read_b128 v[68:71], v5 offset:1024\n"
"v_add_f32 v16, v16, v18\n"
"s_add_u32 s59, s93, 0x60c00\n"
"buffer_load_dwordx2 v[34:35], v11, s[8:11], s59 offen offset:128 sc1\n"
"v_mfma_f32_16x16x32_bf16 v[44:47], v[212:215], v[76:79], v[44:47]\n"
"ds_read_b128 v[72:75], v5 offset:17408\n"
"v_mfma_f32_16x16x32_bf16 v[60:63], v[216:219], v[76:79], v[60:63]\n"
"v_add_f32 v17, v17, v19\n"
"v_mfma_f32_16x16x32_bf16 v[40:43], v[208:211], v[80:83], v[40:43]\n"
"v_mfma_f32_16x16x32_bf16 v[48:51], v[212:215], v[80:83], v[48:51]\n"
"v_permlane16_swap_b32 v16, v17\n"
"s_add_u32 m0, s87, 0x8000\n"
"s_add_u32 s27, s86, 0x208000\n"
"buffer_load_dwordx4 v142, s[8:11], s27 offen sc1 lds\n"
"v_mfma_f32_16x16x32_bf16 v[64:67], v[216:219], v[80:83], v[64:67]\n"
"v_add_f32 v16, v16, v17\n"
"s_waitcnt lgkmcnt(2)\n"
"v_mfma_f32_16x16x32_bf16 v[36:39], a[200:203], v[84:87], v[36:39]\n"
"ds_read_b128 v[76:79], v6 offset:1024\n"
"v_mfma_f32_16x16x32_bf16 v[44:47], a[204:207], v[84:87], v[44:47]\n"
"ds_read_b128 v[80:83], v6 offset:17408\n"
"v_mov_b32 v17, v16\n"
"v_mfma_f32_16x16x32_bf16 v[60:63], a[208:211], v[84:87], v[60:63]\n"
"s_nop 0\n"
"v_permlane32_swap_b32 v16, v17\n"
"buffer_load_dwordx4 v142, s[8:11], s27 offen offset:1024 sc1 lds\n"
"v_mfma_f32_16x16x32_bf16 v[40:43], a[200:203], v[88:91], v[40:43]\n"
"v_mfma_f32_16x16x32_bf16 v[48:51], a[204:207], v[88:91], v[48:51]\n"
"v_add_f32 v16, v16, v17\n"
"v_mfma_f32_16x16x32_bf16 v[64:67], a[208:211], v[88:91], v[64:67]\n"
"v_mov_b32 v17, v16\n"
"s_waitcnt lgkmcnt(2)\n"
"v_mfma_f32_16x16x32_bf16 v[36:39], a[236:239], v[68:71], v[36:39]\n"
"ds_read_b128 v[84:87], v7 offset:1024\n"
"buffer_load_dwordx4 v142, s[8:11], s27 offen offset:2048 sc1 lds\n"
"v_mfma_f32_16x16x32_bf16 v[44:47], a[240:243], v[68:71], v[44:47]\n"
"ds_read_b128 v[88:91], v7 offset:17408\n"
"v_permlane16_swap_b32 v16, v17\n"
"v_mfma_f32_16x16x32_bf16 v[60:63], a[244:247], v[68:71], v[60:63]\n"
"v_mul_f32 v9, 0xbb000000, v16\n"
"v_mfma_f32_16x16x32_bf16 v[40:43], a[236:239], v[72:75], v[40:43]\n"
"v_mfma_f32_16x16x32_bf16 v[48:51], a[240:243], v[72:75], v[48:51]\n"
"v_mul_f32 v18, v9, v9\n"
"buffer_load_dwordx4 v142, s[8:11], s27 offen offset:3072 sc1 lds\n"
"v_mfma_f32_16x16x32_bf16 v[64:67], a[244:247], v[72:75], v[64:67]\n"
"v_fma_f32 v17, v17, s37, -v18\n"
"s_waitcnt lgkmcnt(2)\n"
"v_mfma_f32_16x16x32_bf16 v[36:39], v[172:175], v[76:79], v[36:39]\n"
"ds_read_b128 v[68:71], v4 offset:3072\n"
"v_mfma_f32_16x16x32_bf16 v[44:47], v[176:179], v[76:79], v[44:47]\n"
"ds_read_b128 v[72:75], v4 offset:19456\n"
"v_max_f32 v17, 0, v17\n"
"v_mfma_f32_16x16x32_bf16 v[60:63], v[180:183], v[76:79], v[60:63]\n"
"v_add_f32 v17, 0x3727c5ac, v17\n"
"s_add_u32 m0, s87, 0xc000\n"
"s_add_u32 s27, s86, 0x20c000\n"
"buffer_load_dwordx4 v142, s[8:11], s27 offen sc1 lds\n"
"v_mfma_f32_16x16x32_bf16 v[40:43], v[172:175], v[80:83], v[40:43]\n"
"v_mfma_f32_16x16x32_bf16 v[48:51], v[176:179], v[80:83], v[48:51]\n"
"v_rsq_f32 v17, v17\n"
"v_mfma_f32_16x16x32_bf16 v[64:67], v[180:183], v[80:83], v[64:67]\n"
"s_cmp_lt_u32 s7, 2\n"
"s_cselect_b64 vcc, -1, 0\n"
"v_cndmask_b32 v1, v17, v1, vcc\n"
"s_waitcnt lgkmcnt(2)\n"
"v_mfma_f32_16x16x32_bf16 v[36:39], v[220:223], v[84:87], v[36:39]\n"
"ds_read_b128 v[76:79], v5 offset:2048\n"
"buffer_load_dwordx4 v142, s[8:11], s27 offen offset:1024 sc1 lds\n"
"v_mfma_f32_16x16x32_bf16 v[44:47], v[224:227], v[84:87], v[44:47]\n"
"ds_read_b128 v[80:83], v5 offset:18432\n"
"ds_bpermute_b32 v127, v12, v1\n"
"v_mfma_f32_16x16x32_bf16 v[60:63], v[228:231], v[84:87], v[60:63]\n"
"v_mfma_f32_16x16x32_bf16 v[40:43], v[220:223], v[88:91], v[40:43]\n"
"v_mfma_f32_16x16x32_bf16 v[48:51], v[224:227], v[88:91], v[48:51]\n"
"buffer_load_dwordx4 v142, s[8:11], s27 offen offset:2048 sc1 lds\n"
"v_mfma_f32_16x16x32_bf16 v[64:67], v[228:231], v[88:91], v[64:67]\n"
"s_waitcnt lgkmcnt(3)\n"
"v_mfma_f32_16x16x32_bf16 v[36:39], a[212:215], v[68:71], v[36:39]\n"
"ds_read_b128 v[84:87], v6 offset:2048\n"
"v_mfma_f32_16x16x32_bf16 v[44:47], a[216:219], v[68:71], v[44:47]\n"
"ds_read_b128 v[88:91], v6 offset:18432\n"
"v_mfma_f32_16x16x32_bf16 v[60:63], a[220:223], v[68:71], v[60:63]\n"
"buffer_load_dwordx4 v142, s[8:11], s27 offen offset:3072 sc1 lds\n"
"v_mfma_f32_16x16x32_bf16 v[40:43], a[212:215], v[72:75], v[40:43]\n"
"v_mfma_f32_16x16x32_bf16 v[48:51], a[216:219], v[72:75], v[48:51]\n"
"v_mfma_f32_16x16x32_bf16 v[64:67], a[220:223], v[72:75], v[64:67]\n"
"s_waitcnt lgkmcnt(3)\n"
"v_mfma_f32_16x16x32_bf16 v[36:39], a[248:251], v[76:79], v[36:39]\n"
"ds_read_b128 v[68:71], v7 offset:2048\n"
"v_mfma_f32_16x16x32_bf16 v[44:47], a[252:255], v[76:79], v[44:47]\n"
"ds_read_b128 v[72:75], v7 offset:18432\n"
"v_mfma_f32_16x16x32_bf16 v[60:63], v[144:147], v[76:79], v[60:63]\n"
"v_mfma_f32_16x16x32_bf16 v[40:43], a[248:251], v[80:83], v[40:43]\n"
"v_mfma_f32_16x16x32_bf16 v[48:51], a[252:255], v[80:83], v[48:51]\n"
"v_mfma_f32_16x16x32_bf16 v[64:67], v[144:147], v[80:83], v[64:67]\n"
"s_waitcnt lgkmcnt(2)\n"
"v_mfma_f32_16x16x32_bf16 v[36:39], v[184:187], v[84:87], v[36:39]\n"
"ds_read_b128 v[76:79], v5 offset:3072\n"
"v_mfma_f32_16x16x32_bf16 v[44:47], v[188:191], v[84:87], v[44:47]\n"
"ds_read_b128 v[80:83], v5 offset:19456\n"
"v_mfma_f32_16x16x32_bf16 v[60:63], v[192:195], v[84:87], v[60:63]\n"
"v_mfma_f32_16x16x32_bf16 v[40:43], v[184:187], v[88:91], v[40:43]\n"
"v_mfma_f32_16x16x32_bf16 v[48:51], v[188:191], v[88:91], v[48:51]\n"
"v_mfma_f32_16x16x32_bf16 v[64:67], v[192:195], v[88:91], v[64:67]\n"
"s_waitcnt lgkmcnt(2)\n"
"v_mfma_f32_16x16x32_bf16 v[36:39], v[232:235], v[68:71], v[36:39]\n"
"ds_read_b128 v[84:87], v6 offset:3072\n"
"v_mfma_f32_16x16x32_bf16 v[44:47], v[236:239], v[68:71], v[44:47]\n"
"ds_read_b128 v[88:91], v6 offset:19456\n"
"v_mfma_f32_16x16x32_bf16 v[60:63], v[240:243], v[68:71], v[60:63]\n"
"v_mfma_f32_16x16x32_bf16 v[40:43], v[232:235], v[72:75], v[40:43]\n"
"v_mfma_f32_16x16x32_bf16 v[48:51], v[236:239], v[72:75], v[48:51]\n"
"v_mfma_f32_16x16x32_bf16 v[64:67], v[240:243], v[72:75], v[64:67]\n"
"s_waitcnt lgkmcnt(2)\n"
"v_mfma_f32_16x16x32_bf16 v[36:39], v[148:151], v[76:79], v[36:39]\n"
"ds_read_b128 v[68:71], v7 offset:3072\n"
"v_mfma_f32_16x16x32_bf16 v[44:47], v[152:155], v[76:79], v[44:47]\n"
"ds_read_b128 v[72:75], v7 offset:19456\n"
"v_mfma_f32_16x16x32_bf16 v[60:63], v[156:159], v[76:79], v[60:63]\n"
"v_mfma_f32_16x16x32_bf16 v[40:43], v[148:151], v[80:83], v[40:43]\n"
"v_mfma_f32_16x16x32_bf16 v[48:51], v[152:155], v[80:83], v[48:51]\n"
"v_mfma_f32_16x16x32_bf16 v[64:67], v[156:159], v[80:83], v[64:67]\n"
"s_waitcnt lgkmcnt(2)\n"
"v_mfma_f32_16x16x32_bf16 v[36:39], v[196:199], v[84:87], v[36:39]\n"
"v_mfma_f32_16x16x32_bf16 v[44:47], v[200:203], v[84:87], v[44:47]\n"
"ds_read_b128 v[76:79], v10\n"
"v_mfma_f32_16x16x32_bf16 v[60:63], v[204:207], v[84:87], v[60:63]\n"
"v_mfma_f32_16x16x32_bf16 v[40:43], v[196:199], v[88:91], v[40:43]\n"
"ds_read_b128 v[80:83], v10 offset:64\n"
"v_mfma_f32_16x16x32_bf16 v[48:51], v[200:203], v[88:91], v[48:51]\n"
"v_mfma_f32_16x16x32_bf16 v[64:67], v[204:207], v[88:91], v[64:67]\n"
"s_waitcnt lgkmcnt(2)\n"
"v_mfma_f32_16x16x32_bf16 v[36:39], v[244:247], v[68:71], v[36:39]\n"
"ds_read_b128 v[84:87], v10 offset:128\n"
"v_mfma_f32_16x16x32_bf16 v[44:47], v[248:251], v[68:71], v[44:47]\n"
"v_mfma_f32_16x16x32_bf16 v[60:63], v[252:255], v[68:71], v[60:63]\n"
"v_mfma_f32_16x16x32_bf16 v[40:43], v[244:247], v[72:75], v[40:43]\n"
"v_mfma_f32_16x16x32_bf16 v[48:51], v[248:251], v[72:75], v[48:51]\n"
"v_mfma_f32_16x16x32_bf16 v[64:67], v[252:255], v[72:75], v[64:67]\n"
"s_nop 5\n"
"v_cmp_u_f32 vcc, v36, v40\n"
"s_cbranch_vccnz Lslowx10\n"
"Lbackx10:\n"
"s_waitcnt lgkmcnt(2)\n"
"v_pk_fma_f32 v[36:37], v[76:77], v[8:9], v[36:37] op_sel:[0,0,0] op_sel_hi:[1,0,1]\n"
"v_pk_fma_f32 v[38:39], v[78:79], v[8:9], v[38:39] op_sel:[0,0,0] op_sel_hi:[1,0,1]\n"
"v_pk_fma_f32 v[40:41], v[76:77], v[8:9], v[40:41] op_sel:[0,1,0] op_sel_hi:[1,1,1]\n"
"v_pk_fma_f32 v[42:43], v[78:79], v[8:9], v[42:43] op_sel:[0,1,0] op_sel_hi:[1,1,1]\n"
"v_cvt_pk_bf16_f32 v88, -v36, -v37\n"
"v_cvt_pk_bf16_f32 v89, -v38, -v39\n"
"v_cvt_pk_bf16_f32 v90, -v40, -v41\n"
"v_cvt_pk_bf16_f32 v91, -v42, -v43\n"
"v_pk_max_u16 v88, v88, s38\n"
"v_pk_max_u16 v90, v90, s38\n"
"v_pk_max_u16 v89, v89, s38\n"
"v_pk_max_u16 v91, v91, s38\n"
"s_add_u32 s53, s33, 0\n"
"v_permlane32_swap_b32 v88, v90\n"
"v_permlane32_swap_b32 v89, v91\n"
"v_xor_b32 v88, v14, v88\n"
"buffer_store_dwordx4 v[88:91], v13, s[8:11], s53 offen sc1\n"
"s_waitcnt lgkmcnt(1)\n"
"v_pk_fma_f32 v[44:45], v[80:81], v[8:9], v[44:45] op_sel:[0,0,0] op_sel_hi:[1,0,1]\n"
"v_pk_fma_f32 v[46:47], v[82:83], v[8:9], v[46:47] op_sel:[0,0,0] op_sel_hi:[1,0,1]\n"
"v_pk_fma_f32 v[48:49], v[80:81], v[8:9], v[48:49] op_sel:[0,1,0] op_sel_hi:[1,1,1]\n"
"v_pk_fma_f32 v[50:51], v[82:83], v[8:9], v[50:51] op_sel:[0,1,0] op_sel_hi:[1,1,1]\n"
"v_cvt_pk_bf16_f32 v68, -v44, -v45\n"
"v_cvt_pk_bf16_f32 v69, -v46, -v47\n"
"v_cvt_pk_bf16_f32 v70, -v48, -v49\n"
"v_cvt_pk_bf16_f32 v71, -v50, -v51\n"
"v_pk_max_u16 v68, v68, s38\n"
"v_pk_max_u16 v70, v70, s38\n"
"v_pk_max_u16 v69, v69, s38\n"
"v_pk_max_u16 v71, v71, s38\n"
"s_add_u32 s53, s33, 0x2000\n"
"v_permlane32_swap_b32 v68, v70\n"
"v_permlane32_swap_b32 v69, v71\n"
"v_xor_b32 v68, v14, v68\n"
"buffer_store_dwordx4 v[68:71], v13, s[8:11], s53 offen sc1\n"
"s_waitcnt lgkmcnt(0)\n"
"v_pk_fma_f32 v[60:61], v[84:85], v[8:9], v[60:61] op_sel:[0,0,0] op_sel_hi:[1,0,1]\n"
"v_pk_fma_f32 v[62:63], v[86:87], v[8:9], v[62:63] op_sel:[0,0,0] op_sel_hi:[1,0,1]\n"
"v_pk_fma_f32 v[64:65], v[84:85], v[8:9], v[64:65] op_sel:[0,1,0] op_sel_hi:[1,1,1]\n"
"v_pk_fma_f32 v[66:67], v[86:87], v[8:9], v[66:67] op_sel:[0,1,0] op_sel_hi:[1,1,1]\n"
"v_cvt_pk_bf16_f32 v88, -v60, -v61\n"
"v_cvt_pk_bf16_f32 v89, -v62, -v63\n"
"v_cvt_pk_bf16_f32 v90, -v64, -v65\n"
"v_cvt_pk_bf16_f32 v91, -v66, -v67\n"
"v_pk_max_u16 v88, v88, s38\n"
"v_pk_max_u16 v90, v90, s38\n"
"v_pk_max_u16 v89, v89, s38\n"
"v_pk_max_u16 v91, v91, s38\n"
"s_add_u32 s53, s34, 0\n"
"v_permlane32_swap_b32 v88, v90\n"
"v_permlane32_swap_b32 v89, v91\n"
"v_xor_b32 v88, v14, v88\n"
"buffer_store_dwordx4 v[88:91], v13, s[8:11], s53 offen sc1\n"
"s_waitcnt vmcnt(13)\n"
"v_max3_u32 v134, v28, v29, v30\n"
"s_waitcnt vmcnt(12)\n"
"v_max3_u32 v134, v134, v31, v32\n"
"s_waitcnt vmcnt(11)\n"
"v_max3_u32 v134, v134, v33, v34\n"
"v_max_u32 v134, v134, v35\n"
"v_cmp_eq_u32 vcc, -1, v134\n"
"s_cbranch_vccnz Lslow21_xs11\n"
"Lback21_xs11:\n"
"s_waitcnt vmcnt(3)\n"
"s_barrier\n"
"ds_read_b128 v[68:71], v4 offset:32768\n"
"ds_read_b128 v[72:75], v4 offset:49152\n"
"ds_read_b128 v[76:79], v4 offset:33792\n"
"ds_read_b128 v[80:83], v4 offset:50176\n"
"s_waitcnt lgkmcnt(2)\n"
"v_mfma_f32_16x16x32_bf16 v[36:39], a[176:179], v[68:71], 0\n"
"ds_read_b128 v[84:87], v5 offset:32768\n"
"v_mfma_f32_16x16x32_bf16 v[44:47], a[180:183], v[68:71], 0\n"
"ds_read_b128 v[88:91], v5 offset:49152\n"
"v_mfma_f32_16x16x32_bf16 v[60:63], a[184:187], v[68:71], 0\n"
"v_add_f32 v28, v28, v30\n"
"v_mfma_f32_16x16x32_bf16 v[40:43], a[176:179], v[72:75], 0\n"
"v_add_f32 v29, v29, v31\n"
"v_mfma_f32_16x16x32_bf16 v[48:51], a[180:183], v[72:75], 0\n"
"s_nop 0\n"
"v_permlane16_swap_b32 v28, v29\n"
"v_mfma_f32_16x16x32_bf16 v[64:67], a[184:187], v[72:75], 0\n"
"v_add_f32 v28, v28, v29\n"
"s_waitcnt lgkmcnt(2)\n"
"v_mfma_f32_16x16x32_bf16 v[36:39], a[188:191], v[76:79], v[36:39]\n"
"ds_read_b128 v[68:71], v6 offset:32768\n"
"v_mov_b32 v29, v28\n"
"v_mfma_f32_16x16x32_bf16 v[44:47], a[192:195], v[76:79], v[44:47]\n"
"ds_read_b128 v[72:75], v6 offset:49152\n"
"v_permlane32_swap_b32 v28, v29\n"
"v_mfma_f32_16x16x32_bf16 v[60:63], a[196:199], v[76:79], v[60:63]\n"
"v_add_f32 v28, v28, v29\n"
"v_mfma_f32_16x16x32_bf16 v[40:43], a[188:191], v[80:83], v[40:43]\n"
"v_mov_b32 v29, v28\n"
"v_mfma_f32_16x16x32_bf16 v[48:51], a[192:195], v[80:83], v[48:51]\n"
"s_nop 0\n"
"v_permlane16_swap_b32 v28, v29\n"
"v_mfma_f32_16x16x32_bf16 v[64:67], a[196:199], v[80:83], v[64:67]\n"
"v_mul_f32 v8, 0xbb000000, v28\n"
"s_waitcnt lgkmcnt(2)\n"
"v_mfma_f32_16x16x32_bf16 v[36:39], a[224:227], v[84:87], v[36:39]\n"
"ds_read_b128 v[76:79], v7 offset:32768\n"
"v_mul_f32 v30, v8, v8\n"
"v_mfma_f32_16x16x32_bf16 v[44:47], a[228:231], v[84:87], v[44:47]\n"
"ds_read_b128 v[80:83], v7 offset:49152\n"
"v_fma_f32 v29, v29, s37, -v30\n"
"v_mfma_f32_16x16x32_bf16 v[60:63], a[232:235], v[84:87], v[60:63]\n"
"v_max_f32 v29, 0, v29\n"
"v_mfma_f32_16x16x32_bf16 v[40:43], a[224:227], v[88:91], v[40:43]\n"
"v_add_f32 v29, 0x3727c5ac, v29\n"
"v_mfma_f32_16x16x32_bf16 v[48:51], a[228:231], v[88:91], v[48:51]\n"
"v_rsq_f32 v29, v29\n"
"v_mfma_f32_16x16x32_bf16 v[64:67], a[232:235], v[88:91], v[64:67]\n"
"v_add_f32 v32, v32, v34\n"
"s_waitcnt lgkmcnt(2)\n"
"v_mfma_f32_16x16x32_bf16 v[36:39], v[160:163], v[68:71], v[36:39]\n"
"ds_read_b128 v[84:87], v4 offset:34816\n"
"v_add_f32 v33, v33, v35\n"
"v_mfma_f32_16x16x32_bf16 v[44:47], v[164:167], v[68:71], v[44:47]\n"
"ds_read_b128 v[88:91], v4 offset:51200\n"
"v_permlane16_swap_b32 v32, v33\n"
"v_mfma_f32_16x16x32_bf16 v[60:63], v[168:171], v[68:71], v[60:63]\n"
"v_add_f32 v32, v32, v33\n"
"v_mfma_f32_16x16x32_bf16 v[40:43], v[160:163], v[72:75], v[40:43]\n"
"v_mov_b32 v33, v32\n"
"v_mfma_f32_16x16x32_bf16 v[48:51], v[164:167], v[72:75], v[48:51]\n"
"s_nop 0\n"
"v_permlane32_swap_b32 v32, v33\n"
"v_mfma_f32_16x16x32_bf16 v[64:67], v[168:171], v[72:75], v[64:67]\n"
"v_add_f32 v32, v32, v33\n"
"s_waitcnt lgkmcnt(2)\n"
"v_mfma_f32_16x16x32_bf16 v[36:39], v[208:211], v[76:79], v[36:39]\n"
"ds_read_b128 v[68:71], v5 offset:33792\n"
"v_mov_b32 v33, v32\n"
"v_mfma_f32_16x16x32_bf16 v[44:47], v[212:215], v[76:79], v[44:47]\n"
"ds_read_b128 v[72:75], v5 offset:50176\n"
"v_permlane16_swap_b32 v32, v33\n"
"v_mfma_f32_16x16x32_bf16 v[60:63], v[216:219], v[76:79], v[60:63]\n"
"v_mul_f32 v9, 0xbb000000, v32\n"
"v_mfma_f32_16x16x32_bf16 v[40:43], v[208:211], v[80:83], v[40:43]\n"
"v_mul_f32 v34, v9, v9\n"
"v_mfma_f32_16x16x32_bf16 v[48:51], v[212:215], v[80:83], v[48:51]\n"
"v_fma_f32 v33, v33, s37, -v34\n"
"v_mfma_f32_16x16x32_bf16 v[64:67], v[216:219], v[80:83], v[64:67]\n"
"v_max_f32 v33, 0, v33\n"
"s_waitcnt lgkmcnt(2)\n"
"v_mfma_f32_16x16x32_bf16 v[36:39], a[200:203], v[84:87], v[36:39]\n"
"ds_read_b128 v[76:79], v6 offset:33792\n"
"v_add_f32 v33, 0x3727c5ac, v33\n"
"v_mfma_f32_16x16x32_bf16 v[44:47], a[204:207], v[84:87], v[44:47]\n"
"ds_read_b128 v[80:83], v6 offset:50176\n"
"v_rsq_f32 v33, v33\n"
"v_mfma_f32_16x16x32_bf16 v[60:63], a[208:211], v[84:87], v[60:63]\n"
"s_cmp_lt_u32 s7, 2\n"
"s_cselect_b64 vcc, -1, 0\n"
"v_cndmask_b32 v29, v33, v29, vcc\n"
"v_mfma_f32_16x16x32_bf16 v[40:43], a[200:203], v[88:91], v[40:43]\n"
"ds_bpermute_b32 v126, v12, v29\n"
"v_mfma_f32_16x16x32_bf16 v[48:51], a[204:207], v[88:91], v[48:51]\n"
"v_mfma_f32_16x16x32_bf16 v[64:67], a[208:211], v[88:91], v[64:67]\n"
"s_waitcnt lgkmcnt(3)\n"
"v_mfma_f32_16x16x32_bf16 v[36:39], a[236:239], v[68:71], v[36:39]\n"
"ds_read_b128 v[84:87], v7 offset:33792\n"
"v_mfma_f32_16x16x32_bf16 v[44:47], a[240:243], v[68:71], v[44:47]\n"
"ds_read_b128 v[88:91], v7 offset:50176\n"
"v_mfma_f32_16x16x32_bf16 v[60:63], a[244:247], v[68:71], v[60:63]\n"
"v_mfma_f32_16x16x32_bf16 v[40:43], a[236:239], v[72:75], v[40:43]\n"
"v_mfma_f32_16x16x32_bf16 v[48:51], a[240:243], v[72:75], v[48:51]\n"
"v_mfma_f32_16x16x32_bf16 v[64:67], a[244:247], v[72:75], v[64:67]\n"
"s_waitcnt lgkmcnt(3)\n"
"v_mfma_f32_16x16x32_bf16 v[36:39], v[172:175], v[76:79], v[36:39]\n"
"ds_read_b128 v[68:71], v4 offset:35840\n"
"v_mfma_f32_16x16x32_bf16 v[44:47], v[176:179], v[76:79], v[44:47]\n"
"ds_read_b128 v[72:75], v4 offset:52224\n"
"v_add_u32 v141, s73, v142\n"
"ds_write_b128 v141, v[20:23] offset:16384\n"
"ds_write_b128 v141, v[24:27] offset:17408\n"
"ds_write_b128 v141, v[56:59] offset:18432\n"
"v_mfma_f32_16x16x32_bf16 v[60:63], v[180:183], v[76:79], v[60:63]\n"
"buffer_load_dwordx4 v[0:3], v142, s[8:11], s85 offen sc1\n"
"v_mfma_f32_16x16x32_bf16 v[40:43], v[172:175], v[80:83], v[40:43]\n"
"v_mfma_f32_16x16x32_bf16 v[48:51], v[176:179], v[80:83], v[48:51]\n"
"v_mfma_f32_16x16x32_bf16 v[64:67], v[180:183], v[80:83], v[64:67]\n"
"buffer_load_dwordx4 v[16:19], v142, s[8:11], s85 offen offset:1024 sc1\n"
"s_waitcnt lgkmcnt(5)\n"
"v_mfma_f32_16x16x32_bf16 v[36:39], v[220:223], v[84:87], v[36:39]\n"
"ds_read_b128 v[76:79], v5 offset:34816\n"
"v_mfma_f32_16x16x32_bf16 v[44:47], v[224:227], v[84:87], v[44:47]\n"
"ds_read_b128 v[80:83], v5 offset:51200\n"
"v_mfma_f32_16x16x32_bf16 v[60:63], v[228:231], v[84:87], v[60:63]\n"
"buffer_load_dwordx4 v[92:95], v142, s[8:11], s85 offen offset:2048 sc1\n"
"v_mfma_f32_16x16x32_bf16 v[40:43], v[220:223], v[88:91], v[40:43]\n"
"v_mfma_f32_16x16x32_bf16 v[48:51], v[224:227], v[88:91], v[48:51]\n"
"v_mfma_f32_16x16x32_bf16 v[64:67], v[228:231], v[88:91], v[64:67]\n"
"s_waitcnt lgkmcnt(5)\n"
"v_mfma_f32_16x16x32_bf16 v[36:39], a[212:215], v[68:71], v[36:39]\n"
"ds_read_b128 v[84:87], v6 offset:34816\n"
"buffer_load_dwordx4 v[96:99], v142, s[8:11], s85 offen offset:3072 sc1\n"
"v_mfma_f32_16x16x32_bf16 v[44:47], a[216:219], v[68:71], v[44:47]\n"
"ds_read_b128 v[88:91], v6 offset:51200\n"
"v_mfma_f32_16x16x32_bf16 v[60:63], a[220:223], v[68:71], v[60:63]\n"
"v_mfma_f32_16x16x32_bf16 v[40:43], a[212:215], v[72:75], v[40:43]\n"
"buffer_load_dwordx4 v[100:103], v142, s[8:11], s88 offen sc1\n"
"v_mfma_f32_16x16x32_bf16 v[48:51], a[216:219], v[72:75], v[48:51]\n"
"v_mfma_f32_16x16x32_bf16 v[64:67], a[220:223], v[72:75], v[64:67]\n"
"s_waitcnt lgkmcnt(2)\n"
"v_mfma_f32_16x16x32_bf16 v[36:39], a[248:251], v[76:79], v[36:39]\n"
"ds_read_b128 v[68:71], v7 offset:34816\n"
"v_mfma_f32_16x16x32_bf16 v[44:47], a[252:255], v[76:79], v[44:47]\n"
"ds_read_b128 v[72:75], v7 offset:51200\n"
"buffer_load_dwordx4 v[104:107], v142, s[8:11], s88 offen offset:1024 sc1\n"
"v_mfma_f32_16x16x32_bf16 v[60:63], v[144:147], v[76:79], v[60:63]\n"
"v_mfma_f32_16x16x32_bf16 v[40:43], a[248:251], v[80:83], v[40:43]\n"
"v_mfma_f32_16x16x32_bf16 v[48:51], a[252:255], v[80:83], v[48:51]\n"
"buffer_load_dwordx4 v[108:111], v142, s[8:11], s88 offen offset:2048 sc1\n"
"v_mfma_f32_16x16x32_bf16 v[64:67], v[144:147], v[80:83], v[64:67]\n"
"s_waitcnt lgkmcnt(2)\n"
"v_mfma_f32_16x16x32_bf16 v[36:39], v[184:187], v[84:87], v[36:39]\n"
"ds_read_b128 v[76:79], v5 offset:35840\n"
"v_mfma_f32_16x16x32_bf16 v[44:47], v[188:191], v[84:87], v[44:47]\n"
"ds_read_b128 v[80:83], v5 offset:52224\n"
"v_mfma_f32_16x16x32_bf16 v[60:63], v[192:195], v[84:87], v[60:63]\n"
"buffer_load_dwordx4 v[112:115], v142, s[8:11], s88 offen offset:3072 sc1\n"
"v_mfma_f32_16x16x32_bf16 v[40:43], v[184:187], v[88:91], v[40:43]\n"
"v_mfma_f32_16x16x32_bf16 v[48:51], v[188:191], v[88:91], v[48:51]\n"
"v_mfma_f32_16x16x32_bf16 v[64:67], v[192:195], v[88:91], v[64:67]\n"
"buffer_load_dwordx4 v[116:119], v142, s[8:11], s89 offen sc1\n"
"s_waitcnt lgkmcnt(2)\n"
"v_mfma_f32_16x16x32_bf16 v[36:39], v[232:235], v[68:71], v[36:39]\n"
"ds_read_b128 v[84:87], v6 offset:35840\n"
"v_mfma_f32_16x16x32_bf16 v[44:47], v[236:239], v[68:71], v[44:47]\n"
"ds_read_b128 v[88:91], v6 offset:52224\n"
"v_mfma_f32_16x16x32_bf16 v[60:63], v[240:243], v[68:71], v[60:63]\n"
"v_mfma_f32_16x16x32_bf16 v[40:43], v[232:235], v[72:75], v[40:43]\n"
"buffer_load_dwordx4 v[120:123], v142, s[8:11], s89 offen offset:1024 sc1\n"
"v_mfma_f32_16x16x32_bf16 v[48:51], v[236:239], v[72:75], v[48:51]\n"
"v_mfma_f32_16x16x32_bf16 v[64:67], v[240:243], v[72:75], v[64:67]\n"
"s_waitcnt lgkmcnt(2)\n"
"v_mfma_f32_16x16x32_bf16 v[36:39], v[148:151], v[76:79], v[36:39]\n"
"ds_read_b128 v[68:71], v7 offset:35840\n"
"buffer_load_dwordx4 v[20:23], v142, s[8:11], s89 offen offset:2048 sc1\n"
"v_mfma_f32_16x16x32_bf16 v[44:47], v[152:155], v[76:79], v[44:47]\n"
"ds_read_b128 v[72:75], v7 offset:52224\n"
"v_mfma_f32_16x16x32_bf16 v[60:63], v[156:159], v[76:79], v[60:63]\n"
"v_mfma_f32_16x16x32_bf16 v[40:43], v[148:151], v[80:83], v[40:43]\n"
"v_mfma_f32_16x16x32_bf16 v[48:51], v[152:155], v[80:83], v[48:51]\n"
"buffer_load_dwordx4 v[24:27], v142, s[8:11], s89 offen offset:3072 sc1\n"
"v_mfma_f32_16x16x32_bf16 v[64:67], v[156:159], v[80:83], v[64:67]\n"
"s_waitcnt lgkmcnt(2)\n"
"v_mfma_f32_16x16x32_bf16 v[36:39], v[196:199], v[84:87], v[36:39]\n"
"v_mfma_f32_16x16x32_bf16 v[44:47], v[200:203], v[84:87], v[44:47]\n"
"buffer_load_dwordx4 v[56:59], v142, s[8:11], s90 offen sc1\n"
"ds_read_b128 v[76:79], v10\n"
"v_mfma_f32_16x16x32_bf16 v[60:63], v[204:207], v[84:87], v[60:63]\n"
"v_mfma_f32_16x16x32_bf16 v[40:43], v[196:199], v[88:91], v[40:43]\n"
"ds_read_b128 v[80:83], v10 offset:64\n"
"v_mfma_f32_16x16x32_bf16 v[48:51], v[200:203], v[88:91], v[48:51]\n"
"v_mfma_f32_16x16x32_bf16 v[64:67], v[204:207], v[88:91], v[64:67]\n"
"buffer_load_dwordx4 v[28:31], v142, s[8:11], s90 offen offset:1024 sc1\n"
"s_waitcnt lgkmcnt(2)\n"
"v_mfma_f32_16x16x32_bf16 v[36:39], v[244:247], v[68:71], v[36:39]\n"
"ds_read_b128 v[84:87], v10 offset:128\n"
"v_mfma_f32_16x16x32_bf16 v[44:47], v[248:251], v[68:71], v[44:47]\n"
"v_mfma_f32_16x16x32_bf16 v[60:63], v[252:255], v[68:71], v[60:63]\n"
"buffer_load_dwordx4 v[32:35], v142, s[8:11], s90 offen offset:2048 sc1\n"
"v_mfma_f32_16x16x32_bf16 v[40:43], v[244:247], v[72:75], v[40:43]\n"
"v_mfma_f32_16x16x32_bf16 v[48:51], v[248:251], v[72:75], v[48:51]\n"
"v_mfma_f32_16x16x32_bf16 v[64:67], v[252:255], v[72:75], v[64:67]\n"
"s_nop 5\n"
"v_cmp_u_f32 vcc, v36, v40\n"
"s_cbranch_vccnz Lslowx11\n"
"Lbackx11:\n"
"s_waitcnt lgkmcnt(2)\n"
"v_pk_fma_f32 v[36:37], v[76:77], v[8:9], v[36:37] op_sel:[0,0,0] op_sel_hi:[1,0,1]\n"
"v_pk_fma_f32 v[38:39], v[78:79], v[8:9], v[38:39] op_sel:[0,0,0] op_sel_hi:[1,0,1]\n"
"v_pk_fma_f32 v[40:41], v[76:77], v[8:9], v[40:41] op_sel:[0,1,0] op_sel_hi:[1,1,1]\n"
"v_pk_fma_f32 v[42:43], v[78:79], v[8:9], v[42:43] op_sel:[0,1,0] op_sel_hi:[1,1,1]\n"
"v_cvt_pk_bf16_f32 v88, -v36, -v37\n"
"v_cvt_pk_bf16_f32 v89, -v38, -v39\n"
"v_cvt_pk_bf16_f32 v90, -v40, -v41\n"
"v_cvt_pk_bf16_f32 v91, -v42, -v43\n"
"v_pk_max_u16 v88, v88, s38\n"
"v_pk_max_u16 v90, v90, s38\n"
"v_pk_max_u16 v89, v89, s38\n"
"v_pk_max_u16 v91, v91, s38\n"
"s_add_u32 s53, s33, 0x16000\n"
"v_permlane32_swap_b32 v88, v90\n"
"v_permlane32_swap_b32 v89, v91\n"
"v_xor_b32 v88, v14, v88\n"
"buffer_store_dwordx4 v[88:91], v13, s[8:11], s53 offen sc1\n"
"s_waitcnt lgkmcnt(1)\n"
"v_pk_fma_f32 v[44:45], v[80:81], v[8:9], v[44:45] op_sel:[0,0,0] op_sel_hi:[1,0,1]\n"
"v_pk_fma_f32 v[46:47], v[82:83], v[8:9], v[46:47] op_sel:[0,0,0] op_sel_hi:[1,0,1]\n"
"v_pk_fma_f32 v[48:49], v[80:81], v[8:9], v[48:49] op_sel:[0,1,0] op_sel_hi:[1,1,1]\n"
"v_pk_fma_f32 v[50:51], v[82:83], v[8:9], v[50:51] op_sel:[0,1,0] op_sel_hi:[1,1,1]\n"
"v_cvt_pk_bf16_f32 v68, -v44, -v45\n"
"v_cvt_pk_bf16_f32 v69, -v46, -v47\n"
"v_cvt_pk_bf16_f32 v70, -v48, -v49\n"
"v_cvt_pk_bf16_f32 v71, -v50, -v51\n"
"v_pk_max_u16 v68, v68, s38\n"
"v_pk_max_u16 v70, v70, s38\n"
"v_pk_max_u16 v69, v69, s38\n"
"v_pk_max_u16 v71, v71, s38\n"
"s_add_u32 s53, s33, 0x18000\n"
"v_permlane32_swap_b32 v68, v70\n"
"v_permlane32_swap_b32 v69, v71\n"
"v_xor_b32 v68, v14, v68\n"
"buffer_store_dwordx4 v[68:71], v13, s[8:11], s53 offen sc1\n"
"s_waitcnt lgkmcnt(0)\n"
"v_pk_fma_f32 v[60:61], v[84:85], v[8:9], v[60:61] op_sel:[0,0,0] op_sel_hi:[1,0,1]\n"
"v_pk_fma_f32 v[62:63], v[86:87], v[8:9], v[62:63] op_sel:[0,0,0] op_sel_hi:[1,0,1]\n"
"v_pk_fma_f32 v[64:65], v[84:85], v[8:9], v[64:65] op_sel:[0,1,0] op_sel_hi:[1,1,1]\n"
"v_pk_fma_f32 v[66:67], v[86:87], v[8:9], v[66:67] op_sel:[0,1,0] op_sel_hi:[1,1,1]\n"
"v_cvt_pk_bf16_f32 v88, -v60, -v61\n"
"v_cvt_pk_bf16_f32 v89, -v62, -v63\n"
"v_cvt_pk_bf16_f32 v90, -v64, -v65\n"
"v_cvt_pk_bf16_f32 v91, -v66, -v67\n"
"v_pk_max_u16 v88, v88, s38\n"
"v_pk_max_u16 v90, v90, s38\n"
"v_pk_max_u16 v89, v89, s38\n"
"v_pk_max_u16 v91, v91, s38\n"
"s_add_u32 s53, s34, s35\n"
"v_permlane32_swap_b32 v88, v90\n"
"v_permlane32_swap_b32 v89, v91\n"
"v_xor_b32 v88, v14, v88\n"
"buffer_store_dwordx4 v[88:91], v13, s[8:11], s53 offen sc1\n"
"v_add_u32 v141, s73, v142\n"
"ds_write_b128 v141, v[4:7] offset:12288\n"
"ds_write_b128 v141, v[8:11] offset:13312\n"
"ds_write_b128 v141, v[12:15] offset:14336\n"
"s_lshl_b32 s53, s7, 13\n"
"v_add_u32 v125, 0x16000, v142\n"
"v_add_u32 v133, s53, v142\n"
"v_lshlrev_b32 v141, 4, v139\n"
"v_lshl_add_u32 v124, v138, 10, v141\n"
"v_and_b32 v141, 1, v139\n"
"v_cmp_eq_u32 s[4:5], 1, v141\n"
"s_waitcnt vmcnt(17)\n"
"v_xor_b32 v0, v132, v0\n"
"s_waitcnt vmcnt(16)\n"
"v_xor_b32 v16, v132, v16\n"
"s_waitcnt vmcnt(15)\n"
"v_xor_b32 v92, v132, v92\n"
"s_waitcnt vmcnt(14)\n"
"v_xor_b32 v96, v132, v96\n"
"v_bitop3_b32 v134, v0, v1, v2 bitop3:0x80\n"
"v_bitop3_b32 v134, v134, v3, v16 bitop3:0x80\n"
"v_bitop3_b32 v134, v134, v17, v18 bitop3:0x80\n"
"v_bitop3_b32 v134, v134, v19, v92 bitop3:0x80\n"
"v_bitop3_b32 v134, v134, v93, v94 bitop3:0x80\n"
"v_bitop3_b32 v134, v134, v95, v96 bitop3:0x80\n"
"v_bitop3_b32 v134, v134, v97, v98 bitop3:0x80\n"
"v_bitop3_b32 v134, v134, v99, v99 bitop3:0x80\n"
"v_and_b32 v134, s38, v134\n"
"v_cmp_ne_u32 vcc, s38, v134\n"
"s_cbranch_vccnz Lslow22_h10\n"
"Lback22_h10:\n"
"v_mfma_f32_32x32x16_bf16 v[36:51], a[0:3], v[0:3], 0\n"
"buffer_load_dwordx4 v[76:79], v142, s[8:11], s90 offen offset:3072 sc1\n"
"v_mfma_f32_32x32x16_bf16 v[60:75], a[4:7], v[0:3], 0\n"
"v_mfma_f32_32x32x16_bf16 v[36:51], a[8:11], v[16:19], v[36:51]\n"
"buffer_load_dwordx4 v[80:83], v142, s[8:11], s91 offen sc1\n"
"v_mfma_f32_32x32x16_bf16 v[60:75], a[12:15], v[16:19], v[60:75]\n"
"s_waitcnt vmcnt(15)\n"
"v_xor_b32 v100, v132, v100\n"
"s_waitcnt vmcnt(14)\n"
"v_xor_b32 v104, v132, v104\n"
"s_waitcnt vmcnt(13)\n"
"v_xor_b32 v108, v132, v108\n"
"s_waitcnt vmcnt(12)\n"
"v_xor_b32 v112, v132, v112\n"
"v_mfma_f32_32x32x16_bf16 v[36:51], a[16:19], v[92:95], v[36:51]\n"
"buffer_load_dwordx4 v[84:87], v142, s[8:11], s91 offen offset:1024 sc1\n"
"v_bitop3_b32 v134, v100, v101, v102 bitop3:0x80\n"
"v_bitop3_b32 v134, v134, v103, v104 bitop3:0x80\n"
"v_bitop3_b32 v134, v134, v105, v106 bitop3:0x80\n"
"v_mfma_f32_32x32x16_bf16 v[60:75], a[20:23], v[92:95], v[60:75]\n"
"v_bitop3_b32 v134, v134, v107, v108 bitop3:0x80\n"
"v_bitop3_b32 v134, v134, v109, v110 bitop3:0x80\n"
"v_bitop3_b32 v134, v134, v111, v112 bitop3:0x80\n"
"v_bitop3_b32 v134, v134, v113, v114 bitop3:0x80\n"
"v_mfma_f32_32x32x16_bf16 v[36:51], a[24:27], v[96:99], v[36:51]\n"
"buffer_load_dwordx4 v[88:91], v142, s[8:11], s91 offen offset:2048 sc1\n"
"v_bitop3_b32 v134, v134, v115, v115 bitop3:0x80\n"
"v_and_b32 v134, s38, v134\n"
"v_cmp_ne_u32 s[66:67], s38, v134\n"
"v_mfma_f32_32x32x16_bf16 v[60:75], a[28:31], v[96:99], v[60:75]\n"
"s_cmp_lg_u64 s[66:67], 0\n"
"s_cbranch_scc1 Lslow23_h11\n"
"Lback23_h11:\n"
"v_mfma_f32_32x32x16_bf16 v[36:51], a[32:35], v[100:103], v[36:51]\n"
"buffer_load_dwordx4 v[4:7], v142, s[8:11], s91 offen offset:3072 sc1\n"
"v_mfma_f32_32x32x16_bf16 v[60:75], a[36:39], v[100:103], v[60:75]\n"
"v_mfma_f32_32x32x16_bf16 v[36:51], a[40:43], v[104:107], v[36:51]\n"
"buffer_load_dwordx4 v[8:11], v142, s[8:11], s92 offen sc1\n"
"v_mfma_f32_32x32x16_bf16 v[60:75], a[44:47], v[104:107], v[60:75]\n"
"s_waitcnt vmcnt(15)\n"
"v_xor_b32 v116, v132, v116\n"
"s_waitcnt vmcnt(14)\n"
"v_xor_b32 v120, v132, v120\n"
"s_waitcnt vmcnt(13)\n"
"v_xor_b32 v20, v132, v20\n"
"s_waitcnt vmcnt(12)\n"
"v_xor_b32 v24, v132, v24\n"
"v_mfma_f32_32x32x16_bf16 v[36:51], a[48:51], v[108:111], v[36:51]\n"
"buffer_load_dwordx4 v[12:15], v142, s[8:11], s92 offen offset:1024 sc1\n"
"v_bitop3_b32 v134, v116, v117, v118 bitop3:0x80\n"
"v_bitop3_b32 v134, v134, v119, v120 bitop3:0x80\n"
"v_bitop3_b32 v134, v134, v121, v122 bitop3:0x80\n"
"v_mfma_f32_32x32x16_bf16 v[60:75], a[52:55], v[108:111], v[60:75]\n"
"v_bitop3_b32 v134, v134, v123, v20 bitop3:0x80\n"
"v_bitop3_b32 v134, v134, v21, v22 bitop3:0x80\n"
"v_bitop3_b32 v134, v134, v23, v24 bitop3:0x80\n"
"v_bitop3_b32 v134, v134, v25, v26 bitop3:0x80\n"
"v_mfma_f32_32x32x16_bf16 v[36:51], a[56:59], v[112:115], v[36:51]\n"
"v_bitop3_b32 v134, v134, v27, v27 bitop3:0x80\n"
"v_and_b32 v134, s38, v134\n"
"v_cmp_ne_u32 s[66:67], s38, v134\n"
"v_mfma_f32_32x32x16_bf16 v[60:75], a[60:63], v[112:115], v[60:75]\n"
"s_cmp_lg_u64 s[66:67], 0\n"
"s_cbranch_scc1 Lslow24_h12\n"
"Lback24_h12:\n"
"v_mfma_f32_32x32x16_bf16 v[36:51], a[64:67], v[116:119], v[36:51]\n"
"v_mfma_f32_32x32x16_bf16 v[60:75], a[68:71], v[116:119], v[60:75]\n"
"v_mfma_f32_32x32x16_bf16 v[36:51], a[72:75], v[120:123], v[36:51]\n"
"v_mfma_f32_32x32x16_bf16 v[60:75], a[76:79], v[120:123], v[60:75]\n"
"s_waitcnt vmcnt(12)\n"
"v_xor_b32 v56, v132, v56\n"
"s_waitcnt vmcnt(11)\n"
"v_xor_b32 v28, v132, v28\n"
"s_waitcnt vmcnt(10)\n"
"v_xor_b32 v32, v132, v32\n"
"s_waitcnt vmcnt(6)\n"
"v_xor_b32 v76, v132, v76\n"
"v_mfma_f32_32x32x16_bf16 v[36:51], a[80:83], v[20:23], v[36:51]\n"
"v_bitop3_b32 v134, v56, v57, v58 bitop3:0x80\n"
"v_bitop3_b32 v134, v134, v59, v28 bitop3:0x80\n"
"v_bitop3_b32 v134, v134, v29, v30 bitop3:0x80\n"
"v_mfma_f32_32x32x16_bf16 v[60:75], a[84:87], v[20:23], v[60:75]\n"
"v_bitop3_b32 v134, v134, v31, v32 bitop3:0x80\n"
"v_bitop3_b32 v134, v134, v33, v34 bitop3:0x80\n"
"v_bitop3_b32 v134, v134, v35, v76 bitop3:0x80\n"
"v_bitop3_b32 v134, v134, v77, v78 bitop3:0x80\n"
"v_mfma_f32_32x32x16_bf16 v[36:51], a[88:91], v[24:27], v[36:51]\n"
"v_bitop3_b32 v134, v134, v79, v79 bitop3:0x80\n"
"v_and_b32 v134, s38, v134\n"
"v_cmp_ne_u32 s[66:67], s38, v134\n"
"v_mfma_f32_32x32x16_bf16 v[60:75], a[92:95], v[24:27], v[60:75]\n"
"s_cmp_lg_u64 s[66:67], 0\n"
"s_cbranch_scc1 Lslow25_h13\n"
"Lback25_h13:\n"
"v_mfma_f32_32x32x16_bf16 v[36:51], a[96:99], v[56:59], v[36:51]\n"
"buffer_load_dwordx4 v[0:3], v125, s[8:11], s85 offen sc1\n"
"v_mfma_f32_32x32x16_bf16 v[60:75], a[100:103], v[56:59], v[60:75]\n"
"v_mfma_f32_32x32x16_bf16 v[36:51], a[104:107], v[28:31], v[36:51]\n"
"buffer_load_dwordx4 v[16:19], v125, s[8:11], s85 offen offset:1024 sc1\n"
"v_mfma_f32_32x32x16_bf16 v[60:75], a[108:111], v[28:31], v[60:75]\n"
"s_waitcnt vmcnt(7)\n"
"v_xor_b32 v80, v132, v80\n"
"s_waitcnt vmcnt(6)\n"
"v_xor_b32 v84, v132, v84\n"
"s_waitcnt vmcnt(5)\n"
"v_xor_b32 v88, v132, v88\n"
"s_waitcnt vmcnt(4)\n"
"v_xor_b32 v4, v132, v4\n"
"s_waitcnt vmcnt(3)\n"
"v_xor_b32 v8, v132, v8\n"
"v_mfma_f32_32x32x16_bf16 v[36:51], a[112:115], v[32:35], v[36:51]\n"
"buffer_load_dwordx4 v[92:95], v125, s[8:11], s85 offen offset:2048 sc1\n"
"s_waitcnt vmcnt(3)\n"
"v_xor_b32 v12, v132, v12\n"
"v_bitop3_b32 v134, v80, v81, v82 bitop3:0x80\n"
"v_bitop3_b32 v134, v134, v83, v84 bitop3:0x80\n"
"v_bitop3_b32 v134, v134, v85, v86 bitop3:0x80\n"
"v_bitop3_b32 v134, v134, v87, v88 bitop3:0x80\n"
"v_mfma_f32_32x32x16_bf16 v[60:75], a[116:119], v[32:35], v[60:75]\n"
"v_bitop3_b32 v134, v134, v89, v90 bitop3:0x80\n"
"v_bitop3_b32 v134, v134, v91, v4 bitop3:0x80\n"
"v_bitop3_b32 v134, v134, v5, v6 bitop3:0x80\n"
"v_bitop3_b32 v134, v134, v7, v8 bitop3:0x80\n"
"v_bitop3_b32 v134, v134, v9, v10 bitop3:0x80\n"
"v_mfma_f32_32x32x16_bf16 v[36:51], a[120:123], v[76:79], v[36:51]\n"
"buffer_load_dwordx4 v[96:99], v125, s[8:11], s85 offen offset:3072 sc1\n"
"v_bitop3_b32 v134, v134, v11, v12 bitop3:0x80\n"
"v_bitop3_b32 v134, v134, v13, v14 bitop3:0x80\n"
"v_bitop3_b32 v134, v134, v15, v15 bitop3:0x80\n"
"v_and_b32 v134, s38, v134\n"
"v_cmp_ne_u32 s[66:67], s38, v134\n"
"v_mfma_f32_32x32x16_bf16 v[60:75], a[124:127], v[76:79], v[60:75]\n"
"s_cmp_lg_u64 s[66:67], 0\n"
"s_cbranch_scc1 Lslow26_h14\n"
"Lback26_h14:\n"
"v_mfma_f32_32x32x16_bf16 v[36:51], a[128:131], v[80:83], v[36:51]\n"
"buffer_load_dwordx4 v[100:103], v125, s[8:11], s88 offen sc1\n"
"v_mfma_f32_32x32x16_bf16 v[36:51], a[136:139], v[84:87], v[36:51]\n"
"v_mfma_f32_32x32x16_bf16 v[36:51], a[144:147], v[88:91], v[36:51]\n"
"buffer_load_dwordx4 v[104:107], v125, s[8:11], s88 offen offset:1024 sc1\n"
"v_mfma_f32_32x32x16_bf16 v[36:51], a[152:155], v[4:7], v[36:51]\n"
"v_mfma_f32_32x32x16_bf16 v[36:51], a[160:163], v[8:11], v[36:51]\n"
"buffer_load_dwordx4 v[108:111], v125, s[8:11], s88 offen offset:2048 sc1\n"
"v_mfma_f32_32x32x16_bf16 v[36:51], a[168:171], v[12:15], v[36:51]\n"
"v_mfma_f32_32x32x16_bf16 v[60:75], a[132:135], v[80:83], v[60:75]\n"
"buffer_load_dwordx4 v[112:115], v125, s[8:11], s88 offen offset:3072 sc1\n"
"v_mfma_f32_32x32x16_bf16 v[60:75], a[140:143], v[84:87], v[60:75]\n"
"s_waitcnt vmcnt(7)\n"
"v_xor_b32 v0, v132, v0\n"
"s_waitcnt vmcnt(6)\n"
"v_xor_b32 v16, v132, v16\n"
"s_waitcnt vmcnt(5)\n"
"v_xor_b32 v92, v132, v92\n"
"s_waitcnt vmcnt(4)\n"
"v_xor_b32 v96, v132, v96\n"
"v_mfma_f32_32x32x16_bf16 v[60:75], a[148:151], v[88:91], v[60:75]\n"
"ds_write_b128 v133, v[36:39]\n"
"buffer_load_dwordx4 v[76:79], v125, s[8:11], s89 offen sc1\n"
"v_bitop3_b32 v134, v0, v1, v2 bitop3:0x80\n"
"v_bitop3_b32 v134, v134, v3, v16 bitop3:0x80\n"
"v_bitop3_b32 v134, v134, v17, v18 bitop3:0x80\n"
"v_mfma_f32_32x32x16_bf16 v[60:75], a[156:159], v[4:7], v[60:75]\n"
"ds_write_b128 v133, v[40:43] offset:1024\n"
"v_bitop3_b32 v134, v134, v19, v92 bitop3:0x80\n"
"v_bitop3_b32 v134, v134, v93, v94 bitop3:0x80\n"
"v_bitop3_b32 v134, v134, v95, v96 bitop3:0x80\n"
"v_bitop3_b32 v134, v134, v97, v98 bitop3:0x80\n"
"v_mfma_f32_32x32x16_bf16 v[60:75], a[164:167], v[8:11], v[60:75]\n"
"ds_write_b128 v133, v[44:47] offset:2048\n"
"buffer_load_dwordx4 v[80:83], v125, s[8:11], s89 offen offset:1024 sc1\n"
"v_bitop3_b32 v134, v134, v99, v99 bitop3:0x80\n"
"v_and_b32 v134, s38, v134\n"
"v_cmp_ne_u32 s[66:67], s38, v134\n"
"v_mfma_f32_32x32x16_bf16 v[60:75], a[172:175], v[12:15], v[60:75]\n"
"ds_write_b128 v133, v[48:51] offset:3072\n"
"s_cmp_lg_u64 s[66:67], 0\n"
"s_cbranch_scc1 Lslow27_h15\n"
"Lback27_h15:\n"
"v_mfma_f32_32x32x16_bf16 v[36:51], a[0:3], v[0:3], 0\n"
"s_nop 7\n"
"ds_write_b128 v133, v[60:63] offset:4096\n"
"buffer_load_dwordx4 v[84:87], v125, s[8:11], s89 offen offset:2048 sc1\n"
"v_mfma_f32_32x32x16_bf16 v[36:51], a[8:11], v[16:19], v[36:51]\n"
"ds_write_b128 v133, v[64:67] offset:5120\n"
"buffer_load_dwordx4 v[88:91], v125, s[8:11], s89 offen offset:3072 sc1\n"
"v_mfma_f32_32x32x16_bf16 v[36:51], a[16:19], v[92:95], v[36:51]\n"
"ds_write_b128 v133, v[68:71] offset:6144\n"
"buffer_load_dwordx4 v[4:7], v125, s[8:11], s90 offen sc1\n"
"v_mfma_f32_32x32x16_bf16 v[36:51], a[24:27], v[96:99], v[36:51]\n"
"ds_write_b128 v133, v[72:75] offset:7168\n"
"buffer_load_dwordx4 v[8:11], v125, s[8:11], s90 offen offset:1024 sc1\n"
"s_waitcnt vmcnt(9)\n"
"v_xor_b32 v100, v132, v100\n"
"s_waitcnt vmcnt(8)\n"
"v_xor_b32 v104, v132, v104\n"
"s_waitcnt vmcnt(7)\n"
"v_xor_b32 v108, v132, v108\n"
"s_waitcnt vmcnt(6)\n"
"v_xor_b32 v112, v132, v112\n"
"v_mfma_f32_32x32x16_bf16 v[60:75], a[4:7], v[0:3], 0\n"
"buffer_load_dwordx4 v[12:15], v125, s[8:11], s90 offen offset:2048 sc1\n"
"v_bitop3_b32 v134, v100, v101, v102 bitop3:0x80\n"
"v_bitop3_b32 v134, v134, v103, v104 bitop3:0x80\n"
"v_bitop3_b32 v134, v134, v105, v106 bitop3:0x80\n"
"v_add_u32 v141, s73, v142\n"
"ds_read_b128 v[20:23], v141 offset:16384\n"
"ds_read_b128 v[24:27], v141 offset:17408\n"
"ds_read_b128 v[56:59], v141 offset:18432\n"
"v_mfma_f32_32x32x16_bf16 v[60:75], a[12:15], v[16:19], v[60:75]\n"
"buffer_load_dwordx4 v[0:3], v125, s[8:11], s90 offen offset:3072 sc1\n"
"v_bitop3_b32 v134, v134, v107, v108 bitop3:0x80\n"
"v_bitop3_b32 v134, v134, v109, v110 bitop3:0x80\n"
"v_bitop3_b32 v134, v134, v111, v112 bitop3:0x80\n"
"v_bitop3_b32 v134, v134, v113, v114 bitop3:0x80\n"
"s_waitcnt lgkmcnt(0)\n"
"s_barrier\n"
"v_mfma_f32_32x32x16_bf16 v[60:75], a[20:23], v[92:95], v[60:75]\n"
"buffer_load_dwordx4 v[16:19], v125, s[8:11], s91 offen sc1\n"
"v_bitop3_b32 v134, v134, v115, v115 bitop3:0x80\n"
"v_and_b32 v134, s38, v134\n"
"v_cmp_ne_u32 s[66:67], s38, v134\n"
"ds_read_b128 v[116:119], v135\n"
"ds_read_b128 v[120:123], v135 offset:1024\n"
"ds_read_b128 v[28:31], v135 offset:8192\n"
"ds_read_b128 v[32:35], v135 offset:9216\n"
"v_mfma_f32_32x32x16_bf16 v[60:75], a[28:31], v[96:99], v[60:75]\n"
"buffer_load_dwordx4 v[92:95], v125, s[8:11], s91 offen offset:1024 sc1\n"
"s_cmp_lg_u64 s[66:67], 0\n"
"s_cbranch_scc1 Lslow28_h16\n"
"Lback28_h16:\n"
"v_mfma_f32_32x32x16_bf16 v[36:51], a[32:35], v[100:103], v[36:51]\n"
"buffer_load_dwordx4 v[96:99], v125, s[8:11], s91 offen offset:2048 sc1\n"
"s_waitcnt lgkmcnt(1)\n"
"v_add_f32 v116, v116, v28\n"
"v_add_f32 v117, v117, v29\n"
"v_mfma_f32_32x32x16_bf16 v[60:75], a[36:39], v[100:103], v[60:75]\n"
"buffer_load_dwordx4 v[100:103], v125, s[8:11], s91 offen offset:3072 sc1\n"
"v_add_f32 v118, v118, v30\n"
"v_add_f32 v119, v119, v31\n"
"s_waitcnt lgkmcnt(0)\n"
"v_add_f32 v120, v120, v32\n"
"v_add_f32 v121, v121, v33\n"
"v_mfma_f32_32x32x16_bf16 v[36:51], a[40:43], v[104:107], v[36:51]\n"
"v_add_f32 v122, v122, v34\n"
"v_add_f32 v123, v123, v35\n"
"ds_read_b128 v[28:31], v135 offset:16384\n"
"ds_read_b128 v[32:35], v135 offset:17408\n"
"v_mfma_f32_32x32x16_bf16 v[60:75], a[44:47], v[104:107], v[60:75]\n"
"buffer_load_dwordx4 v[104:107], v125, s[8:11], s92 offen sc1\n"
"s_waitcnt vmcnt(12)\n"
"v_xor_b32 v76, v132, v76\n"
"s_waitcnt vmcnt(11)\n"
"v_xor_b32 v80, v132, v80\n"
"s_waitcnt vmcnt(10)\n"
"v_xor_b32 v84, v132, v84\n"
"s_waitcnt vmcnt(9)\n"
"v_xor_b32 v88, v132, v88\n"
"v_mfma_f32_32x32x16_bf16 v[36:51], a[48:51], v[108:111], v[36:51]\n"
"v_bitop3_b32 v134, v76, v77, v78 bitop3:0x80\n"
"v_bitop3_b32 v134, v134, v79, v80 bitop3:0x80\n"
"v_bitop3_b32 v134, v134, v81, v82 bitop3:0x80\n"
"v_mfma_f32_32x32x16_bf16 v[60:75], a[52:55], v[108:111], v[60:75]\n"
"buffer_load_dwordx4 v[108:111], v125, s[8:11], s92 offen offset:1024 sc1\n"
"v_bitop3_b32 v134, v134, v83, v84 bitop3:0x80\n"
"v_bitop3_b32 v134, v134, v85, v86 bitop3:0x80\n"
"v_bitop3_b32 v134, v134, v87, v88 bitop3:0x80\n"
"v_bitop3_b32 v134, v134, v89, v90 bitop3:0x80\n"
"s_waitcnt lgkmcnt(1)\n"
"v_add_f32 v116, v116, v28\n"
"v_add_f32 v117, v117, v29\n"
"v_add_f32 v118, v118, v30\n"
"v_add_f32 v119, v119, v31\n"
"v_mfma_f32_32x32x16_bf16 v[36:51], a[56:59], v[112:115], v[36:51]\n"
"v_bitop3_b32 v134, v134, v91, v91 bitop3:0x80\n"
"v_and_b32 v134, s38, v134\n"
"v_cmp_ne_u32 s[66:67], s38, v134\n"
"s_waitcnt lgkmcnt(0)\n"
"v_add_f32 v120, v120, v32\n"
"v_add_f32 v121, v121, v33\n"
"v_add_f32 v122, v122, v34\n"
"v_add_f32 v123, v123, v35\n"
"v_mfma_f32_32x32x16_bf16 v[60:75], a[60:63], v[112:115], v[60:75]\n"
"s_cmp_lg_u64 s[66:67], 0\n"
"s_cbranch_scc1 Lslow29_h17\n"
"Lback29_h17:\n"
"ds_read_b128 v[28:31], v135 offset:24576\n"
"ds_read_b128 v[32:35], v135 offset:25600\n"
"v_mfma_f32_32x32x16_bf16 v[36:51], a[64:67], v[76:79], v[36:51]\n"
"v_mfma_f32_32x32x16_bf16 v[60:75], a[68:71], v[76:79], v[60:75]\n"
"s_waitcnt lgkmcnt(1)\n"
"v_add_f32 v116, v116, v28\n"
"v_add_f32 v117, v117, v29\n"
"v_add_f32 v118, v118, v30\n"
"v_mfma_f32_32x32x16_bf16 v[36:51], a[72:75], v[80:83], v[36:51]\n"
"v_add_f32 v119, v119, v31\n"
"s_waitcnt lgkmcnt(0)\n"
"v_add_f32 v120, v120, v32\n"
"v_add_f32 v121, v121, v33\n"
"v_add_f32 v122, v122, v34\n"
"v_mfma_f32_32x32x16_bf16 v[60:75], a[76:79], v[80:83], v[60:75]\n"
"s_waitcnt vmcnt(9)\n"
"v_xor_b32 v4, v132, v4\n"
"s_waitcnt vmcnt(8)\n"
"v_xor_b32 v8, v132, v8\n"
"s_waitcnt vmcnt(7)\n"
"v_xor_b32 v12, v132, v12\n"
"s_waitcnt vmcnt(6)\n"
"v_xor_b32 v0, v132, v0\n"
"v_add_f32 v123, v123, v35\n"
"v_lshlrev_b32 v28, 16, v52\n"
"v_and_b32 v29, s61, v52\n"
"v_mfma_f32_32x32x16_bf16 v[36:51], a[80:83], v[84:87], v[36:51]\n"
"v_bitop3_b32 v134, v4, v5, v6 bitop3:0x80\n"
"v_bitop3_b32 v134, v134, v7, v8 bitop3:0x80\n"
"v_bitop3_b32 v134, v134, v9, v10 bitop3:0x80\n"
"v_lshlrev_b32 v30, 16, v53\n"
"v_and_b32 v31, s61, v53\n"
"v_lshlrev_b32 v32, 16, v54\n"
"v_and_b32 v33, s61, v54\n"
"v_mfma_f32_32x32x16_bf16 v[60:75], a[84:87], v[84:87], v[60:75]\n"
"v_bitop3_b32 v134, v134, v11, v12 bitop3:0x80\n"
"v_bitop3_b32 v134, v134, v13, v14 bitop3:0x80\n"
"v_bitop3_b32 v134, v134, v15, v0 bitop3:0x80\n"
"v_bitop3_b32 v134, v134, v1, v2 bitop3:0x80\n"
"v_lshlrev_b32 v34, 16, v55\n"
"v_and_b32 v35, s61, v55\n"
"v_fma_f32 v28, v116, -v127, v28\n"
"v_fma_f32 v29, v117, -v127, v29\n"
"v_mfma_f32_32x32x16_bf16 v[36:51], a[88:91], v[88:91], v[36:51]\n"
"v_bitop3_b32 v134, v134, v3, v3 bitop3:0x80\n"
"v_and_b32 v134, s38, v134\n"
"v_cmp_ne_u32 s[66:67], s38, v134\n"
"v_fma_f32 v30, v118, -v127, v30\n"
"v_fma_f32 v31, v119, -v127, v31\n"
"v_fma_f32 v32, v120, -v127, v32\n"
"v_fma_f32 v33, v121, -v127, v33\n"
"v_mfma_f32_32x32x16_bf16 v[60:75], a[92:95], v[88:91], v[60:75]\n"
"s_cmp_lg_u64 s[66:67], 0\n"
"s_cbranch_scc1 Lslow30_h18\n"
"Lback30_h18:\n"
"v_fma_f32 v34, v122, -v127, v34\n"
"v_fma_f32 v35, v123, -v127, v35\n"
"v_cvt_pk_bf16_f32 v120, v28, v29\n"
"v_cvt_pk_bf16_f32 v121, v30, v31\n"
"v_mfma_f32_32x32x16_bf16 v[36:51], a[96:99], v[4:7], v[36:51]\n"
"v_cvt_pk_bf16_f32 v122, v32, v33\n"
"v_cvt_pk_bf16_f32 v123, v34, v35\n"
"v_cndmask_b32 v116, v122, v120, s[4:5]\n"
"v_cndmask_b32 v117, v123, v121, s[4:5]\n"
"v_mfma_f32_32x32x16_bf16 v[60:75], a[100:103], v[4:7], v[60:75]\n"
"v_mov_b32_dpp v118, v116 row_ror:8 row_mask:0xf bank_mask:0xf\n"
"v_mov_b32_dpp v119, v117 row_ror:8 row_mask:0xf bank_mask:0xf\n"
"v_cndmask_b32 v120, v120, v118, s[4:5]\n"
"v_cndmask_b32 v121, v121, v119, s[4:5]\n"
"v_mfma_f32_32x32x16_bf16 v[36:51], a[104:107], v[8:11], v[36:51]\n"
"v_cndmask_b32 v122, v118, v122, s[4:5]\n"
"v_cndmask_b32 v123, v119, v123, s[4:5]\n"
"s_add_u32 s53, s76, 0\n"
"buffer_store_dwordx4 v[120:123], v124, s[40:43], s53 offen sc1\n"
"v_mfma_f32_32x32x16_bf16 v[60:75], a[108:111], v[8:11], v[60:75]\n"
"s_waitcnt vmcnt(6)\n"
"v_xor_b32 v16, v132, v16\n"
"s_waitcnt vmcnt(5)\n"
"v_xor_b32 v92, v132, v92\n"
"s_waitcnt vmcnt(4)\n"
"v_xor_b32 v96, v132, v96\n"
"s_waitcnt vmcnt(3)\n"
"v_xor_b32 v100, v132, v100\n"
"s_waitcnt vmcnt(2)\n"
"v_xor_b32 v104, v132, v104\n"
"v_mfma_f32_32x32x16_bf16 v[36:51], a[112:115], v[12:15], v[36:51]\n"
"s_waitcnt vmcnt(1)\n"
"v_xor_b32 v108, v132, v108\n"
"v_bitop3_b32 v134, v16, v17, v18 bitop3:0x80\n"
"v_bitop3_b32 v134, v134, v19, v92 bitop3:0x80\n"
"v_bitop3_b32 v134, v134, v93, v94 bitop3:0x80\n"
"v_bitop3_b32 v134, v134, v95, v96 bitop3:0x80\n"
"v_mfma_f32_32x32x16_bf16 v[60:75], a[116:119], v[12:15], v[60:75]\n"
"v_bitop3_b32 v134, v134, v97, v98 bitop3:0x80\n"
"v_bitop3_b32 v134, v134, v99, v100 bitop3:0x80\n"
"v_bitop3_b32 v134, v134, v101, v102 bitop3:0x80\n"
"v_bitop3_b32 v134, v134, v103, v104 bitop3:0x80\n"
"v_bitop3_b32 v134, v134, v105, v106 bitop3:0x80\n"
"v_mfma_f32_32x32x16_bf16 v[36:51], a[120:123], v[0:3], v[36:51]\n"
"v_bitop3_b32 v134, v134, v107, v108 bitop3:0x80\n"
"v_bitop3_b32 v134, v134, v109, v110 bitop3:0x80\n"
"v_bitop3_b32 v134, v134, v111, v111 bitop3:0x80\n"
"v_and_b32 v134, s38, v134\n"
"v_cmp_ne_u32 s[66:67], s38, v134\n"
"v_mfma_f32_32x32x16_bf16 v[60:75], a[124:127], v[0:3], v[60:75]\n"
"s_cmp_lg_u64 s[66:67], 0\n"
"s_cbranch_scc1 Lslow31_h19\n"
"Lback31_h19:\n"
"v_mfma_f32_32x32x16_bf16 v[36:51], a[128:131], v[16:19], v[36:51]\n"
"v_mfma_f32_32x32x16_bf16 v[36:51], a[136:139], v[92:95], v[36:51]\n"
"v_mfma_f32_32x32x16_bf16 v[36:51], a[144:147], v[96:99], v[36:51]\n"
"v_mfma_f32_32x32x16_bf16 v[36:51], a[152:155], v[100:103], v[36:51]\n"
"v_mfma_f32_32x32x16_bf16 v[36:51], a[160:163], v[104:107], v[36:51]\n"
"v_mfma_f32_32x32x16_bf16 v[36:51], a[168:171], v[108:111], v[36:51]\n"
"v_mfma_f32_32x32x16_bf16 v[60:75], a[132:135], v[16:19], v[60:75]\n"
"v_mfma_f32_32x32x16_bf16 v[60:75], a[140:143], v[92:95], v[60:75]\n"
"v_mfma_f32_32x32x16_bf16 v[60:75], a[148:151], v[96:99], v[60:75]\n"
"s_nop 7\n"
"s_nop 0\n"
"ds_write_b128 v133, v[36:39] offset:32768\n"
"v_mfma_f32_32x32x16_bf16 v[60:75], a[156:159], v[100:103], v[60:75]\n"
"ds_write_b128 v133, v[40:43] offset:33792\n"
"v_mfma_f32_32x32x16_bf16 v[60:75], a[164:167], v[104:107], v[60:75]\n"
"ds_write_b128 v133, v[44:47] offset:34816\n"
"v_mfma_f32_32x32x16_bf16 v[60:75], a[172:175], v[108:111], v[60:75]\n"
"ds_write_b128 v133, v[48:51] offset:35840\n"
"s_nop 7\n"
"s_nop 2\n"
"ds_write_b128 v133, v[60:63] offset:36864\n"
"ds_write_b128 v133, v[64:67] offset:37888\n"
"ds_write_b128 v133, v[68:71] offset:38912\n"
"ds_write_b128 v133, v[72:75] offset:39936\n"
"s_waitcnt lgkmcnt(0)\n"
"s_barrier\n"
"ds_read_b128 v[28:31], v135 offset:32768\n"
"ds_read_b128 v[36:39], v135 offset:40960\n"
"ds_read_b128 v[32:35], v135 offset:33792\n"
"ds_read_b128 v[40:43], v135 offset:41984\n"
"ds_read_b128 v[44:47], v135 offset:49152\n"
"ds_read_b128 v[48:51], v135 offset:50176\n"
"ds_read_b128 v[0:3], v135 offset:57344\n"
"ds_read_b128 v[16:19], v135 offset:58368\n"
"v_lshlrev_b32 v60, 16, v56\n"
"v_and_b32 v61, s61, v56\n"
"v_lshlrev_b32 v62, 16, v57\n"
"v_and_b32 v63, s61, v57\n"
"v_lshlrev_b32 v64, 16, v58\n"
"v_and_b32 v65, s61, v58\n"
"v_lshlrev_b32 v66, 16, v59\n"
"v_and_b32 v67, s61, v59\n"
"s_waitcnt lgkmcnt(6)\n"
"v_pk_add_f32 v[28:29], v[28:29], v[36:37]\n"
"v_pk_add_f32 v[30:31], v[30:31], v[38:39]\n"
"s_waitcnt lgkmcnt(4)\n"
"v_pk_add_f32 v[32:33], v[32:33], v[40:41]\n"
"v_pk_add_f32 v[34:35], v[34:35], v[42:43]\n"
"s_waitcnt lgkmcnt(1)\n"
"v_pk_add_f32 v[44:45], v[44:45], v[0:1]\n"
"v_pk_add_f32 v[46:47], v[46:47], v[2:3]\n"
"s_waitcnt lgkmcnt(0)\n"
"v_pk_add_f32 v[48:49], v[48:49], v[16:17]\n"
"v_pk_add_f32 v[50:51], v[50:51], v[18:19]\n"
"v_pk_add_f32 v[28:29], v[28:29], v[44:45]\n"
"v_pk_add_f32 v[30:31], v[30:31], v[46:47]\n"
"v_pk_add_f32 v[32:33], v[32:33], v[48:49]\n"
"v_pk_add_f32 v[34:35], v[34:35], v[50:51]\n"
"v_fma_f32 v60, v28, -v126, v60\n"
"v_fma_f32 v61, v29, -v126, v61\n"
"v_fma_f32 v62, v30, -v126, v62\n"
"v_fma_f32 v63, v31, -v126, v63\n"
"v_fma_f32 v64, v32, -v126, v64\n"
"v_fma_f32 v65, v33, -v126, v65\n"
"v_fma_f32 v66, v34, -v126, v66\n"
"v_fma_f32 v67, v35, -v126, v67\n"
"v_cvt_pk_bf16_f32 v32, v60, v61\n"
"v_cvt_pk_bf16_f32 v33, v62, v63\n"
"v_cvt_pk_bf16_f32 v34, v64, v65\n"
"v_cvt_pk_bf16_f32 v35, v66, v67\n"
"v_cndmask_b32 v28, v34, v32, s[4:5]\n"
"v_cndmask_b32 v29, v35, v33, s[4:5]\n"
"s_nop 0\n"
"v_mov_b32_dpp v30, v28 row_ror:8 row_mask:0xf bank_mask:0xf\n"
"v_mov_b32_dpp v31, v29 row_ror:8 row_mask:0xf bank_mask:0xf\n"
"v_cndmask_b32 v32, v32, v30, s[4:5]\n"
"v_cndmask_b32 v33, v33, v31, s[4:5]\n"
"v_cndmask_b32 v34, v30, v34, s[4:5]\n"
"v_cndmask_b32 v35, v31, v35, s[4:5]\n"
"s_add_u32 s53, s76, 0x8000\n"
"buffer_store_dwordx4 v[32:35], v124, s[40:43], s53 offen sc1\n"
"s_endpgm\n"
"Lslow1_f1:\n"
"s_waitcnt vmcnt(0) lgkmcnt(0)\n"
"s_nop 7\n"
"s_nop 7\n"
"s_mov_b32 s64, 0\n"
"s_mov_b32 s65, m0\n"
"Lslow1_f1l:\n"
"s_sleep 1\n"
"buffer_load_dwordx4 v[48:51], v133, s[8:11], s31 offen sc1\n"
"s_waitcnt vmcnt(0) lgkmcnt(0)\n"
"v_cmp_ne_u64 s[62:63], s[54:55], v[48:49]\n"
"v_cmp_ne_u64 vcc, s[54:55], v[50:51]\n"
"s_or_b64 vcc, vcc, s[62:63]\n"
"s_cbranch_vccz Lback1_f1x\n"
"s_add_u32 s64, s64, 1\n"
"s_cmp_lt_u32 s64, 0x4000\n"
"s_cbranch_scc1 Lslow1_f1l\n"
"Lback1_f1x:\n"
"s_waitcnt vmcnt(0) lgkmcnt(0)\n"
"s_mov_b32 m0, s65\n"
"s_nop 7\n"
"s_branch Lback1_f1\n"
"Lslow2_kvt:\n"
"s_waitcnt vmcnt(0) lgkmcnt(0)\n"
"s_nop 7\n"
"s_nop 7\n"
"s_mov_b32 s64, 0\n"
"s_mov_b32 s65, m0\n"
"Lslow2_kvtl:\n"
"s_sleep 1\n"
"buffer_load_dwordx4 v[64:67], v120, s[8:11], s36 offen sc1\n"
"buffer_load_dwordx4 v[68:71], v120, s[8:11], s69 offen sc1\n"
"s_waitcnt vmcnt(0) lgkmcnt(0)\n"
"v_max3_u32 v96, v64, v65, v66\n"
"v_max3_u32 v96, v96, v67, v68\n"
"v_max3_u32 v96, v96, v69, v70\n"
"v_max_u32 v96, v96, v71\n"
"v_cmp_eq_u32 vcc, -1, v96\n"
"s_cbranch_vccz Lback2_kvtx\n"
"s_add_u32 s64, s64, 1\n"
"s_cmp_lt_u32 s64, 0x4000\n"
"s_cbranch_scc1 Lslow2_kvtl\n"
"Lback2_kvtx:\n"
"s_waitcnt vmcnt(0) lgkmcnt(0)\n"
"s_mov_b32 m0, s65\n"
"s_nop 7\n"
"s_branch Lback2_kvt\n"
"Lslow3_f2:\n"
"s_waitcnt vmcnt(0) lgkmcnt(0)\n"
"s_nop 7\n"
"s_nop 7\n"
"s_mov_b32 s64, 0\n"
"s_mov_b32 s65, m0\n"
"Lslow3_f2l:\n"
"s_sleep 2\n"
"buffer_load_dwordx2 v[48:49], v102, s[8:11], s75 offen sc1\n"
"s_waitcnt vmcnt(0) lgkmcnt(0)\n"
"v_cmp_ne_u64 vcc, s[54:55], v[48:49]\n"
"s_cbranch_vccz Lback3_f2x\n"
"s_add_u32 s64, s64, 1\n"
"s_cmp_lt_u32 s64, 0x4000\n"
"s_cbranch_scc1 Lslow3_f2l\n"
"Lback3_f2x:\n"
"s_waitcnt vmcnt(0) lgkmcnt(0)\n"
"s_mov_b32 m0, s65\n"
"s_nop 7\n"
"s_branch Lback3_f2\n"
"Lslow4_s0:\n"
"s_waitcnt vmcnt(0) lgkmcnt(0)\n"
"s_nop 7\n"
"s_nop 7\n"
"s_mov_b32 s64, 0\n"
"s_mov_b32 s65, m0\n"
"Lslow4_s0l:\n"
"s_sleep 1\n"
"s_add_u32 s59, s74, 0\n"
"buffer_load_dwordx2 v[130:131], v136, s[8:11], s59 offen sc1\n"
"s_add_u32 s59, s74, 0x800\n"
"buffer_load_dwordx2 v[128:129], v136, s[8:11], s59 offen sc1\n"
"s_waitcnt vmcnt(0) lgkmcnt(0)\n"
"v_max3_u32 v134, v130, v131, v128\n"
"v_max_u32 v134, v134, v129\n"
"v_cmp_eq_u32 vcc, -1, v134\n"
"s_cbranch_vccz Lback4_s0x\n"
"s_add_u32 s64, s64, 1\n"
"s_cmp_lt_u32 s64, 0x4000\n"
"s_cbranch_scc1 Lslow4_s0l\n"
"Lback4_s0x:\n"
"s_waitcnt vmcnt(0) lgkmcnt(0)\n"
"s_mov_b32 m0, s65\n"
"s_nop 7\n"
"s_branch Lback4_s0\n"
"Lslow5_g:\n"
"s_waitcnt vmcnt(0) lgkmcnt(0)\n"
"s_nop 7\n"
"s_nop 7\n"
"s_mov_b32 s64, 0\n"
"s_mov_b32 s65, m0\n"
"Lslow5_gl:\n"
"s_sleep 1\n"
"buffer_load_dwordx4 v[20:23], v85, s[8:11], s29 offen sc1\n"
"buffer_load_dwordx4 v[24:27], v85, s[8:11], s4 offen sc1\n"
"s_waitcnt vmcnt(0) lgkmcnt(0)\n"
"v_max3_u32 v134, v20, v21, v22\n"
"v_max3_u32 v134, v134, v23, v24\n"
"v_max3_u32 v134, v134, v25, v26\n"
"v_max_u32 v134, v134, v27\n"
"v_cmp_eq_u32 vcc, -1, v134\n"
"s_cbranch_vccz Lback5_gx\n"
"s_add_u32 s64, s64, 1\n"
"s_cmp_lt_u32 s64, 0x4000\n"
"s_cbranch_scc1 Lslow5_gl\n"
"Lback5_gx:\n"
"s_waitcnt vmcnt(0) lgkmcnt(0)\n"
"s_mov_b32 m0, s65\n"
"s_nop 7\n"
"s_branch Lback5_g\n"
"Lslow6_xs00:\n"
"s_waitcnt vmcnt(0) lgkmcnt(0)\n"
"s_nop 7\n"
"s_nop 7\n"
"s_mov_b32 s64, 0\n"
"s_mov_b32 s65, m0\n"
"Lslow6_xs00l:\n"
"s_sleep 1\n"
"s_add_u32 s59, s93, 0x20000\n"
"buffer_load_dwordx2 v[0:1], v11, s[8:11], s59 offen sc1\n"
"s_add_u32 s59, s93, 0x20000\n"
"buffer_load_dwordx2 v[2:3], v11, s[8:11], s59 offen offset:128 sc1\n"
"s_add_u32 s59, s93, 0x20400\n"
"buffer_load_dwordx2 v[28:29], v11, s[8:11], s59 offen sc1\n"
"s_add_u32 s59, s93, 0x20400\n"
"buffer_load_dwordx2 v[30:31], v11, s[8:11], s59 offen offset:128 sc1\n"
"s_add_u32 m0, s87, 0\n"
"s_add_u32 s27, s86, 0\n"
"buffer_load_dwordx4 v142, s[8:11], s27 offen sc1 lds\n"
"buffer_load_dwordx4 v142, s[8:11], s27 offen offset:1024 sc1 lds\n"
"buffer_load_dwordx4 v142, s[8:11], s27 offen offset:2048 sc1 lds\n"
"buffer_load_dwordx4 v142, s[8:11], s27 offen offset:3072 sc1 lds\n"
"s_add_u32 m0, s87, 0x4000\n"
"s_add_u32 s27, s86, 0x4000\n"
"buffer_load_dwordx4 v142, s[8:11], s27 offen sc1 lds\n"
"buffer_load_dwordx4 v142, s[8:11], s27 offen offset:1024 sc1 lds\n"
"buffer_load_dwordx4 v142, s[8:11], s27 offen offset:2048 sc1 lds\n"
"buffer_load_dwordx4 v142, s[8:11], s27 offen offset:3072 sc1 lds\n"
"s_waitcnt vmcnt(0) lgkmcnt(0)\n"
"v_max3_u32 v134, v0, v1, v2\n"
"v_max3_u32 v134, v134, v3, v28\n"
"v_max3_u32 v134, v134, v29, v30\n"
"v_max_u32 v134, v134, v31\n"
"v_cmp_eq_u32 vcc, -1, v134\n"
"s_cbranch_vccz Lback6_xs00x\n"
"s_add_u32 s64, s64, 1\n"
"s_cmp_lt_u32 s64, 0x4000\n"
"s_cbranch_scc1 Lslow6_xs00l\n"
"Lback6_xs00x:\n"
"s_waitcnt vmcnt(0) lgkmcnt(0)\n"
"s_mov_b32 m0, s65\n"
"s_nop 7\n"
"s_branch Lback6_xs00\n"
"Lslowx00:\n"
"s_waitcnt vmcnt(0) lgkmcnt(0)\n"
"s_nop 7\n"
"s_nop 7\n"
"s_mov_b32 s64, 0\n"
"s_mov_b32 s65, m0\n"
"Lslowx00l:\n"
"s_barrier\n"
"s_sleep 4\n"
"s_add_u32 m0, s87, 0\n"
"s_add_u32 s27, s86, 0\n"
"buffer_load_dwordx4 v142, s[8:11], s27 offen sc1 lds\n"
"buffer_load_dwordx4 v142, s[8:11], s27 offen offset:1024 sc1 lds\n"
"buffer_load_dwordx4 v142, s[8:11], s27 offen offset:2048 sc1 lds\n"
"buffer_load_dwordx4 v142, s[8:11], s27 offen offset:3072 sc1 lds\n"
"s_add_u32 m0, s87, 0x4000\n"
"s_add_u32 s27, s86, 0x4000\n"
"buffer_load_dwordx4 v142, s[8:11], s27 offen sc1 lds\n"
"buffer_load_dwordx4 v142, s[8:11], s27 offen offset:1024 sc1 lds\n"
"buffer_load_dwordx4 v142, s[8:11], s27 offen offset:2048 sc1 lds\n"
"buffer_load_dwordx4 v142, s[8:11], s27 offen offset:3072 sc1 lds\n"
"s_waitcnt vmcnt(0) lgkmcnt(0)\n"
"s_barrier\n"
"v_mov_b32 v134, 0\n"
"ds_read_b128 v[72:75], v4\n"
"ds_read_b128 v[76:79], v4 offset:16384\n"
"ds_read_b128 v[80:83], v4 offset:1024\n"
"ds_read_b128 v[84:87], v4 offset:17408\n"
"s_waitcnt lgkmcnt(2)\n"
"v_mfma_f32_16x16x32_bf16 v[40:43], a[176:179], v[72:75], 0\n"
"ds_read_b128 v[88:91], v5\n"
"v_max3_u32 v134, v134, v72, v73\n"
"v_max3_u32 v134, v134, v74, v75\n"
"v_mfma_f32_16x16x32_bf16 v[48:51], a[180:183], v[72:75], 0\n"
"ds_read_b128 v[92:95], v5 offset:16384\n"
"v_mfma_f32_16x16x32_bf16 v[64:67], a[184:187], v[72:75], 0\n"
"v_max3_u32 v134, v134, v76, v77\n"
"v_max3_u32 v134, v134, v78, v79\n"
"v_mfma_f32_16x16x32_bf16 v[44:47], a[176:179], v[76:79], 0\n"
"v_mfma_f32_16x16x32_bf16 v[60:63], a[180:183], v[76:79], 0\n"
"v_mfma_f32_16x16x32_bf16 v[68:71], a[184:187], v[76:79], 0\n"
"s_waitcnt lgkmcnt(2)\n"
"v_mfma_f32_16x16x32_bf16 v[40:43], a[188:191], v[80:83], v[40:43]\n"
"ds_read_b128 v[72:75], v6\n"
"v_max3_u32 v134, v134, v80, v81\n"
"v_max3_u32 v134, v134, v82, v83\n"
"v_mfma_f32_16x16x32_bf16 v[48:51], a[192:195], v[80:83], v[48:51]\n"
"ds_read_b128 v[76:79], v6 offset:16384\n"
"v_mfma_f32_16x16x32_bf16 v[64:67], a[196:199], v[80:83], v[64:67]\n"
"v_max3_u32 v134, v134, v84, v85\n"
"v_max3_u32 v134, v134, v86, v87\n"
"v_mfma_f32_16x16x32_bf16 v[44:47], a[188:191], v[84:87], v[44:47]\n"
"v_mfma_f32_16x16x32_bf16 v[60:63], a[192:195], v[84:87], v[60:63]\n"
"v_mfma_f32_16x16x32_bf16 v[68:71], a[196:199], v[84:87], v[68:71]\n"
"s_waitcnt lgkmcnt(2)\n"
"v_mfma_f32_16x16x32_bf16 v[40:43], a[224:227], v[88:91], v[40:43]\n"
"ds_read_b128 v[80:83], v7\n"
"v_max3_u32 v134, v134, v88, v89\n"
"v_max3_u32 v134, v134, v90, v91\n"
"v_mfma_f32_16x16x32_bf16 v[48:51], a[228:231], v[88:91], v[48:51]\n"
"ds_read_b128 v[84:87], v7 offset:16384\n"
"v_mfma_f32_16x16x32_bf16 v[64:67], a[232:235], v[88:91], v[64:67]\n"
"v_max3_u32 v134, v134, v92, v93\n"
"v_max3_u32 v134, v134, v94, v95\n"
"v_mfma_f32_16x16x32_bf16 v[44:47], a[224:227], v[92:95], v[44:47]\n"
"v_mfma_f32_16x16x32_bf16 v[60:63], a[228:231], v[92:95], v[60:63]\n"
"v_mfma_f32_16x16x32_bf16 v[68:71], a[232:235], v[92:95], v[68:71]\n"
"s_waitcnt lgkmcnt(2)\n"
"v_mfma_f32_16x16x32_bf16 v[40:43], v[160:163], v[72:75], v[40:43]\n"
"ds_read_b128 v[88:91], v4 offset:2048\n"
"v_max3_u32 v134, v134, v72, v73\n"
"v_max3_u32 v134, v134, v74, v75\n"
"v_mfma_f32_16x16x32_bf16 v[48:51], v[164:167], v[72:75], v[48:51]\n"
"ds_read_b128 v[92:95], v4 offset:18432\n"
"v_mfma_f32_16x16x32_bf16 v[64:67], v[168:171], v[72:75], v[64:67]\n"
"v_max3_u32 v134, v134, v76, v77\n"
"v_max3_u32 v134, v134, v78, v79\n"
"v_mfma_f32_16x16x32_bf16 v[44:47], v[160:163], v[76:79], v[44:47]\n"
"v_mfma_f32_16x16x32_bf16 v[60:63], v[164:167], v[76:79], v[60:63]\n"
"v_mfma_f32_16x16x32_bf16 v[68:71], v[168:171], v[76:79], v[68:71]\n"
"s_waitcnt lgkmcnt(2)\n"
"v_mfma_f32_16x16x32_bf16 v[40:43], v[208:211], v[80:83], v[40:43]\n"
"ds_read_b128 v[72:75], v5 offset:1024\n"
"v_max3_u32 v134, v134, v80, v81\n"
"v_max3_u32 v134, v134, v82, v83\n"
"v_mfma_f32_16x16x32_bf16 v[48:51], v[212:215], v[80:83], v[48:51]\n"
"ds_read_b128 v[76:79], v5 offset:17408\n"
"v_mfma_f32_16x16x32_bf16 v[64:67], v[216:219], v[80:83], v[64:67]\n"
"v_max3_u32 v134, v134, v84, v85\n"
"v_max3_u32 v134, v134, v86, v87\n"
"v_mfma_f32_16x16x32_bf16 v[44:47], v[208:211], v[84:87], v[44:47]\n"
"v_mfma_f32_16x16x32_bf16 v[60:63], v[212:215], v[84:87], v[60:63]\n"
"v_mfma_f32_16x16x32_bf16 v[68:71], v[216:219], v[84:87], v[68:71]\n"
"s_waitcnt lgkmcnt(2)\n"
"v_mfma_f32_16x16x32_bf16 v[40:43], a[200:203], v[88:91], v[40:43]\n"
"ds_read_b128 v[80:83], v6 offset:1024\n"
"v_max3_u32 v134, v134, v88, v89\n"
"v_max3_u32 v134, v134, v90, v91\n"
"v_mfma_f32_16x16x32_bf16 v[48:51], a[204:207], v[88:91], v[48:51]\n"
"ds_read_b128 v[84:87], v6 offset:17408\n"
"v_mfma_f32_16x16x32_bf16 v[64:67], a[208:211], v[88:91], v[64:67]\n"
"v_max3_u32 v134, v134, v92, v93\n"
"v_max3_u32 v134, v134, v94, v95\n"
"v_mfma_f32_16x16x32_bf16 v[44:47], a[200:203], v[92:95], v[44:47]\n"
"v_mfma_f32_16x16x32_bf16 v[60:63], a[204:207], v[92:95], v[60:63]\n"
"v_mfma_f32_16x16x32_bf16 v[68:71], a[208:211], v[92:95], v[68:71]\n"
"s_waitcnt lgkmcnt(2)\n"
"v_mfma_f32_16x16x32_bf16 v[40:43], a[236:239], v[72:75], v[40:43]\n"
"ds_read_b128 v[88:91], v7 offset:1024\n"
"v_max3_u32 v134, v134, v72, v73\n"
"v_max3_u32 v134, v134, v74, v75\n"
"v_mfma_f32_16x16x32_bf16 v[48:51], a[240:243], v[72:75], v[48:51]\n"
"ds_read_b128 v[92:95], v7 offset:17408\n"
"v_mfma_f32_16x16x32_bf16 v[64:67], a[244:247], v[72:75], v[64:67]\n"
"v_max3_u32 v134, v134, v76, v77\n"
"v_max3_u32 v134, v134, v78, v79\n"
"v_mfma_f32_16x16x32_bf16 v[44:47], a[236:239], v[76:79], v[44:47]\n"
"v_mfma_f32_16x16x32_bf16 v[60:63], a[240:243], v[76:79], v[60:63]\n"
"v_mfma_f32_16x16x32_bf16 v[68:71], a[244:247], v[76:79], v[68:71]\n"
"s_waitcnt lgkmcnt(2)\n"
"v_mfma_f32_16x16x32_bf16 v[40:43], v[172:175], v[80:83], v[40:43]\n"
"ds_read_b128 v[72:75], v4 offset:3072\n"
"v_max3_u32 v134, v134, v80, v81\n"
"v_max3_u32 v134, v134, v82, v83\n"
"v_mfma_f32_16x16x32_bf16 v[48:51], v[176:179], v[80:83], v[48:51]\n"
"ds_read_b128 v[76:79], v4 offset:19456\n"
"v_mfma_f32_16x16x32_bf16 v[64:67], v[180:183], v[80:83], v[64:67]\n"
"v_max3_u32 v134, v134, v84, v85\n"
"v_max3_u32 v134, v134, v86, v87\n"
"v_mfma_f32_16x16x32_bf16 v[44:47], v[172:175], v[84:87], v[44:47]\n"
"v_mfma_f32_16x16x32_bf16 v[60:63], v[176:179], v[84:87], v[60:63]\n"
"v_mfma_f32_16x16x32_bf16 v[68:71], v[180:183], v[84:87], v[68:71]\n"
"s_waitcnt lgkmcnt(2)\n"
"v_mfma_f32_16x16x32_bf16 v[40:43], v[220:223], v[88:91], v[40:43]\n"
"ds_read_b128 v[80:83], v5 offset:2048\n"
"v_max3_u32 v134, v134, v88, v89\n"
"v_max3_u32 v134, v134, v90, v91\n"
"v_mfma_f32_16x16x32_bf16 v[48:51], v[224:227], v[88:91], v[48:51]\n"
"ds_read_b128 v[84:87], v5 offset:18432\n"
"v_mfma_f32_16x16x32_bf16 v[64:67], v[228:231], v[88:91], v[64:67]\n"
"v_max3_u32 v134, v134, v92, v93\n"
"v_max3_u32 v134, v134, v94, v95\n"
"v_mfma_f32_16x16x32_bf16 v[44:47], v[220:223], v[92:95], v[44:47]\n"
"v_mfma_f32_16x16x32_bf16 v[60:63], v[224:227], v[92:95], v[60:63]\n"
"v_mfma_f32_16x16x32_bf16 v[68:71], v[228:231], v[92:95], v[68:71]\n"
"s_waitcnt lgkmcnt(2)\n"
"v_mfma_f32_16x16x32_bf16 v[40:43], a[212:215], v[72:75], v[40:43]\n"
"ds_read_b128 v[88:91], v6 offset:2048\n"
"v_max3_u32 v134, v134, v72, v73\n"
"v_max3_u32 v134, v134, v74, v75\n"
"v_mfma_f32_16x16x32_bf16 v[48:51], a[216:219], v[72:75], v[48:51]\n"
"ds_read_b128 v[92:95], v6 offset:18432\n"
"v_mfma_f32_16x16x32_bf16 v[64:67], a[220:223], v[72:75], v[64:67]\n"
"v_max3_u32 v134, v134, v76, v77\n"
"v_max3_u32 v134, v134, v78, v79\n"
"v_mfma_f32_16x16x32_bf16 v[44:47], a[212:215], v[76:79], v[44:47]\n"
"v_mfma_f32_16x16x32_bf16 v[60:63], a[216:219], v[76:79], v[60:63]\n"
"v_mfma_f32_16x16x32_bf16 v[68:71], a[220:223], v[76:79], v[68:71]\n"
"s_waitcnt lgkmcnt(2)\n"
"v_mfma_f32_16x16x32_bf16 v[40:43], a[248:251], v[80:83], v[40:43]\n"
"ds_read_b128 v[72:75], v7 offset:2048\n"
"v_max3_u32 v134, v134, v80, v81\n"
"v_max3_u32 v134, v134, v82, v83\n"
"v_mfma_f32_16x16x32_bf16 v[48:51], a[252:255], v[80:83], v[48:51]\n"
"ds_read_b128 v[76:79], v7 offset:18432\n"
"v_mfma_f32_16x16x32_bf16 v[64:67], v[144:147], v[80:83], v[64:67]\n"
"v_max3_u32 v134, v134, v84, v85\n"
"v_max3_u32 v134, v134, v86, v87\n"
"v_mfma_f32_16x16x32_bf16 v[44:47], a[248:251], v[84:87], v[44:47]\n"
"v_mfma_f32_16x16x32_bf16 v[60:63], a[252:255], v[84:87], v[60:63]\n"
"v_mfma_f32_16x16x32_bf16 v[68:71], v[144:147], v[84:87], v[68:71]\n"
"s_waitcnt lgkmcnt(2)\n"
"v_mfma_f32_16x16x32_bf16 v[40:43], v[184:187], v[88:91], v[40:43]\n"
"ds_read_b128 v[80:83], v5 offset:3072\n"
"v_max3_u32 v134, v134, v88, v89\n"
"v_max3_u32 v134, v134, v90, v91\n"
"v_mfma_f32_16x16x32_bf16 v[48:51], v[188:191], v[88:91], v[48:51]\n"
"ds_read_b128 v[84:87], v5 offset:19456\n"
"v_mfma_f32_16x16x32_bf16 v[64:67], v[192:195], v[88:91], v[64:67]\n"
"v_max3_u32 v134, v134, v92, v93\n"
"v_max3_u32 v134, v134, v94, v95\n"
"v_mfma_f32_16x16x32_bf16 v[44:47], v[184:187], v[92:95], v[44:47]\n"
"v_mfma_f32_16x16x32_bf16 v[60:63], v[188:191], v[92:95], v[60:63]\n"
"v_mfma_f32_16x16x32_bf16 v[68:71], v[192:195], v[92:95], v[68:71]\n"
"s_waitcnt lgkmcnt(2)\n"
"v_mfma_f32_16x16x32_bf16 v[40:43], v[232:235], v[72:75], v[40:43]\n"
"ds_read_b128 v[88:91], v6 offset:3072\n"
"v_max3_u32 v134, v134, v72, v73\n"
"v_max3_u32 v134, v134, v74, v75\n"
"v_mfma_f32_16x16x32_bf16 v[48:51], v[236:239], v[72:75], v[48:51]\n"
"ds_read_b128 v[92:95], v6 offset:19456\n"
"v_mfma_f32_16x16x32_bf16 v[64:67], v[240:243], v[72:75], v[64:67]\n"
"v_max3_u32 v134, v134, v76, v77\n"
"v_max3_u32 v134, v134, v78, v79\n"
"v_mfma_f32_16x16x32_bf16 v[44:47], v[232:235], v[76:79], v[44:47]\n"
"v_mfma_f32_16x16x32_bf16 v[60:63], v[236:239], v[76:79], v[60:63]\n"
"v_mfma_f32_16x16x32_bf16 v[68:71], v[240:243], v[76:79], v[68:71]\n"
"s_waitcnt lgkmcnt(2)\n"
"v_mfma_f32_16x16x32_bf16 v[40:43], v[148:151], v[80:83], v[40:43]\n"
"ds_read_b128 v[72:75], v7 offset:3072\n"
"v_max3_u32 v134, v134, v80, v81\n"
"v_max3_u32 v134, v134, v82, v83\n"
"v_mfma_f32_16x16x32_bf16 v[48:51], v[152:155], v[80:83], v[48:51]\n"
"ds_read_b128 v[76:79], v7 offset:19456\n"
"v_mfma_f32_16x16x32_bf16 v[64:67], v[156:159], v[80:83], v[64:67]\n"
"v_max3_u32 v134, v134, v84, v85\n"
"v_max3_u32 v134, v134, v86, v87\n"
"v_mfma_f32_16x16x32_bf16 v[44:47], v[148:151], v[84:87], v[44:47]\n"
"v_mfma_f32_16x16x32_bf16 v[60:63], v[152:155], v[84:87], v[60:63]\n"
"v_mfma_f32_16x16x32_bf16 v[68:71], v[156:159], v[84:87], v[68:71]\n"
"s_waitcnt lgkmcnt(2)\n"
"v_mfma_f32_16x16x32_bf16 v[40:43], v[196:199], v[88:91], v[40:43]\n"
"v_max3_u32 v134, v134, v88, v89\n"
"v_max3_u32 v134, v134, v90, v91\n"
"v_mfma_f32_16x16x32_bf16 v[48:51], v[200:203], v[88:91], v[48:51]\n"
"v_mfma_f32_16x16x32_bf16 v[64:67], v[204:207], v[88:91], v[64:67]\n"
"v_max3_u32 v134, v134, v92, v93\n"
"v_max3_u32 v134, v134, v94, v95\n"
"v_mfma_f32_16x16x32_bf16 v[44:47], v[196:199], v[92:95], v[44:47]\n"
"v_mfma_f32_16x16x32_bf16 v[60:63], v[200:203], v[92:95], v[60:63]\n"
"v_mfma_f32_16x16x32_bf16 v[68:71], v[204:207], v[92:95], v[68:71]\n"
"s_waitcnt lgkmcnt(0)\n"
"v_mfma_f32_16x16x32_bf16 v[40:43], v[244:247], v[72:75], v[40:43]\n"
"v_max3_u32 v134, v134, v72, v73\n"
"v_max3_u32 v134, v134, v74, v75\n"
"v_mfma_f32_16x16x32_bf16 v[48:51], v[248:251], v[72:75], v[48:51]\n"
"v_mfma_f32_16x16x32_bf16 v[64:67], v[252:255], v[72:75], v[64:67]\n"
"v_max3_u32 v134, v134, v76, v77\n"
"v_max3_u32 v134, v134, v78, v79\n"
"v_mfma_f32_16x16x32_bf16 v[44:47], v[244:247], v[76:79], v[44:47]\n"
"v_mfma_f32_16x16x32_bf16 v[60:63], v[248:251], v[76:79], v[60:63]\n"
"v_mfma_f32_16x16x32_bf16 v[68:71], v[252:255], v[76:79], v[68:71]\n"
"v_cmp_eq_u32 vcc, -1, v134\n"
"s_cbranch_vccz Lbackx00x\n"
"s_add_u32 s64, s64, 1\n"
"s_cmp_lt_u32 s64, 0x4000\n"
"s_cbranch_scc1 Lslowx00l\n"
"Lbackx00x:\n"
"ds_read_b128 v[80:83], v10\n"
"ds_read_b128 v[84:87], v10 offset:64\n"
"ds_read_b128 v[88:91], v10 offset:128\n"
"s_waitcnt vmcnt(0) lgkmcnt(0)\n"
"s_mov_b32 m0, s65\n"
"s_nop 7\n"
"s_nop 7\n"
"s_branch Lbackx00\n"
"Lslow7_xs01:\n"
"s_waitcnt vmcnt(0) lgkmcnt(0)\n"
"s_nop 7\n"
"s_nop 7\n"
"s_mov_b32 s64, 0\n"
"s_mov_b32 s65, m0\n"
"Lslow7_xs01l:\n"
"s_sleep 1\n"
"s_add_u32 s59, s93, 0x20800\n"
"buffer_load_dwordx2 v[32:33], v11, s[8:11], s59 offen sc1\n"
"s_add_u32 s59, s93, 0x20800\n"
"buffer_load_dwordx2 v[34:35], v11, s[8:11], s59 offen offset:128 sc1\n"
"s_add_u32 s59, s93, 0x20c00\n"
"buffer_load_dwordx2 v[36:37], v11, s[8:11], s59 offen sc1\n"
"s_add_u32 s59, s93, 0x20c00\n"
"buffer_load_dwordx2 v[38:39], v11, s[8:11], s59 offen offset:128 sc1\n"
"s_add_u32 m0, s87, 0x8000\n"
"s_add_u32 s27, s86, 0x8000\n"
"buffer_load_dwordx4 v142, s[8:11], s27 offen sc1 lds\n"
"s_add_u32 m0, s87, 0x8000\n"
"s_add_u32 s27, s86, 0x8000\n"
"buffer_load_dwordx4 v142, s[8:11], s27 offen offset:1024 sc1 lds\n"
"s_add_u32 m0, s87, 0x8000\n"
"s_add_u32 s27, s86, 0x8000\n"
"buffer_load_dwordx4 v142, s[8:11], s27 offen offset:2048 sc1 lds\n"
"s_add_u32 m0, s87, 0x8000\n"
"s_add_u32 s27, s86, 0x8000\n"
"buffer_load_dwordx4 v142, s[8:11], s27 offen offset:3072 sc1 lds\n"
"s_add_u32 m0, s87, 0xc000\n"
"s_add_u32 s27, s86, 0xc000\n"
"buffer_load_dwordx4 v142, s[8:11], s27 offen sc1 lds\n"
"s_add_u32 m0, s87, 0xc000\n"
"s_add_u32 s27, s86, 0xc000\n"
"buffer_load_dwordx4 v142, s[8:11], s27 offen offset:1024 sc1 lds\n"
"s_add_u32 m0, s87, 0xc000\n"
"s_add_u32 s27, s86, 0xc000\n"
"buffer_load_dwordx4 v142, s[8:11], s27 offen offset:2048 sc1 lds\n"
"s_add_u32 m0, s87, 0xc000\n"
"s_add_u32 s27, s86, 0xc000\n"
"buffer_load_dwordx4 v142, s[8:11], s27 offen offset:3072 sc1 lds\n"
"s_waitcnt vmcnt(0) lgkmcnt(0)\n"
"v_max3_u32 v134, v32, v33, v34\n"
"v_max3_u32 v134, v134, v35, v36\n"
"v_max3_u32 v134, v134, v37, v38\n"
"v_max_u32 v134, v134, v39\n"
"v_cmp_eq_u32 vcc, -1, v134\n"
"s_cbranch_vccz Lback7_xs01x\n"
"s_add_u32 s64, s64, 1\n"
"s_cmp_lt_u32 s64, 0x4000\n"
"s_cbranch_scc1 Lslow7_xs01l\n"
"Lback7_xs01x:\n"
"s_waitcnt vmcnt(0) lgkmcnt(0)\n"
"s_mov_b32 m0, s65\n"
"s_nop 7\n"
"s_branch Lback7_xs01\n"
"Lslowx01:\n"
"s_waitcnt vmcnt(0) lgkmcnt(0)\n"
"s_nop 7\n"
"s_nop 7\n"
"s_mov_b32 s64, 0\n"
"s_mov_b32 s65, m0\n"
"Lslowx01l:\n"
"s_barrier\n"
"s_sleep 4\n"
"s_add_u32 m0, s87, 0x8000\n"
"s_add_u32 s27, s86, 0x8000\n"
"buffer_load_dwordx4 v142, s[8:11], s27 offen sc1 lds\n"
"buffer_load_dwordx4 v142, s[8:11], s27 offen offset:1024 sc1 lds\n"
"buffer_load_dwordx4 v142, s[8:11], s27 offen offset:2048 sc1 lds\n"
"buffer_load_dwordx4 v142, s[8:11], s27 offen offset:3072 sc1 lds\n"
"s_add_u32 m0, s87, 0xc000\n"
"s_add_u32 s27, s86, 0xc000\n"
"buffer_load_dwordx4 v142, s[8:11], s27 offen sc1 lds\n"
"buffer_load_dwordx4 v142, s[8:11], s27 offen offset:1024 sc1 lds\n"
"buffer_load_dwordx4 v142, s[8:11], s27 offen offset:2048 sc1 lds\n"
"buffer_load_dwordx4 v142, s[8:11], s27 offen offset:3072 sc1 lds\n"
"s_waitcnt vmcnt(0) lgkmcnt(0)\n"
"s_barrier\n"
"v_mov_b32 v134, 0\n"
"ds_read_b128 v[72:75], v4 offset:32768\n"
"ds_read_b128 v[76:79], v4 offset:49152\n"
"ds_read_b128 v[80:83], v4 offset:33792\n"
"ds_read_b128 v[84:87], v4 offset:50176\n"
"s_waitcnt lgkmcnt(2)\n"
"v_mfma_f32_16x16x32_bf16 v[40:43], a[176:179], v[72:75], 0\n"
"ds_read_b128 v[88:91], v5 offset:32768\n"
"v_max3_u32 v134, v134, v72, v73\n"
"v_max3_u32 v134, v134, v74, v75\n"
"v_mfma_f32_16x16x32_bf16 v[48:51], a[180:183], v[72:75], 0\n"
"ds_read_b128 v[92:95], v5 offset:49152\n"
"v_mfma_f32_16x16x32_bf16 v[64:67], a[184:187], v[72:75], 0\n"
"v_max3_u32 v134, v134, v76, v77\n"
"v_max3_u32 v134, v134, v78, v79\n"
"v_mfma_f32_16x16x32_bf16 v[44:47], a[176:179], v[76:79], 0\n"
"v_mfma_f32_16x16x32_bf16 v[60:63], a[180:183], v[76:79], 0\n"
"v_mfma_f32_16x16x32_bf16 v[68:71], a[184:187], v[76:79], 0\n"
"s_waitcnt lgkmcnt(2)\n"
"v_mfma_f32_16x16x32_bf16 v[40:43], a[188:191], v[80:83], v[40:43]\n"
"ds_read_b128 v[72:75], v6 offset:32768\n"
"v_max3_u32 v134, v134, v80, v81\n"
"v_max3_u32 v134, v134, v82, v83\n"
"v_mfma_f32_16x16x32_bf16 v[48:51], a[192:195], v[80:83], v[48:51]\n"
"ds_read_b128 v[76:79], v6 offset:49152\n"
"v_mfma_f32_16x16x32_bf16 v[64:67], a[196:199], v[80:83], v[64:67]\n"
"v_max3_u32 v134, v134, v84, v85\n"
"v_max3_u32 v134, v134, v86, v87\n"
"v_mfma_f32_16x16x32_bf16 v[44:47], a[188:191], v[84:87], v[44:47]\n"
"v_mfma_f32_16x16x32_bf16 v[60:63], a[192:195], v[84:87], v[60:63]\n"
"v_mfma_f32_16x16x32_bf16 v[68:71], a[196:199], v[84:87], v[68:71]\n"
"s_waitcnt lgkmcnt(2)\n"
"v_mfma_f32_16x16x32_bf16 v[40:43], a[224:227], v[88:91], v[40:43]\n"
"ds_read_b128 v[80:83], v7 offset:32768\n"
"v_max3_u32 v134, v134, v88, v89\n"
"v_max3_u32 v134, v134, v90, v91\n"
"v_mfma_f32_16x16x32_bf16 v[48:51], a[228:231], v[88:91], v[48:51]\n"
"ds_read_b128 v[84:87], v7 offset:49152\n"
"v_mfma_f32_16x16x32_bf16 v[64:67], a[232:235], v[88:91], v[64:67]\n"
"v_max3_u32 v134, v134, v92, v93\n"
"v_max3_u32 v134, v134, v94, v95\n"
"v_mfma_f32_16x16x32_bf16 v[44:47], a[224:227], v[92:95], v[44:47]\n"
"v_mfma_f32_16x16x32_bf16 v[60:63], a[228:231], v[92:95], v[60:63]\n"
"v_mfma_f32_16x16x32_bf16 v[68:71], a[232:235], v[92:95], v[68:71]\n"
"s_waitcnt lgkmcnt(2)\n"
"v_mfma_f32_16x16x32_bf16 v[40:43], v[160:163], v[72:75], v[40:43]\n"
"ds_read_b128 v[88:91], v4 offset:34816\n"
"v_max3_u32 v134, v134, v72, v73\n"
"v_max3_u32 v134, v134, v74, v75\n"
"v_mfma_f32_16x16x32_bf16 v[48:51], v[164:167], v[72:75], v[48:51]\n"
"ds_read_b128 v[92:95], v4 offset:51200\n"
"v_mfma_f32_16x16x32_bf16 v[64:67], v[168:171], v[72:75], v[64:67]\n"
"v_max3_u32 v134, v134, v76, v77\n"
"v_max3_u32 v134, v134, v78, v79\n"
"v_mfma_f32_16x16x32_bf16 v[44:47], v[160:163], v[76:79], v[44:47]\n"
"v_mfma_f32_16x16x32_bf16 v[60:63], v[164:167], v[76:79], v[60:63]\n"
"v_mfma_f32_16x16x32_bf16 v[68:71], v[168:171], v[76:79], v[68:71]\n"
"s_waitcnt lgkmcnt(2)\n"
"v_mfma_f32_16x16x32_bf16 v[40:43], v[208:211], v[80:83], v[40:43]\n"
"ds_read_b128 v[72:75], v5 offset:33792\n"
"v_max3_u32 v134, v134, v80, v81\n"
"v_max3_u32 v134, v134, v82, v83\n"
"v_mfma_f32_16x16x32_bf16 v[48:51], v[212:215], v[80:83], v[48:51]\n"
"ds_read_b128 v[76:79], v5 offset:50176\n"
"v_mfma_f32_16x16x32_bf16 v[64:67], v[216:219], v[80:83], v[64:67]\n"
"v_max3_u32 v134, v134, v84, v85\n"
"v_max3_u32 v134, v134, v86, v87\n"
"v_mfma_f32_16x16x32_bf16 v[44:47], v[208:211], v[84:87], v[44:47]\n"
"v_mfma_f32_16x16x32_bf16 v[60:63], v[212:215], v[84:87], v[60:63]\n"
"v_mfma_f32_16x16x32_bf16 v[68:71], v[216:219], v[84:87], v[68:71]\n"
"s_waitcnt lgkmcnt(2)\n"
"v_mfma_f32_16x16x32_bf16 v[40:43], a[200:203], v[88:91], v[40:43]\n"
"ds_read_b128 v[80:83], v6 offset:33792\n"
"v_max3_u32 v134, v134, v88, v89\n"
"v_max3_u32 v134, v134, v90, v91\n"
"v_mfma_f32_16x16x32_bf16 v[48:51], a[204:207], v[88:91], v[48:51]\n"
"ds_read_b128 v[84:87], v6 offset:50176\n"
"v_mfma_f32_16x16x32_bf16 v[64:67], a[208:211], v[88:91], v[64:67]\n"
"v_max3_u32 v134, v134, v92, v93\n"
"v_max3_u32 v134, v134, v94, v95\n"
"v_mfma_f32_16x16x32_bf16 v[44:47], a[200:203], v[92:95], v[44:47]\n"
"v_mfma_f32_16x16x32_bf16 v[60:63], a[204:207], v[92:95], v[60:63]\n"
"v_mfma_f32_16x16x32_bf16 v[68:71], a[208:211], v[92:95], v[68:71]\n"
"s_waitcnt lgkmcnt(2)\n"
"v_mfma_f32_16x16x32_bf16 v[40:43], a[236:239], v[72:75], v[40:43]\n"
"ds_read_b128 v[88:91], v7 offset:33792\n"
"v_max3_u32 v134, v134, v72, v73\n"
"v_max3_u32 v134, v134, v74, v75\n"
"v_mfma_f32_16x16x32_bf16 v[48:51], a[240:243], v[72:75], v[48:51]\n"
"ds_read_b128 v[92:95], v7 offset:50176\n"
"v_mfma_f32_16x16x32_bf16 v[64:67], a[244:247], v[72:75], v[64:67]\n"
"v_max3_u32 v134, v134, v76, v77\n"
"v_max3_u32 v134, v134, v78, v79\n"
"v_mfma_f32_16x16x32_bf16 v[44:47], a[236:239], v[76:79], v[44:47]\n"
"v_mfma_f32_16x16x32_bf16 v[60:63], a[240:243], v[76:79], v[60:63]\n"
"v_mfma_f32_16x16x32_bf16 v[68:71], a[244:247], v[76:79], v[68:71]\n"
"s_waitcnt lgkmcnt(2)\n"
"v_mfma_f32_16x16x32_bf16 v[40:43], v[172:175], v[80:83], v[40:43]\n"
"ds_read_b128 v[72:75], v4 offset:35840\n"
"v_max3_u32 v134, v134, v80, v81\n"
"v_max3_u32 v134, v134, v82, v83\n"
"v_mfma_f32_16x16x32_bf16 v[48:51], v[176:179], v[80:83], v[48:51]\n"
"ds_read_b128 v[76:79], v4 offset:52224\n"
"v_mfma_f32_16x16x32_bf16 v[64:67], v[180:183], v[80:83], v[64:67]\n"
"v_max3_u32 v134, v134, v84, v85\n"
"v_max3_u32 v134, v134, v86, v87\n"
"v_mfma_f32_16x16x32_bf16 v[44:47], v[172:175], v[84:87], v[44:47]\n"
"v_mfma_f32_16x16x32_bf16 v[60:63], v[176:179], v[84:87], v[60:63]\n"
"v_mfma_f32_16x16x32_bf16 v[68:71], v[180:183], v[84:87], v[68:71]\n"
"s_waitcnt lgkmcnt(2)\n"
"v_mfma_f32_16x16x32_bf16 v[40:43], v[220:223], v[88:91], v[40:43]\n"
"ds_read_b128 v[80:83], v5 offset:34816\n"
"v_max3_u32 v134, v134, v88, v89\n"
"v_max3_u32 v134, v134, v90, v91\n"
"v_mfma_f32_16x16x32_bf16 v[48:51], v[224:227], v[88:91], v[48:51]\n"
"ds_read_b128 v[84:87], v5 offset:51200\n"
"v_mfma_f32_16x16x32_bf16 v[64:67], v[228:231], v[88:91], v[64:67]\n"
"v_max3_u32 v134, v134, v92, v93\n"
"v_max3_u32 v134, v134, v94, v95\n"
"v_mfma_f32_16x16x32_bf16 v[44:47], v[220:223], v[92:95], v[44:47]\n"
"v_mfma_f32_16x16x32_bf16 v[60:63], v[224:227], v[92:95], v[60:63]\n"
"v_mfma_f32_16x16x32_bf16 v[68:71], v[228:231], v[92:95], v[68:71]\n"
"s_waitcnt lgkmcnt(2)\n"
"v_mfma_f32_16x16x32_bf16 v[40:43], a[212:215], v[72:75], v[40:43]\n"
"ds_read_b128 v[88:91], v6 offset:34816\n"
"v_max3_u32 v134, v134, v72, v73\n"
"v_max3_u32 v134, v134, v74, v75\n"
"v_mfma_f32_16x16x32_bf16 v[48:51], a[216:219], v[72:75], v[48:51]\n"
"ds_read_b128 v[92:95], v6 offset:51200\n"
"v_mfma_f32_16x16x32_bf16 v[64:67], a[220:223], v[72:75], v[64:67]\n"
"v_max3_u32 v134, v134, v76, v77\n"
"v_max3_u32 v134, v134, v78, v79\n"
"v_mfma_f32_16x16x32_bf16 v[44:47], a[212:215], v[76:79], v[44:47]\n"
"v_mfma_f32_16x16x32_bf16 v[60:63], a[216:219], v[76:79], v[60:63]\n"
"v_mfma_f32_16x16x32_bf16 v[68:71], a[220:223], v[76:79], v[68:71]\n"
"s_waitcnt lgkmcnt(2)\n"
"v_mfma_f32_16x16x32_bf16 v[40:43], a[248:251], v[80:83], v[40:43]\n"
"ds_read_b128 v[72:75], v7 offset:34816\n"
"v_max3_u32 v134, v134, v80, v81\n"
"v_max3_u32 v134, v134, v82, v83\n"
"v_mfma_f32_16x16x32_bf16 v[48:51], a[252:255], v[80:83], v[48:51]\n"
"ds_read_b128 v[76:79], v7 offset:51200\n"
"v_mfma_f32_16x16x32_bf16 v[64:67], v[144:147], v[80:83], v[64:67]\n"
"v_max3_u32 v134, v134, v84, v85\n"
"v_max3_u32 v134, v134, v86, v87\n"
"v_mfma_f32_16x16x32_bf16 v[44:47], a[248:251], v[84:87], v[44:47]\n"
"v_mfma_f32_16x16x32_bf16 v[60:63], a[252:255], v[84:87], v[60:63]\n"
"v_mfma_f32_16x16x32_bf16 v[68:71], v[144:147], v[84:87], v[68:71]\n"
"s_waitcnt lgkmcnt(2)\n"
"v_mfma_f32_16x16x32_bf16 v[40:43], v[184:187], v[88:91], v[40:43]\n"
"ds_read_b128 v[80:83], v5 offset:35840\n"
"v_max3_u32 v134, v134, v88, v89\n"
"v_max3_u32 v134, v134, v90, v91\n"
"v_mfma_f32_16x16x32_bf16 v[48:51], v[188:191], v[88:91], v[48:51]\n"
"ds_read_b128 v[84:87], v5 offset:52224\n"
"v_mfma_f32_16x16x32_bf16 v[64:67], v[192:195], v[88:91], v[64:67]\n"
"v_max3_u32 v134, v134, v92, v93\n"
"v_max3_u32 v134, v134, v94, v95\n"
"v_mfma_f32_16x16x32_bf16 v[44:47], v[184:187], v[92:95], v[44:47]\n"
"v_mfma_f32_16x16x32_bf16 v[60:63], v[188:191], v[92:95], v[60:63]\n"
"v_mfma_f32_16x16x32_bf16 v[68:71], v[192:195], v[92:95], v[68:71]\n"
"s_waitcnt lgkmcnt(2)\n"
"v_mfma_f32_16x16x32_bf16 v[40:43], v[232:235], v[72:75], v[40:43]\n"
"ds_read_b128 v[88:91], v6 offset:35840\n"
"v_max3_u32 v134, v134, v72, v73\n"
"v_max3_u32 v134, v134, v74, v75\n"
"v_mfma_f32_16x16x32_bf16 v[48:51], v[236:239], v[72:75], v[48:51]\n"
"ds_read_b128 v[92:95], v6 offset:52224\n"
"v_mfma_f32_16x16x32_bf16 v[64:67], v[240:243], v[72:75], v[64:67]\n"
"v_max3_u32 v134, v134, v76, v77\n"
"v_max3_u32 v134, v134, v78, v79\n"
"v_mfma_f32_16x16x32_bf16 v[44:47], v[232:235], v[76:79], v[44:47]\n"
"v_mfma_f32_16x16x32_bf16 v[60:63], v[236:239], v[76:79], v[60:63]\n"
"v_mfma_f32_16x16x32_bf16 v[68:71], v[240:243], v[76:79], v[68:71]\n"
"s_waitcnt lgkmcnt(2)\n"
"v_mfma_f32_16x16x32_bf16 v[40:43], v[148:151], v[80:83], v[40:43]\n"
"ds_read_b128 v[72:75], v7 offset:35840\n"
"v_max3_u32 v134, v134, v80, v81\n"
"v_max3_u32 v134, v134, v82, v83\n"
"v_mfma_f32_16x16x32_bf16 v[48:51], v[152:155], v[80:83], v[48:51]\n"
"ds_read_b128 v[76:79], v7 offset:52224\n"
"v_mfma_f32_16x16x32_bf16 v[64:67], v[156:159], v[80:83], v[64:67]\n"
"v_max3_u32 v134, v134, v84, v85\n"
"v_max3_u32 v134, v134, v86, v87\n"
"v_mfma_f32_16x16x32_bf16 v[44:47], v[148:151], v[84:87], v[44:47]\n"
"v_mfma_f32_16x16x32_bf16 v[60:63], v[152:155], v[84:87], v[60:63]\n"
"v_mfma_f32_16x16x32_bf16 v[68:71], v[156:159], v[84:87], v[68:71]\n"
"s_waitcnt lgkmcnt(2)\n"
"v_mfma_f32_16x16x32_bf16 v[40:43], v[196:199], v[88:91], v[40:43]\n"
"v_max3_u32 v134, v134, v88, v89\n"
"v_max3_u32 v134, v134, v90, v91\n"
"v_mfma_f32_16x16x32_bf16 v[48:51], v[200:203], v[88:91], v[48:51]\n"
"v_mfma_f32_16x16x32_bf16 v[64:67], v[204:207], v[88:91], v[64:67]\n"
"v_max3_u32 v134, v134, v92, v93\n"
"v_max3_u32 v134, v134, v94, v95\n"
"v_mfma_f32_16x16x32_bf16 v[44:47], v[196:199], v[92:95], v[44:47]\n"
"v_mfma_f32_16x16x32_bf16 v[60:63], v[200:203], v[92:95], v[60:63]\n"
"v_mfma_f32_16x16x32_bf16 v[68:71], v[204:207], v[92:95], v[68:71]\n"
"s_waitcnt lgkmcnt(0)\n"
"v_mfma_f32_16x16x32_bf16 v[40:43], v[244:247], v[72:75], v[40:43]\n"
"v_max3_u32 v134, v134, v72, v73\n"
"v_max3_u32 v134, v134, v74, v75\n"
"v_mfma_f32_16x16x32_bf16 v[48:51], v[248:251], v[72:75], v[48:51]\n"
"v_mfma_f32_16x16x32_bf16 v[64:67], v[252:255], v[72:75], v[64:67]\n"
"v_max3_u32 v134, v134, v76, v77\n"
"v_max3_u32 v134, v134, v78, v79\n"
"v_mfma_f32_16x16x32_bf16 v[44:47], v[244:247], v[76:79], v[44:47]\n"
"v_mfma_f32_16x16x32_bf16 v[60:63], v[248:251], v[76:79], v[60:63]\n"
"v_mfma_f32_16x16x32_bf16 v[68:71], v[252:255], v[76:79], v[68:71]\n"
"v_cmp_eq_u32 vcc, -1, v134\n"
"s_cbranch_vccz Lbackx01x\n"
"s_add_u32 s64, s64, 1\n"
"s_cmp_lt_u32 s64, 0x4000\n"
"s_cbranch_scc1 Lslowx01l\n"
"Lbackx01x:\n"
"ds_read_b128 v[80:83], v10\n"
"ds_read_b128 v[84:87], v10 offset:64\n"
"ds_read_b128 v[88:91], v10 offset:128\n"
"s_waitcnt vmcnt(0) lgkmcnt(0)\n"
"s_mov_b32 m0, s65\n"
"s_nop 7\n"
"s_nop 7\n"
"s_branch Lbackx01\n"
"Lslow8_h00:\n"
"s_waitcnt vmcnt(0) lgkmcnt(0)\n"
"s_nop 7\n"
"s_nop 7\n"
"s_mov_b32 s64, 0\n"
"s_mov_b32 s65, m0\n"
"Lslow8_h00l:\n"
"s_sleep 1\n"
"buffer_load_dwordx4 v[0:3], v142, s[8:11], s85 offen sc1\n"
"buffer_load_dwordx4 v[16:19], v142, s[8:11], s85 offen offset:1024 sc1\n"
"buffer_load_dwordx4 v[28:31], v142, s[8:11], s85 offen offset:2048 sc1\n"
"buffer_load_dwordx4 v[96:99], v142, s[8:11], s85 offen offset:3072 sc1\n"
"buffer_load_dwordx4 v[100:103], v142, s[8:11], s88 offen sc1\n"
"buffer_load_dwordx4 v[104:107], v142, s[8:11], s88 offen offset:1024 sc1\n"
"buffer_load_dwordx4 v[108:111], v142, s[8:11], s88 offen offset:2048 sc1\n"
"buffer_load_dwordx4 v[112:115], v142, s[8:11], s88 offen offset:3072 sc1\n"
"buffer_load_dwordx4 v[116:119], v142, s[8:11], s89 offen sc1\n"
"buffer_load_dwordx4 v[20:23], v142, s[8:11], s89 offen offset:1024 sc1\n"
"buffer_load_dwordx4 v[24:27], v142, s[8:11], s89 offen offset:2048 sc1\n"
"buffer_load_dwordx4 v[56:59], v142, s[8:11], s89 offen offset:3072 sc1\n"
"buffer_load_dwordx4 v[32:35], v142, s[8:11], s90 offen sc1\n"
"buffer_load_dwordx4 v[36:39], v142, s[8:11], s90 offen offset:1024 sc1\n"
"s_waitcnt vmcnt(13)\n"
"v_xor_b32 v0, v132, v0\n"
"s_waitcnt vmcnt(12)\n"
"v_xor_b32 v16, v132, v16\n"
"s_waitcnt vmcnt(11)\n"
"v_xor_b32 v28, v132, v28\n"
"s_waitcnt vmcnt(10)\n"
"v_xor_b32 v96, v132, v96\n"
"s_waitcnt vmcnt(0) lgkmcnt(0)\n"
"v_or3_b32 v134, v0, v1, v2\n"
"v_or3_b32 v134, v134, v3, v16\n"
"v_or3_b32 v134, v134, v17, v18\n"
"v_or3_b32 v134, v134, v19, v28\n"
"v_or3_b32 v134, v134, v29, v30\n"
"v_or3_b32 v134, v134, v31, v96\n"
"v_or3_b32 v134, v134, v97, v98\n"
"v_or3_b32 v134, v134, v99, v99\n"
"v_and_b32 v134, s38, v134\n"
"v_cmp_ne_u32 vcc, 0, v134\n"
"s_cbranch_vccz Lback8_h00x\n"
"s_add_u32 s64, s64, 1\n"
"s_cmp_lt_u32 s64, 0x4000\n"
"s_cbranch_scc1 Lslow8_h00l\n"
"Lback8_h00x:\n"
"s_waitcnt vmcnt(0) lgkmcnt(0)\n"
"s_mov_b32 m0, s65\n"
"s_nop 7\n"
"s_branch Lback8_h00\n"
"Lslow9_h01:\n"
"s_waitcnt vmcnt(0) lgkmcnt(0)\n"
"s_nop 7\n"
"s_nop 7\n"
"s_mov_b32 s64, 0\n"
"s_mov_b32 s65, m0\n"
"Lslow9_h01l:\n"
"s_sleep 1\n"
"buffer_load_dwordx4 v[100:103], v142, s[8:11], s88 offen sc1\n"
"buffer_load_dwordx4 v[104:107], v142, s[8:11], s88 offen offset:1024 sc1\n"
"buffer_load_dwordx4 v[108:111], v142, s[8:11], s88 offen offset:2048 sc1\n"
"buffer_load_dwordx4 v[112:115], v142, s[8:11], s88 offen offset:3072 sc1\n"
"buffer_load_dwordx4 v[116:119], v142, s[8:11], s89 offen sc1\n"
"buffer_load_dwordx4 v[20:23], v142, s[8:11], s89 offen offset:1024 sc1\n"
"buffer_load_dwordx4 v[24:27], v142, s[8:11], s89 offen offset:2048 sc1\n"
"buffer_load_dwordx4 v[56:59], v142, s[8:11], s89 offen offset:3072 sc1\n"
"buffer_load_dwordx4 v[32:35], v142, s[8:11], s90 offen sc1\n"
"buffer_load_dwordx4 v[36:39], v142, s[8:11], s90 offen offset:1024 sc1\n"
"buffer_load_dwordx4 v[40:43], v142, s[8:11], s90 offen offset:2048 sc1\n"
"buffer_load_dwordx4 v[44:47], v142, s[8:11], s90 offen offset:3072 sc1\n"
"s_waitcnt vmcnt(11)\n"
"v_xor_b32 v100, v132, v100\n"
"s_waitcnt vmcnt(10)\n"
"v_xor_b32 v104, v132, v104\n"
"s_waitcnt vmcnt(9)\n"
"v_xor_b32 v108, v132, v108\n"
"s_waitcnt vmcnt(8)\n"
"v_xor_b32 v112, v132, v112\n"
"s_waitcnt vmcnt(0) lgkmcnt(0)\n"
"v_or3_b32 v134, v100, v101, v102\n"
"v_or3_b32 v134, v134, v103, v104\n"
"v_or3_b32 v134, v134, v105, v106\n"
"v_or3_b32 v134, v134, v107, v108\n"
"v_or3_b32 v134, v134, v109, v110\n"
"v_or3_b32 v134, v134, v111, v112\n"
"v_or3_b32 v134, v134, v113, v114\n"
"v_or3_b32 v134, v134, v115, v115\n"
"v_and_b32 v134, s38, v134\n"
"v_cmp_ne_u32 vcc, 0, v134\n"
"s_cbranch_vccz Lback9_h01x\n"
"s_add_u32 s64, s64, 1\n"
"s_cmp_lt_u32 s64, 0x4000\n"
"s_cbranch_scc1 Lslow9_h01l\n"
"Lback9_h01x:\n"
"s_waitcnt vmcnt(0) lgkmcnt(0)\n"
"s_mov_b32 m0, s65\n"
"s_nop 7\n"
"s_branch Lback9_h01\n"
"Lslow10_h02:\n"
"s_waitcnt vmcnt(0) lgkmcnt(0)\n"
"s_nop 7\n"
"s_nop 7\n"
"s_mov_b32 s64, 0\n"
"s_mov_b32 s65, m0\n"
"Lslow10_h02l:\n"
"s_sleep 1\n"
"buffer_load_dwordx4 v[116:119], v142, s[8:11], s89 offen sc1\n"
"buffer_load_dwordx4 v[20:23], v142, s[8:11], s89 offen offset:1024 sc1\n"
"buffer_load_dwordx4 v[24:27], v142, s[8:11], s89 offen offset:2048 sc1\n"
"buffer_load_dwordx4 v[56:59], v142, s[8:11], s89 offen offset:3072 sc1\n"
"buffer_load_dwordx4 v[32:35], v142, s[8:11], s90 offen sc1\n"
"buffer_load_dwordx4 v[36:39], v142, s[8:11], s90 offen offset:1024 sc1\n"
"buffer_load_dwordx4 v[40:43], v142, s[8:11], s90 offen offset:2048 sc1\n"
"buffer_load_dwordx4 v[44:47], v142, s[8:11], s90 offen offset:3072 sc1\n"
"buffer_load_dwordx4 v[48:51], v142, s[8:11], s91 offen sc1\n"
"buffer_load_dwordx4 v[92:95], v142, s[8:11], s91 offen offset:1024 sc1\n"
"buffer_load_dwordx4 v[120:123], v142, s[8:11], s91 offen offset:2048 sc1\n"
"buffer_load_dwordx4 v[4:7], v142, s[8:11], s91 offen offset:3072 sc1\n"
"s_waitcnt vmcnt(11)\n"
"v_xor_b32 v116, v132, v116\n"
"s_waitcnt vmcnt(10)\n"
"v_xor_b32 v20, v132, v20\n"
"s_waitcnt vmcnt(9)\n"
"v_xor_b32 v24, v132, v24\n"
"s_waitcnt vmcnt(8)\n"
"v_xor_b32 v56, v132, v56\n"
"s_waitcnt vmcnt(0) lgkmcnt(0)\n"
"v_or3_b32 v134, v116, v117, v118\n"
"v_or3_b32 v134, v134, v119, v20\n"
"v_or3_b32 v134, v134, v21, v22\n"
"v_or3_b32 v134, v134, v23, v24\n"
"v_or3_b32 v134, v134, v25, v26\n"
"v_or3_b32 v134, v134, v27, v56\n"
"v_or3_b32 v134, v134, v57, v58\n"
"v_or3_b32 v134, v134, v59, v59\n"
"v_and_b32 v134, s38, v134\n"
"v_cmp_ne_u32 vcc, 0, v134\n"
"s_cbranch_vccz Lback10_h02x\n"
"s_add_u32 s64, s64, 1\n"
"s_cmp_lt_u32 s64, 0x4000\n"
"s_cbranch_scc1 Lslow10_h02l\n"
"Lback10_h02x:\n"
"s_waitcnt vmcnt(0) lgkmcnt(0)\n"
"s_mov_b32 m0, s65\n"
"s_nop 7\n"
"s_branch Lback10_h02\n"
"Lslow11_h03:\n"
"s_waitcnt vmcnt(0) lgkmcnt(0)\n"
"s_nop 7\n"
"s_nop 7\n"
"s_mov_b32 s64, 0\n"
"s_mov_b32 s65, m0\n"
"Lslow11_h03l:\n"
"s_sleep 1\n"
"buffer_load_dwordx4 v[32:35], v142, s[8:11], s90 offen sc1\n"
"buffer_load_dwordx4 v[36:39], v142, s[8:11], s90 offen offset:1024 sc1\n"
"buffer_load_dwordx4 v[40:43], v142, s[8:11], s90 offen offset:2048 sc1\n"
"buffer_load_dwordx4 v[44:47], v142, s[8:11], s90 offen offset:3072 sc1\n"
"buffer_load_dwordx4 v[48:51], v142, s[8:11], s91 offen sc1\n"
"buffer_load_dwordx4 v[92:95], v142, s[8:11], s91 offen offset:1024 sc1\n"
"buffer_load_dwordx4 v[120:123], v142, s[8:11], s91 offen offset:2048 sc1\n"
"buffer_load_dwordx4 v[4:7], v142, s[8:11], s91 offen offset:3072 sc1\n"
"buffer_load_dwordx4 v[8:11], v142, s[8:11], s92 offen sc1\n"
"buffer_load_dwordx4 v[12:15], v142, s[8:11], s92 offen offset:1024 sc1\n"
"s_waitcnt vmcnt(9)\n"
"v_xor_b32 v32, v132, v32\n"
"s_waitcnt vmcnt(8)\n"
"v_xor_b32 v36, v132, v36\n"
"s_waitcnt vmcnt(7)\n"
"v_xor_b32 v40, v132, v40\n"
"s_waitcnt vmcnt(6)\n"
"v_xor_b32 v44, v132, v44\n"
"s_waitcnt vmcnt(0) lgkmcnt(0)\n"
"v_or3_b32 v134, v32, v33, v34\n"
"v_or3_b32 v134, v134, v35, v36\n"
"v_or3_b32 v134, v134, v37, v38\n"
"v_or3_b32 v134, v134, v39, v40\n"
"v_or3_b32 v134, v134, v41, v42\n"
"v_or3_b32 v134, v134, v43, v44\n"
"v_or3_b32 v134, v134, v45, v46\n"
"v_or3_b32 v134, v134, v47, v47\n"
"v_and_b32 v134, s38, v134\n"
"v_cmp_ne_u32 vcc, 0, v134\n"
"s_cbranch_vccz Lback11_h03x\n"
"s_add_u32 s64, s64, 1\n"
"s_cmp_lt_u32 s64, 0x4000\n"
"s_cbranch_scc1 Lslow11_h03l\n"
"Lback11_h03x:\n"
"s_waitcnt vmcnt(0) lgkmcnt(0)\n"
"s_mov_b32 m0, s65\n"
"s_nop 7\n"
"s_branch Lback11_h03\n"
"Lslow12_h04:\n"
"s_waitcnt vmcnt(0) lgkmcnt(0)\n"
"s_nop 7\n"
"s_nop 7\n"
"s_mov_b32 s64, 0\n"
"s_mov_b32 s65, m0\n"
"Lslow12_h04l:\n"
"s_sleep 1\n"
"buffer_load_dwordx4 v[48:51], v142, s[8:11], s91 offen sc1\n"
"buffer_load_dwordx4 v[92:95], v142, s[8:11], s91 offen offset:1024 sc1\n"
"buffer_load_dwordx4 v[120:123], v142, s[8:11], s91 offen offset:2048 sc1\n"
"buffer_load_dwordx4 v[4:7], v142, s[8:11], s91 offen offset:3072 sc1\n"
"buffer_load_dwordx4 v[8:11], v142, s[8:11], s92 offen sc1\n"
"buffer_load_dwordx4 v[12:15], v142, s[8:11], s92 offen offset:1024 sc1\n"
"s_waitcnt vmcnt(5)\n"
"v_xor_b32 v48, v132, v48\n"
"s_waitcnt vmcnt(4)\n"
"v_xor_b32 v92, v132, v92\n"
"s_waitcnt vmcnt(3)\n"
"v_xor_b32 v120, v132, v120\n"
"s_waitcnt vmcnt(2)\n"
"v_xor_b32 v4, v132, v4\n"
"s_waitcnt vmcnt(1)\n"
"v_xor_b32 v8, v132, v8\n"
"s_waitcnt vmcnt(0)\n"
"v_xor_b32 v12, v132, v12\n"
"s_waitcnt vmcnt(0) lgkmcnt(0)\n"
"v_or3_b32 v134, v48, v49, v50\n"
"v_or3_b32 v134, v134, v51, v92\n"
"v_or3_b32 v134, v134, v93, v94\n"
"v_or3_b32 v134, v134, v95, v120\n"
"v_or3_b32 v134, v134, v121, v122\n"
"v_or3_b32 v134, v134, v123, v4\n"
"v_or3_b32 v134, v134, v5, v6\n"
"v_or3_b32 v134, v134, v7, v8\n"
"v_or3_b32 v134, v134, v9, v10\n"
"v_or3_b32 v134, v134, v11, v12\n"
"v_or3_b32 v134, v134, v13, v14\n"
"v_or3_b32 v134, v134, v15, v15\n"
"v_and_b32 v134, s38, v134\n"
"v_cmp_ne_u32 vcc, 0, v134\n"
"s_cbranch_vccz Lback12_h04x\n"
"s_add_u32 s64, s64, 1\n"
"s_cmp_lt_u32 s64, 0x4000\n"
"s_cbranch_scc1 Lslow12_h04l\n"
"Lback12_h04x:\n"
"s_waitcnt vmcnt(0) lgkmcnt(0)\n"
"s_mov_b32 m0, s65\n"
"s_nop 7\n"
"s_branch Lback12_h04\n"
"Lslow13_h05:\n"
"s_waitcnt vmcnt(0) lgkmcnt(0)\n"
"s_nop 7\n"
"s_nop 7\n"
"s_mov_b32 s64, 0\n"
"s_mov_b32 s65, m0\n"
"Lslow13_h05l:\n"
"s_sleep 1\n"
"buffer_load_dwordx4 v[0:3], v125, s[8:11], s85 offen sc1\n"
"buffer_load_dwordx4 v[16:19], v125, s[8:11], s85 offen offset:1024 sc1\n"
"buffer_load_dwordx4 v[28:31], v125, s[8:11], s85 offen offset:2048 sc1\n"
"buffer_load_dwordx4 v[96:99], v125, s[8:11], s85 offen offset:3072 sc1\n"
"buffer_load_dwordx4 v[100:103], v125, s[8:11], s88 offen sc1\n"
"buffer_load_dwordx4 v[104:107], v125, s[8:11], s88 offen offset:1024 sc1\n"
"buffer_load_dwordx4 v[108:111], v125, s[8:11], s88 offen offset:2048 sc1\n"
"buffer_load_dwordx4 v[40:43], v125, s[8:11], s88 offen offset:3072 sc1\n"
"s_waitcnt vmcnt(7)\n"
"v_xor_b32 v0, v132, v0\n"
"s_waitcnt vmcnt(6)\n"
"v_xor_b32 v16, v132, v16\n"
"s_waitcnt vmcnt(5)\n"
"v_xor_b32 v28, v132, v28\n"
"s_waitcnt vmcnt(4)\n"
"v_xor_b32 v96, v132, v96\n"
"s_waitcnt vmcnt(0) lgkmcnt(0)\n"
"v_or3_b32 v134, v0, v1, v2\n"
"v_or3_b32 v134, v134, v3, v16\n"
"v_or3_b32 v134, v134, v17, v18\n"
"v_or3_b32 v134, v134, v19, v28\n"
"v_or3_b32 v134, v134, v29, v30\n"
"v_or3_b32 v134, v134, v31, v96\n"
"v_or3_b32 v134, v134, v97, v98\n"
"v_or3_b32 v134, v134, v99, v99\n"
"v_and_b32 v134, s38, v134\n"
"v_cmp_ne_u32 vcc, 0, v134\n"
"s_cbranch_vccz Lback13_h05x\n"
"s_add_u32 s64, s64, 1\n"
"s_cmp_lt_u32 s64, 0x4000\n"
"s_cbranch_scc1 Lslow13_h05l\n"
"Lback13_h05x:\n"
"s_waitcnt vmcnt(0) lgkmcnt(0)\n"
"s_mov_b32 m0, s65\n"
"s_nop 7\n"
"s_branch Lback13_h05\n"
"Lslow14_h06:\n"
"s_waitcnt vmcnt(0) lgkmcnt(0)\n"
"s_nop 7\n"
"s_nop 7\n"
"s_mov_b32 s64, 0\n"
"s_mov_b32 s65, m0\n"
"Lslow14_h06l:\n"
"s_sleep 1\n"
"buffer_load_dwordx4 v[100:103], v125, s[8:11], s88 offen sc1\n"
"buffer_load_dwordx4 v[104:107], v125, s[8:11], s88 offen offset:1024 sc1\n"
"buffer_load_dwordx4 v[108:111], v125, s[8:11], s88 offen offset:2048 sc1\n"
"buffer_load_dwordx4 v[40:43], v125, s[8:11], s88 offen offset:3072 sc1\n"
"buffer_load_dwordx4 v[44:47], v125, s[8:11], s89 offen sc1\n"
"buffer_load_dwordx4 v[48:51], v125, s[8:11], s89 offen offset:1024 sc1\n"
"buffer_load_dwordx4 v[92:95], v125, s[8:11], s89 offen offset:2048 sc1\n"
"buffer_load_dwordx4 v[120:123], v125, s[8:11], s89 offen offset:3072 sc1\n"
"buffer_load_dwordx4 v[4:7], v125, s[8:11], s90 offen sc1\n"
"s_waitcnt vmcnt(8)\n"
"v_xor_b32 v100, v132, v100\n"
"s_waitcnt vmcnt(7)\n"
"v_xor_b32 v104, v132, v104\n"
"s_waitcnt vmcnt(6)\n"
"v_xor_b32 v108, v132, v108\n"
"s_waitcnt vmcnt(5)\n"
"v_xor_b32 v40, v132, v40\n"
"s_waitcnt vmcnt(0) lgkmcnt(0)\n"
"v_or3_b32 v134, v100, v101, v102\n"
"v_or3_b32 v134, v134, v103, v104\n"
"v_or3_b32 v134, v134, v105, v106\n"
"v_or3_b32 v134, v134, v107, v108\n"
"v_or3_b32 v134, v134, v109, v110\n"
"v_or3_b32 v134, v134, v111, v40\n"
"v_or3_b32 v134, v134, v41, v42\n"
"v_or3_b32 v134, v134, v43, v43\n"
"v_and_b32 v134, s38, v134\n"
"v_cmp_ne_u32 vcc, 0, v134\n"
"s_cbranch_vccz Lback14_h06x\n"
"s_add_u32 s64, s64, 1\n"
"s_cmp_lt_u32 s64, 0x4000\n"
"s_cbranch_scc1 Lslow14_h06l\n"
"Lback14_h06x:\n"
"s_waitcnt vmcnt(0) lgkmcnt(0)\n"
"s_mov_b32 m0, s65\n"
"s_nop 7\n"
"s_branch Lback14_h06\n"
"Lslow15_h07:\n"
"s_waitcnt vmcnt(0) lgkmcnt(0)\n"
"s_nop 7\n"
"s_nop 7\n"
"s_mov_b32 s64, 0\n"
"s_mov_b32 s65, m0\n"
"Lslow15_h07l:\n"
"s_sleep 1\n"
"buffer_load_dwordx4 v[44:47], v125, s[8:11], s89 offen sc1\n"
"buffer_load_dwordx4 v[48:51], v125, s[8:11], s89 offen offset:1024 sc1\n"
"buffer_load_dwordx4 v[92:95], v125, s[8:11], s89 offen offset:2048 sc1\n"
"buffer_load_dwordx4 v[120:123], v125, s[8:11], s89 offen offset:3072 sc1\n"
"buffer_load_dwordx4 v[4:7], v125, s[8:11], s90 offen sc1\n"
"buffer_load_dwordx4 v[8:11], v125, s[8:11], s90 offen offset:1024 sc1\n"
"buffer_load_dwordx4 v[12:15], v125, s[8:11], s90 offen offset:2048 sc1\n"
"buffer_load_dwordx4 v[0:3], v125, s[8:11], s90 offen offset:3072 sc1\n"
"buffer_load_dwordx4 v[16:19], v125, s[8:11], s91 offen sc1\n"
"buffer_load_dwordx4 v[28:31], v125, s[8:11], s91 offen offset:1024 sc1\n"
"buffer_load_dwordx4 v[96:99], v125, s[8:11], s91 offen offset:2048 sc1\n"
"buffer_load_dwordx4 v[100:103], v125, s[8:11], s91 offen offset:3072 sc1\n"
"s_waitcnt vmcnt(11)\n"
"v_xor_b32 v44, v132, v44\n"
"s_waitcnt vmcnt(10)\n"
"v_xor_b32 v48, v132, v48\n"
"s_waitcnt vmcnt(9)\n"
"v_xor_b32 v92, v132, v92\n"
"s_waitcnt vmcnt(8)\n"
"v_xor_b32 v120, v132, v120\n"
"s_waitcnt vmcnt(0) lgkmcnt(0)\n"
"v_or3_b32 v134, v44, v45, v46\n"
"v_or3_b32 v134, v134, v47, v48\n"
"v_or3_b32 v134, v134, v49, v50\n"
"v_or3_b32 v134, v134, v51, v92\n"
"v_or3_b32 v134, v134, v93, v94\n"
"v_or3_b32 v134, v134, v95, v120\n"
"v_or3_b32 v134, v134, v121, v122\n"
"v_or3_b32 v134, v134, v123, v123\n"
"v_and_b32 v134, s38, v134\n"
"v_cmp_ne_u32 vcc, 0, v134\n"
"s_cbranch_vccz Lback15_h07x\n"
"s_add_u32 s64, s64, 1\n"
"s_cmp_lt_u32 s64, 0x4000\n"
"s_cbranch_scc1 Lslow15_h07l\n"
"Lback15_h07x:\n"
"s_waitcnt vmcnt(0) lgkmcnt(0)\n"
"s_mov_b32 m0, s65\n"
"s_nop 7\n"
"s_branch Lback15_h07\n"
"Lslow16_h08:\n"
"s_waitcnt vmcnt(0) lgkmcnt(0)\n"
"s_nop 7\n"
"s_nop 7\n"
"s_mov_b32 s64, 0\n"
"s_mov_b32 s65, m0\n"
"Lslow16_h08l:\n"
"s_sleep 1\n"
"buffer_load_dwordx4 v[4:7], v125, s[8:11], s90 offen sc1\n"
"buffer_load_dwordx4 v[8:11], v125, s[8:11], s90 offen offset:1024 sc1\n"
"buffer_load_dwordx4 v[12:15], v125, s[8:11], s90 offen offset:2048 sc1\n"
"buffer_load_dwordx4 v[0:3], v125, s[8:11], s90 offen offset:3072 sc1\n"
"buffer_load_dwordx4 v[16:19], v125, s[8:11], s91 offen sc1\n"
"buffer_load_dwordx4 v[28:31], v125, s[8:11], s91 offen offset:1024 sc1\n"
"buffer_load_dwordx4 v[96:99], v125, s[8:11], s91 offen offset:2048 sc1\n"
"buffer_load_dwordx4 v[100:103], v125, s[8:11], s91 offen offset:3072 sc1\n"
"buffer_load_dwordx4 v[104:107], v125, s[8:11], s92 offen sc1\n"
"buffer_load_dwordx4 v[108:111], v125, s[8:11], s92 offen offset:1024 sc1\n"
"s_waitcnt vmcnt(9)\n"
"v_xor_b32 v4, v132, v4\n"
"s_waitcnt vmcnt(8)\n"
"v_xor_b32 v8, v132, v8\n"
"s_waitcnt vmcnt(7)\n"
"v_xor_b32 v12, v132, v12\n"
"s_waitcnt vmcnt(6)\n"
"v_xor_b32 v0, v132, v0\n"
"s_waitcnt vmcnt(0) lgkmcnt(0)\n"
"v_or3_b32 v134, v4, v5, v6\n"
"v_or3_b32 v134, v134, v7, v8\n"
"v_or3_b32 v134, v134, v9, v10\n"
"v_or3_b32 v134, v134, v11, v12\n"
"v_or3_b32 v134, v134, v13, v14\n"
"v_or3_b32 v134, v134, v15, v0\n"
"v_or3_b32 v134, v134, v1, v2\n"
"v_or3_b32 v134, v134, v3, v3\n"
"v_and_b32 v134, s38, v134\n"
"v_cmp_ne_u32 vcc, 0, v134\n"
"s_cbranch_vccz Lback16_h08x\n"
"s_add_u32 s64, s64, 1\n"
"s_cmp_lt_u32 s64, 0x4000\n"
"s_cbranch_scc1 Lslow16_h08l\n"
"Lback16_h08x:\n"
"s_waitcnt vmcnt(0) lgkmcnt(0)\n"
"s_mov_b32 m0, s65\n"
"s_nop 7\n"
"s_branch Lback16_h08\n"
"Lslow17_h09:\n"
"s_waitcnt vmcnt(0) lgkmcnt(0)\n"
"s_nop 7\n"
"s_nop 7\n"
"s_mov_b32 s64, 0\n"
"s_mov_b32 s65, m0\n"
"Lslow17_h09l:\n"
"s_sleep 1\n"
"buffer_load_dwordx4 v[16:19], v125, s[8:11], s91 offen sc1\n"
"buffer_load_dwordx4 v[28:31], v125, s[8:11], s91 offen offset:1024 sc1\n"
"buffer_load_dwordx4 v[96:99], v125, s[8:11], s91 offen offset:2048 sc1\n"
"buffer_load_dwordx4 v[100:103], v125, s[8:11], s91 offen offset:3072 sc1\n"
"buffer_load_dwordx4 v[104:107], v125, s[8:11], s92 offen sc1\n"
"buffer_load_dwordx4 v[108:111], v125, s[8:11], s92 offen offset:1024 sc1\n"
"s_waitcnt vmcnt(5)\n"
"v_xor_b32 v16, v132, v16\n"
"s_waitcnt vmcnt(4)\n"
"v_xor_b32 v28, v132, v28\n"
"s_waitcnt vmcnt(3)\n"
"v_xor_b32 v96, v132, v96\n"
"s_waitcnt vmcnt(2)\n"
"v_xor_b32 v100, v132, v100\n"
"s_waitcnt vmcnt(1)\n"
"v_xor_b32 v104, v132, v104\n"
"s_waitcnt vmcnt(0)\n"
"v_xor_b32 v108, v132, v108\n"
"s_waitcnt vmcnt(0) lgkmcnt(0)\n"
"v_or3_b32 v134, v16, v17, v18\n"
"v_or3_b32 v134, v134, v19, v28\n"
"v_or3_b32 v134, v134, v29, v30\n"
"v_or3_b32 v134, v134, v31, v96\n"
"v_or3_b32 v134, v134, v97, v98\n"
"v_or3_b32 v134, v134, v99, v100\n"
"v_or3_b32 v134, v134, v101, v102\n"
"v_or3_b32 v134, v134, v103, v104\n"
"v_or3_b32 v134, v134, v105, v106\n"
"v_or3_b32 v134, v134, v107, v108\n"
"v_or3_b32 v134, v134, v109, v110\n"
"v_or3_b32 v134, v134, v111, v111\n"
"v_and_b32 v134, s38, v134\n"
"v_cmp_ne_u32 vcc, 0, v134\n"
"s_cbranch_vccz Lback17_h09x\n"
"s_add_u32 s64, s64, 1\n"
"s_cmp_lt_u32 s64, 0x4000\n"
"s_cbranch_scc1 Lslow17_h09l\n"
"Lback17_h09x:\n"
"s_waitcnt vmcnt(0) lgkmcnt(0)\n"
"s_mov_b32 m0, s65\n"
"s_nop 7\n"
"s_branch Lback17_h09\n"
"Lslow18_sa:\n"
"s_waitcnt vmcnt(0) lgkmcnt(0)\n"
"s_nop 7\n"
"s_nop 7\n"
"s_mov_b32 s64, 0\n"
"s_mov_b32 s65, m0\n"
"Lslow18_sal:\n"
"s_sleep 1\n"
"s_add_u32 s59, s74, 0x40000\n"
"buffer_load_dwordx2 v[130:131], v136, s[8:11], s59 offen sc1\n"
"s_waitcnt vmcnt(0) lgkmcnt(0)\n"
"v_max_u32 v134, v130, v131\n"
"v_cmp_eq_u32 vcc, -1, v134\n"
"s_cbranch_vccz Lback18_sax\n"
"s_add_u32 s64, s64, 1\n"
"s_cmp_lt_u32 s64, 0x4000\n"
"s_cbranch_scc1 Lslow18_sal\n"
"Lback18_sax:\n"
"s_waitcnt vmcnt(0) lgkmcnt(0)\n"
"s_mov_b32 m0, s65\n"
"s_nop 7\n"
"s_branch Lback18_sa\n"
"Lslow19_sb:\n"
"s_waitcnt vmcnt(0) lgkmcnt(0)\n"
"s_nop 7\n"
"s_nop 7\n"
"s_mov_b32 s64, 0\n"
"s_mov_b32 s65, m0\n"
"Lslow19_sbl:\n"
"s_sleep 1\n"
"s_add_u32 s59, s74, 0x40800\n"
"buffer_load_dwordx2 v[128:129], v136, s[8:11], s59 offen sc1\n"
"s_waitcnt vmcnt(0) lgkmcnt(0)\n"
"v_max_u32 v134, v128, v129\n"
"v_cmp_eq_u32 vcc, -1, v134\n"
"s_cbranch_vccz Lback19_sbx\n"
"s_add_u32 s64, s64, 1\n"
"s_cmp_lt_u32 s64, 0x4000\n"
"s_cbranch_scc1 Lslow19_sbl\n"
"Lback19_sbx:\n"
"s_waitcnt vmcnt(0) lgkmcnt(0)\n"
"s_mov_b32 m0, s65\n"
"s_nop 7\n"
"s_branch Lback19_sb\n"
"Lslow20_xs10:\n"
"s_waitcnt vmcnt(0) lgkmcnt(0)\n"
"s_nop 7\n"
"s_nop 7\n"
"s_mov_b32 s64, 0\n"
"s_mov_b32 s65, m0\n"
"Lslow20_xs10l:\n"
"s_sleep 1\n"
"s_add_u32 s59, s93, 0x60000\n"
"buffer_load_dwordx2 v[0:1], v11, s[8:11], s59 offen sc1\n"
"s_add_u32 s59, s93, 0x60000\n"
"buffer_load_dwordx2 v[2:3], v11, s[8:11], s59 offen offset:128 sc1\n"
"s_add_u32 s59, s93, 0x60400\n"
"buffer_load_dwordx2 v[16:17], v11, s[8:11], s59 offen sc1\n"
"s_add_u32 s59, s93, 0x60400\n"
"buffer_load_dwordx2 v[18:19], v11, s[8:11], s59 offen offset:128 sc1\n"
"s_add_u32 m0, s87, 0\n"
"s_add_u32 s27, s86, 0x200000\n"
"buffer_load_dwordx4 v142, s[8:11], s27 offen sc1 lds\n"
"buffer_load_dwordx4 v142, s[8:11], s27 offen offset:1024 sc1 lds\n"
"buffer_load_dwordx4 v142, s[8:11], s27 offen offset:2048 sc1 lds\n"
"buffer_load_dwordx4 v142, s[8:11], s27 offen offset:3072 sc1 lds\n"
"s_add_u32 m0, s87, 0x4000\n"
"s_add_u32 s27, s86, 0x204000\n"
"buffer_load_dwordx4 v142, s[8:11], s27 offen sc1 lds\n"
"buffer_load_dwordx4 v142, s[8:11], s27 offen offset:1024 sc1 lds\n"
"buffer_load_dwordx4 v142, s[8:11], s27 offen offset:2048 sc1 lds\n"
"buffer_load_dwordx4 v142, s[8:11], s27 offen offset:3072 sc1 lds\n"
"s_waitcnt vmcnt(0) lgkmcnt(0)\n"
"v_max3_u32 v134, v0, v1, v2\n"
"v_max3_u32 v134, v134, v3, v16\n"
"v_max3_u32 v134, v134, v17, v18\n"
"v_max_u32 v134, v134, v19\n"
"v_cmp_eq_u32 vcc, -1, v134\n"
"s_cbranch_vccz Lback20_xs10x\n"
"s_add_u32 s64, s64, 1\n"
"s_cmp_lt_u32 s64, 0x4000\n"
"s_cbranch_scc1 Lslow20_xs10l\n"
"Lback20_xs10x:\n"
"s_waitcnt vmcnt(0) lgkmcnt(0)\n"
"s_mov_b32 m0, s65\n"
"s_nop 7\n"
"s_branch Lback20_xs10\n"
"Lslowx10:\n"
"s_waitcnt vmcnt(0) lgkmcnt(0)\n"
"s_nop 7\n"
"s_nop 7\n"
"s_mov_b32 s64, 0\n"
"s_mov_b32 s65, m0\n"
"Lslowx10l:\n"
"s_barrier\n"
"s_sleep 4\n"
"s_add_u32 m0, s87, 0\n"
"s_add_u32 s27, s86, 0x200000\n"
"buffer_load_dwordx4 v142, s[8:11], s27 offen sc1 lds\n"
"buffer_load_dwordx4 v142, s[8:11], s27 offen offset:1024 sc1 lds\n"
"buffer_load_dwordx4 v142, s[8:11], s27 offen offset:2048 sc1 lds\n"
"buffer_load_dwordx4 v142, s[8:11], s27 offen offset:3072 sc1 lds\n"
"s_add_u32 m0, s87, 0x4000\n"
"s_add_u32 s27, s86, 0x204000\n"
"buffer_load_dwordx4 v142, s[8:11], s27 offen sc1 lds\n"
"buffer_load_dwordx4 v142, s[8:11], s27 offen offset:1024 sc1 lds\n"
"buffer_load_dwordx4 v142, s[8:11], s27 offen offset:2048 sc1 lds\n"
"buffer_load_dwordx4 v142, s[8:11], s27 offen offset:3072 sc1 lds\n"
"s_waitcnt vmcnt(0) lgkmcnt(0)\n"
"s_barrier\n"
"v_mov_b32 v134, 0\n"
"ds_read_b128 v[68:71], v4\n"
"ds_read_b128 v[72:75], v4 offset:16384\n"
"ds_read_b128 v[76:79], v4 offset:1024\n"
"ds_read_b128 v[80:83], v4 offset:17408\n"
"s_waitcnt lgkmcnt(2)\n"
"v_mfma_f32_16x16x32_bf16 v[36:39], a[176:179], v[68:71], 0\n"
"ds_read_b128 v[84:87], v5\n"
"v_max3_u32 v134, v134, v68, v69\n"
"v_max3_u32 v134, v134, v70, v71\n"
"v_mfma_f32_16x16x32_bf16 v[44:47], a[180:183], v[68:71], 0\n"
"ds_read_b128 v[88:91], v5 offset:16384\n"
"v_mfma_f32_16x16x32_bf16 v[60:63], a[184:187], v[68:71], 0\n"
"v_max3_u32 v134, v134, v72, v73\n"
"v_max3_u32 v134, v134, v74, v75\n"
"v_mfma_f32_16x16x32_bf16 v[40:43], a[176:179], v[72:75], 0\n"
"v_mfma_f32_16x16x32_bf16 v[48:51], a[180:183], v[72:75], 0\n"
"v_mfma_f32_16x16x32_bf16 v[64:67], a[184:187], v[72:75], 0\n"
"s_waitcnt lgkmcnt(2)\n"
"v_mfma_f32_16x16x32_bf16 v[36:39], a[188:191], v[76:79], v[36:39]\n"
"ds_read_b128 v[68:71], v6\n"
"v_max3_u32 v134, v134, v76, v77\n"
"v_max3_u32 v134, v134, v78, v79\n"
"v_mfma_f32_16x16x32_bf16 v[44:47], a[192:195], v[76:79], v[44:47]\n"
"ds_read_b128 v[72:75], v6 offset:16384\n"
"v_mfma_f32_16x16x32_bf16 v[60:63], a[196:199], v[76:79], v[60:63]\n"
"v_max3_u32 v134, v134, v80, v81\n"
"v_max3_u32 v134, v134, v82, v83\n"
"v_mfma_f32_16x16x32_bf16 v[40:43], a[188:191], v[80:83], v[40:43]\n"
"v_mfma_f32_16x16x32_bf16 v[48:51], a[192:195], v[80:83], v[48:51]\n"
"v_mfma_f32_16x16x32_bf16 v[64:67], a[196:199], v[80:83], v[64:67]\n"
"s_waitcnt lgkmcnt(2)\n"
"v_mfma_f32_16x16x32_bf16 v[36:39], a[224:227], v[84:87], v[36:39]\n"
"ds_read_b128 v[76:79], v7\n"
"v_max3_u32 v134, v134, v84, v85\n"
"v_max3_u32 v134, v134, v86, v87\n"
"v_mfma_f32_16x16x32_bf16 v[44:47], a[228:231], v[84:87], v[44:47]\n"
"ds_read_b128 v[80:83], v7 offset:16384\n"
"v_mfma_f32_16x16x32_bf16 v[60:63], a[232:235], v[84:87], v[60:63]\n"
"v_max3_u32 v134, v134, v88, v89\n"
"v_max3_u32 v134, v134, v90, v91\n"
"v_mfma_f32_16x16x32_bf16 v[40:43], a[224:227], v[88:91], v[40:43]\n"
"v_mfma_f32_16x16x32_bf16 v[48:51], a[228:231], v[88:91], v[48:51]\n"
"v_mfma_f32_16x16x32_bf16 v[64:67], a[232:235], v[88:91], v[64:67]\n"
"s_waitcnt lgkmcnt(2)\n"
"v_mfma_f32_16x16x32_bf16 v[36:39], v[160:163], v[68:71], v[36:39]\n"
"ds_read_b128 v[84:87], v4 offset:2048\n"
"v_max3_u32 v134, v134, v68, v69\n"
"v_max3_u32 v134, v134, v70, v71\n"
"v_mfma_f32_16x16x32_bf16 v[44:47], v[164:167], v[68:71], v[44:47]\n"
"ds_read_b128 v[88:91], v4 offset:18432\n"
"v_mfma_f32_16x16x32_bf16 v[60:63], v[168:171], v[68:71], v[60:63]\n"
"v_max3_u32 v134, v134, v72, v73\n"
"v_max3_u32 v134, v134, v74, v75\n"
"v_mfma_f32_16x16x32_bf16 v[40:43], v[160:163], v[72:75], v[40:43]\n"
"v_mfma_f32_16x16x32_bf16 v[48:51], v[164:167], v[72:75], v[48:51]\n"
"v_mfma_f32_16x16x32_bf16 v[64:67], v[168:171], v[72:75], v[64:67]\n"
"s_waitcnt lgkmcnt(2)\n"
"v_mfma_f32_16x16x32_bf16 v[36:39], v[208:211], v[76:79], v[36:39]\n"
"ds_read_b128 v[68:71], v5 offset:1024\n"
"v_max3_u32 v134, v134, v76, v77\n"
"v_max3_u32 v134, v134, v78, v79\n"
"v_mfma_f32_16x16x32_bf16 v[44:47], v[212:215], v[76:79], v[44:47]\n"
"ds_read_b128 v[72:75], v5 offset:17408\n"
"v_mfma_f32_16x16x32_bf16 v[60:63], v[216:219], v[76:79], v[60:63]\n"
"v_max3_u32 v134, v134, v80, v81\n"
"v_max3_u32 v134, v134, v82, v83\n"
"v_mfma_f32_16x16x32_bf16 v[40:43], v[208:211], v[80:83], v[40:43]\n"
"v_mfma_f32_16x16x32_bf16 v[48:51], v[212:215], v[80:83], v[48:51]\n"
"v_mfma_f32_16x16x32_bf16 v[64:67], v[216:219], v[80:83], v[64:67]\n"
"s_waitcnt lgkmcnt(2)\n"
"v_mfma_f32_16x16x32_bf16 v[36:39], a[200:203], v[84:87], v[36:39]\n"
"ds_read_b128 v[76:79], v6 offset:1024\n"
"v_max3_u32 v134, v134, v84, v85\n"
"v_max3_u32 v134, v134, v86, v87\n"
"v_mfma_f32_16x16x32_bf16 v[44:47], a[204:207], v[84:87], v[44:47]\n"
"ds_read_b128 v[80:83], v6 offset:17408\n"
"v_mfma_f32_16x16x32_bf16 v[60:63], a[208:211], v[84:87], v[60:63]\n"
"v_max3_u32 v134, v134, v88, v89\n"
"v_max3_u32 v134, v134, v90, v91\n"
"v_mfma_f32_16x16x32_bf16 v[40:43], a[200:203], v[88:91], v[40:43]\n"
"v_mfma_f32_16x16x32_bf16 v[48:51], a[204:207], v[88:91], v[48:51]\n"
"v_mfma_f32_16x16x32_bf16 v[64:67], a[208:211], v[88:91], v[64:67]\n"
"s_waitcnt lgkmcnt(2)\n"
"v_mfma_f32_16x16x32_bf16 v[36:39], a[236:239], v[68:71], v[36:39]\n"
"ds_read_b128 v[84:87], v7 offset:1024\n"
"v_max3_u32 v134, v134, v68, v69\n"
"v_max3_u32 v134, v134, v70, v71\n"
"v_mfma_f32_16x16x32_bf16 v[44:47], a[240:243], v[68:71], v[44:47]\n"
"ds_read_b128 v[88:91], v7 offset:17408\n"
"v_mfma_f32_16x16x32_bf16 v[60:63], a[244:247], v[68:71], v[60:63]\n"
"v_max3_u32 v134, v134, v72, v73\n"
"v_max3_u32 v134, v134, v74, v75\n"
"v_mfma_f32_16x16x32_bf16 v[40:43], a[236:239], v[72:75], v[40:43]\n"
"v_mfma_f32_16x16x32_bf16 v[48:51], a[240:243], v[72:75], v[48:51]\n"
"v_mfma_f32_16x16x32_bf16 v[64:67], a[244:247], v[72:75], v[64:67]\n"
"s_waitcnt lgkmcnt(2)\n"
"v_mfma_f32_16x16x32_bf16 v[36:39], v[172:175], v[76:79], v[36:39]\n"
"ds_read_b128 v[68:71], v4 offset:3072\n"
"v_max3_u32 v134, v134, v76, v77\n"
"v_max3_u32 v134, v134, v78, v79\n"
"v_mfma_f32_16x16x32_bf16 v[44:47], v[176:179], v[76:79], v[44:47]\n"
"ds_read_b128 v[72:75], v4 offset:19456\n"
"v_mfma_f32_16x16x32_bf16 v[60:63], v[180:183], v[76:79], v[60:63]\n"
"v_max3_u32 v134, v134, v80, v81\n"
"v_max3_u32 v134, v134, v82, v83\n"
"v_mfma_f32_16x16x32_bf16 v[40:43], v[172:175], v[80:83], v[40:43]\n"
"v_mfma_f32_16x16x32_bf16 v[48:51], v[176:179], v[80:83], v[48:51]\n"
"v_mfma_f32_16x16x32_bf16 v[64:67], v[180:183], v[80:83], v[64:67]\n"
"s_waitcnt lgkmcnt(2)\n"
"v_mfma_f32_16x16x32_bf16 v[36:39], v[220:223], v[84:87], v[36:39]\n"
"ds_read_b128 v[76:79], v5 offset:2048\n"
"v_max3_u32 v134, v134, v84, v85\n"
"v_max3_u32 v134, v134, v86, v87\n"
"v_mfma_f32_16x16x32_bf16 v[44:47], v[224:227], v[84:87], v[44:47]\n"
"ds_read_b128 v[80:83], v5 offset:18432\n"
"v_mfma_f32_16x16x32_bf16 v[60:63], v[228:231], v[84:87], v[60:63]\n"
"v_max3_u32 v134, v134, v88, v89\n"
"v_max3_u32 v134, v134, v90, v91\n"
"v_mfma_f32_16x16x32_bf16 v[40:43], v[220:223], v[88:91], v[40:43]\n"
"v_mfma_f32_16x16x32_bf16 v[48:51], v[224:227], v[88:91], v[48:51]\n"
"v_mfma_f32_16x16x32_bf16 v[64:67], v[228:231], v[88:91], v[64:67]\n"
"s_waitcnt lgkmcnt(2)\n"
"v_mfma_f32_16x16x32_bf16 v[36:39], a[212:215], v[68:71], v[36:39]\n"
"ds_read_b128 v[84:87], v6 offset:2048\n"
"v_max3_u32 v134, v134, v68, v69\n"
"v_max3_u32 v134, v134, v70, v71\n"
"v_mfma_f32_16x16x32_bf16 v[44:47], a[216:219], v[68:71], v[44:47]\n"
"ds_read_b128 v[88:91], v6 offset:18432\n"
"v_mfma_f32_16x16x32_bf16 v[60:63], a[220:223], v[68:71], v[60:63]\n"
"v_max3_u32 v134, v134, v72, v73\n"
"v_max3_u32 v134, v134, v74, v75\n"
"v_mfma_f32_16x16x32_bf16 v[40:43], a[212:215], v[72:75], v[40:43]\n"
"v_mfma_f32_16x16x32_bf16 v[48:51], a[216:219], v[72:75], v[48:51]\n"
"v_mfma_f32_16x16x32_bf16 v[64:67], a[220:223], v[72:75], v[64:67]\n"
"s_waitcnt lgkmcnt(2)\n"
"v_mfma_f32_16x16x32_bf16 v[36:39], a[248:251], v[76:79], v[36:39]\n"
"ds_read_b128 v[68:71], v7 offset:2048\n"
"v_max3_u32 v134, v134, v76, v77\n"
"v_max3_u32 v134, v134, v78, v79\n"
"v_mfma_f32_16x16x32_bf16 v[44:47], a[252:255], v[76:79], v[44:47]\n"
"ds_read_b128 v[72:75], v7 offset:18432\n"
"v_mfma_f32_16x16x32_bf16 v[60:63], v[144:147], v[76:79], v[60:63]\n"
"v_max3_u32 v134, v134, v80, v81\n"
"v_max3_u32 v134, v134, v82, v83\n"
"v_mfma_f32_16x16x32_bf16 v[40:43], a[248:251], v[80:83], v[40:43]\n"
"v_mfma_f32_16x16x32_bf16 v[48:51], a[252:255], v[80:83], v[48:51]\n"
"v_mfma_f32_16x16x32_bf16 v[64:67], v[144:147], v[80:83], v[64:67]\n"
"s_waitcnt lgkmcnt(2)\n"
"v_mfma_f32_16x16x32_bf16 v[36:39], v[184:187], v[84:87], v[36:39]\n"
"ds_read_b128 v[76:79], v5 offset:3072\n"
"v_max3_u32 v134, v134, v84, v85\n"
"v_max3_u32 v134, v134, v86, v87\n"
"v_mfma_f32_16x16x32_bf16 v[44:47], v[188:191], v[84:87], v[44:47]\n"
"ds_read_b128 v[80:83], v5 offset:19456\n"
"v_mfma_f32_16x16x32_bf16 v[60:63], v[192:195], v[84:87], v[60:63]\n"
"v_max3_u32 v134, v134, v88, v89\n"
"v_max3_u32 v134, v134, v90, v91\n"
"v_mfma_f32_16x16x32_bf16 v[40:43], v[184:187], v[88:91], v[40:43]\n"
"v_mfma_f32_16x16x32_bf16 v[48:51], v[188:191], v[88:91], v[48:51]\n"
"v_mfma_f32_16x16x32_bf16 v[64:67], v[192:195], v[88:91], v[64:67]\n"
"s_waitcnt lgkmcnt(2)\n"
"v_mfma_f32_16x16x32_bf16 v[36:39], v[232:235], v[68:71], v[36:39]\n"
"ds_read_b128 v[84:87], v6 offset:3072\n"
"v_max3_u32 v134, v134, v68, v69\n"
"v_max3_u32 v134, v134, v70, v71\n"
"v_mfma_f32_16x16x32_bf16 v[44:47], v[236:239], v[68:71], v[44:47]\n"
"ds_read_b128 v[88:91], v6 offset:19456\n"
"v_mfma_f32_16x16x32_bf16 v[60:63], v[240:243], v[68:71], v[60:63]\n"
"v_max3_u32 v134, v134, v72, v73\n"
"v_max3_u32 v134, v134, v74, v75\n"
"v_mfma_f32_16x16x32_bf16 v[40:43], v[232:235], v[72:75], v[40:43]\n"
"v_mfma_f32_16x16x32_bf16 v[48:51], v[236:239], v[72:75], v[48:51]\n"
"v_mfma_f32_16x16x32_bf16 v[64:67], v[240:243], v[72:75], v[64:67]\n"
"s_waitcnt lgkmcnt(2)\n"
"v_mfma_f32_16x16x32_bf16 v[36:39], v[148:151], v[76:79], v[36:39]\n"
"ds_read_b128 v[68:71], v7 offset:3072\n"
"v_max3_u32 v134, v134, v76, v77\n"
"v_max3_u32 v134, v134, v78, v79\n"
"v_mfma_f32_16x16x32_bf16 v[44:47], v[152:155], v[76:79], v[44:47]\n"
"ds_read_b128 v[72:75], v7 offset:19456\n"
"v_mfma_f32_16x16x32_bf16 v[60:63], v[156:159], v[76:79], v[60:63]\n"
"v_max3_u32 v134, v134, v80, v81\n"
"v_max3_u32 v134, v134, v82, v83\n"
"v_mfma_f32_16x16x32_bf16 v[40:43], v[148:151], v[80:83], v[40:43]\n"
"v_mfma_f32_16x16x32_bf16 v[48:51], v[152:155], v[80:83], v[48:51]\n"
"v_mfma_f32_16x16x32_bf16 v[64:67], v[156:159], v[80:83], v[64:67]\n"
"s_waitcnt lgkmcnt(2)\n"
"v_mfma_f32_16x16x32_bf16 v[36:39], v[196:199], v[84:87], v[36:39]\n"
"v_max3_u32 v134, v134, v84, v85\n"
"v_max3_u32 v134, v134, v86, v87\n"
"v_mfma_f32_16x16x32_bf16 v[44:47], v[200:203], v[84:87], v[44:47]\n"
"v_mfma_f32_16x16x32_bf16 v[60:63], v[204:207], v[84:87], v[60:63]\n"
"v_max3_u32 v134, v134, v88, v89\n"
"v_max3_u32 v134, v134, v90, v91\n"
"v_mfma_f32_16x16x32_bf16 v[40:43], v[196:199], v[88:91], v[40:43]\n"
"v_mfma_f32_16x16x32_bf16 v[48:51], v[200:203], v[88:91], v[48:51]\n"
"v_mfma_f32_16x16x32_bf16 v[64:67], v[204:207], v[88:91], v[64:67]\n"
"s_waitcnt lgkmcnt(0)\n"
"v_mfma_f32_16x16x32_bf16 v[36:39], v[244:247], v[68:71], v[36:39]\n"
"v_max3_u32 v134, v134, v68, v69\n"
"v_max3_u32 v134, v134, v70, v71\n"
"v_mfma_f32_16x16x32_bf16 v[44:47], v[248:251], v[68:71], v[44:47]\n"
"v_mfma_f32_16x16x32_bf16 v[60:63], v[252:255], v[68:71], v[60:63]\n"
"v_max3_u32 v134, v134, v72, v73\n"
"v_max3_u32 v134, v134, v74, v75\n"
"v_mfma_f32_16x16x32_bf16 v[40:43], v[244:247], v[72:75], v[40:43]\n"
"v_mfma_f32_16x16x32_bf16 v[48:51], v[248:251], v[72:75], v[48:51]\n"
"v_mfma_f32_16x16x32_bf16 v[64:67], v[252:255], v[72:75], v[64:67]\n"
"v_cmp_eq_u32 vcc, -1, v134\n"
"s_cbranch_vccz Lbackx10x\n"
"s_add_u32 s64, s64, 1\n"
"s_cmp_lt_u32 s64, 0x4000\n"
"s_cbranch_scc1 Lslowx10l\n"
"Lbackx10x:\n"
"ds_read_b128 v[76:79], v10\n"
"ds_read_b128 v[80:83], v10 offset:64\n"
"ds_read_b128 v[84:87], v10 offset:128\n"
"s_waitcnt vmcnt(0) lgkmcnt(0)\n"
"s_mov_b32 m0, s65\n"
"s_nop 7\n"
"s_nop 7\n"
"s_branch Lbackx10\n"
"Lslow21_xs11:\n"
"s_waitcnt vmcnt(0) lgkmcnt(0)\n"
"s_nop 7\n"
"s_nop 7\n"
"s_mov_b32 s64, 0\n"
"s_mov_b32 s65, m0\n"
"Lslow21_xs11l:\n"
"s_sleep 1\n"
"s_add_u32 s59, s93, 0x60800\n"
"buffer_load_dwordx2 v[28:29], v11, s[8:11], s59 offen sc1\n"
"s_add_u32 s59, s93, 0x60800\n"
"buffer_load_dwordx2 v[30:31], v11, s[8:11], s59 offen offset:128 sc1\n"
"s_add_u32 s59, s93, 0x60c00\n"
"buffer_load_dwordx2 v[32:33], v11, s[8:11], s59 offen sc1\n"
"s_add_u32 s59, s93, 0x60c00\n"
"buffer_load_dwordx2 v[34:35], v11, s[8:11], s59 offen offset:128 sc1\n"
"s_add_u32 m0, s87, 0x8000\n"
"s_add_u32 s27, s86, 0x208000\n"
"buffer_load_dwordx4 v142, s[8:11], s27 offen sc1 lds\n"
"buffer_load_dwordx4 v142, s[8:11], s27 offen offset:1024 sc1 lds\n"
"buffer_load_dwordx4 v142, s[8:11], s27 offen offset:2048 sc1 lds\n"
"buffer_load_dwordx4 v142, s[8:11], s27 offen offset:3072 sc1 lds\n"
"s_add_u32 m0, s87, 0xc000\n"
"s_add_u32 s27, s86, 0x20c000\n"
"buffer_load_dwordx4 v142, s[8:11], s27 offen sc1 lds\n"
"buffer_load_dwordx4 v142, s[8:11], s27 offen offset:1024 sc1 lds\n"
"buffer_load_dwordx4 v142, s[8:11], s27 offen offset:2048 sc1 lds\n"
"buffer_load_dwordx4 v142, s[8:11], s27 offen offset:3072 sc1 lds\n"
"s_waitcnt vmcnt(0) lgkmcnt(0)\n"
"v_max3_u32 v134, v28, v29, v30\n"
"v_max3_u32 v134, v134, v31, v32\n"
"v_max3_u32 v134, v134, v33, v34\n"
"v_max_u32 v134, v134, v35\n"
"v_cmp_eq_u32 vcc, -1, v134\n"
"s_cbranch_vccz Lback21_xs11x\n"
"s_add_u32 s64, s64, 1\n"
"s_cmp_lt_u32 s64, 0x4000\n"
"s_cbranch_scc1 Lslow21_xs11l\n"
"Lback21_xs11x:\n"
"s_waitcnt vmcnt(0) lgkmcnt(0)\n"
"s_mov_b32 m0, s65\n"
"s_nop 7\n"
"s_branch Lback21_xs11\n"
"Lslowx11:\n"
"s_waitcnt vmcnt(0) lgkmcnt(0)\n"
"s_nop 7\n"
"s_nop 7\n"
"s_mov_b32 s64, 0\n"
"s_mov_b32 s65, m0\n"
"Lslowx11l:\n"
"s_barrier\n"
"s_sleep 4\n"
"s_add_u32 m0, s87, 0x8000\n"
"s_add_u32 s27, s86, 0x208000\n"
"buffer_load_dwordx4 v142, s[8:11], s27 offen sc1 lds\n"
"buffer_load_dwordx4 v142, s[8:11], s27 offen offset:1024 sc1 lds\n"
"buffer_load_dwordx4 v142, s[8:11], s27 offen offset:2048 sc1 lds\n"
"buffer_load_dwordx4 v142, s[8:11], s27 offen offset:3072 sc1 lds\n"
"s_add_u32 m0, s87, 0xc000\n"
"s_add_u32 s27, s86, 0x20c000\n"
"buffer_load_dwordx4 v142, s[8:11], s27 offen sc1 lds\n"
"buffer_load_dwordx4 v142, s[8:11], s27 offen offset:1024 sc1 lds\n"
"buffer_load_dwordx4 v142, s[8:11], s27 offen offset:2048 sc1 lds\n"
"buffer_load_dwordx4 v142, s[8:11], s27 offen offset:3072 sc1 lds\n"
"s_waitcnt vmcnt(0) lgkmcnt(0)\n"
"s_barrier\n"
"v_mov_b32 v134, 0\n"
"ds_read_b128 v[68:71], v4 offset:32768\n"
"ds_read_b128 v[72:75], v4 offset:49152\n"
"ds_read_b128 v[76:79], v4 offset:33792\n"
"ds_read_b128 v[80:83], v4 offset:50176\n"
"s_waitcnt lgkmcnt(2)\n"
"v_mfma_f32_16x16x32_bf16 v[36:39], a[176:179], v[68:71], 0\n"
"ds_read_b128 v[84:87], v5 offset:32768\n"
"v_max3_u32 v134, v134, v68, v69\n"
"v_max3_u32 v134, v134, v70, v71\n"
"v_mfma_f32_16x16x32_bf16 v[44:47], a[180:183], v[68:71], 0\n"
"ds_read_b128 v[88:91], v5 offset:49152\n"
"v_mfma_f32_16x16x32_bf16 v[60:63], a[184:187], v[68:71], 0\n"
"v_max3_u32 v134, v134, v72, v73\n"
"v_max3_u32 v134, v134, v74, v75\n"
"v_mfma_f32_16x16x32_bf16 v[40:43], a[176:179], v[72:75], 0\n"
"v_mfma_f32_16x16x32_bf16 v[48:51], a[180:183], v[72:75], 0\n"
"v_mfma_f32_16x16x32_bf16 v[64:67], a[184:187], v[72:75], 0\n"
"s_waitcnt lgkmcnt(2)\n"
"v_mfma_f32_16x16x32_bf16 v[36:39], a[188:191], v[76:79], v[36:39]\n"
"ds_read_b128 v[68:71], v6 offset:32768\n"
"v_max3_u32 v134, v134, v76, v77\n"
"v_max3_u32 v134, v134, v78, v79\n"
"v_mfma_f32_16x16x32_bf16 v[44:47], a[192:195], v[76:79], v[44:47]\n"
"ds_read_b128 v[72:75], v6 offset:49152\n"
"v_mfma_f32_16x16x32_bf16 v[60:63], a[196:199], v[76:79], v[60:63]\n"
"v_max3_u32 v134, v134, v80, v81\n"
"v_max3_u32 v134, v134, v82, v83\n"
"v_mfma_f32_16x16x32_bf16 v[40:43], a[188:191], v[80:83], v[40:43]\n"
"v_mfma_f32_16x16x32_bf16 v[48:51], a[192:195], v[80:83], v[48:51]\n"
"v_mfma_f32_16x16x32_bf16 v[64:67], a[196:199], v[80:83], v[64:67]\n"
"s_waitcnt lgkmcnt(2)\n"
"v_mfma_f32_16x16x32_bf16 v[36:39], a[224:227], v[84:87], v[36:39]\n"
"ds_read_b128 v[76:79], v7 offset:32768\n"
"v_max3_u32 v134, v134, v84, v85\n"
"v_max3_u32 v134, v134, v86, v87\n"
"v_mfma_f32_16x16x32_bf16 v[44:47], a[228:231], v[84:87], v[44:47]\n"
"ds_read_b128 v[80:83], v7 offset:49152\n"
"v_mfma_f32_16x16x32_bf16 v[60:63], a[232:235], v[84:87], v[60:63]\n"
"v_max3_u32 v134, v134, v88, v89\n"
"v_max3_u32 v134, v134, v90, v91\n"
"v_mfma_f32_16x16x32_bf16 v[40:43], a[224:227], v[88:91], v[40:43]\n"
"v_mfma_f32_16x16x32_bf16 v[48:51], a[228:231], v[88:91], v[48:51]\n"
"v_mfma_f32_16x16x32_bf16 v[64:67], a[232:235], v[88:91], v[64:67]\n"
"s_waitcnt lgkmcnt(2)\n"
"v_mfma_f32_16x16x32_bf16 v[36:39], v[160:163], v[68:71], v[36:39]\n"
"ds_read_b128 v[84:87], v4 offset:34816\n"
"v_max3_u32 v134, v134, v68, v69\n"
"v_max3_u32 v134, v134, v70, v71\n"
"v_mfma_f32_16x16x32_bf16 v[44:47], v[164:167], v[68:71], v[44:47]\n"
"ds_read_b128 v[88:91], v4 offset:51200\n"
"v_mfma_f32_16x16x32_bf16 v[60:63], v[168:171], v[68:71], v[60:63]\n"
"v_max3_u32 v134, v134, v72, v73\n"
"v_max3_u32 v134, v134, v74, v75\n"
"v_mfma_f32_16x16x32_bf16 v[40:43], v[160:163], v[72:75], v[40:43]\n"
"v_mfma_f32_16x16x32_bf16 v[48:51], v[164:167], v[72:75], v[48:51]\n"
"v_mfma_f32_16x16x32_bf16 v[64:67], v[168:171], v[72:75], v[64:67]\n"
"s_waitcnt lgkmcnt(2)\n"
"v_mfma_f32_16x16x32_bf16 v[36:39], v[208:211], v[76:79], v[36:39]\n"
"ds_read_b128 v[68:71], v5 offset:33792\n"
"v_max3_u32 v134, v134, v76, v77\n"
"v_max3_u32 v134, v134, v78, v79\n"
"v_mfma_f32_16x16x32_bf16 v[44:47], v[212:215], v[76:79], v[44:47]\n"
"ds_read_b128 v[72:75], v5 offset:50176\n"
"v_mfma_f32_16x16x32_bf16 v[60:63], v[216:219], v[76:79], v[60:63]\n"
"v_max3_u32 v134, v134, v80, v81\n"
"v_max3_u32 v134, v134, v82, v83\n"
"v_mfma_f32_16x16x32_bf16 v[40:43], v[208:211], v[80:83], v[40:43]\n"
"v_mfma_f32_16x16x32_bf16 v[48:51], v[212:215], v[80:83], v[48:51]\n"
"v_mfma_f32_16x16x32_bf16 v[64:67], v[216:219], v[80:83], v[64:67]\n"
"s_waitcnt lgkmcnt(2)\n"
"v_mfma_f32_16x16x32_bf16 v[36:39], a[200:203], v[84:87], v[36:39]\n"
"ds_read_b128 v[76:79], v6 offset:33792\n"
"v_max3_u32 v134, v134, v84, v85\n"
"v_max3_u32 v134, v134, v86, v87\n"
"v_mfma_f32_16x16x32_bf16 v[44:47], a[204:207], v[84:87], v[44:47]\n"
"ds_read_b128 v[80:83], v6 offset:50176\n"
"v_mfma_f32_16x16x32_bf16 v[60:63], a[208:211], v[84:87], v[60:63]\n"
"v_max3_u32 v134, v134, v88, v89\n"
"v_max3_u32 v134, v134, v90, v91\n"
"v_mfma_f32_16x16x32_bf16 v[40:43], a[200:203], v[88:91], v[40:43]\n"
"v_mfma_f32_16x16x32_bf16 v[48:51], a[204:207], v[88:91], v[48:51]\n"
"v_mfma_f32_16x16x32_bf16 v[64:67], a[208:211], v[88:91], v[64:67]\n"
"s_waitcnt lgkmcnt(2)\n"
"v_mfma_f32_16x16x32_bf16 v[36:39], a[236:239], v[68:71], v[36:39]\n"
"ds_read_b128 v[84:87], v7 offset:33792\n"
"v_max3_u32 v134, v134, v68, v69\n"
"v_max3_u32 v134, v134, v70, v71\n"
"v_mfma_f32_16x16x32_bf16 v[44:47], a[240:243], v[68:71], v[44:47]\n"
"ds_read_b128 v[88:91], v7 offset:50176\n"
"v_mfma_f32_16x16x32_bf16 v[60:63], a[244:247], v[68:71], v[60:63]\n"
"v_max3_u32 v134, v134, v72, v73\n"
"v_max3_u32 v134, v134, v74, v75\n"
"v_mfma_f32_16x16x32_bf16 v[40:43], a[236:239], v[72:75], v[40:43]\n"
"v_mfma_f32_16x16x32_bf16 v[48:51], a[240:243], v[72:75], v[48:51]\n"
"v_mfma_f32_16x16x32_bf16 v[64:67], a[244:247], v[72:75], v[64:67]\n"
"s_waitcnt lgkmcnt(2)\n"
"v_mfma_f32_16x16x32_bf16 v[36:39], v[172:175], v[76:79], v[36:39]\n"
"ds_read_b128 v[68:71], v4 offset:35840\n"
"v_max3_u32 v134, v134, v76, v77\n"
"v_max3_u32 v134, v134, v78, v79\n"
"v_mfma_f32_16x16x32_bf16 v[44:47], v[176:179], v[76:79], v[44:47]\n"
"ds_read_b128 v[72:75], v4 offset:52224\n"
"v_mfma_f32_16x16x32_bf16 v[60:63], v[180:183], v[76:79], v[60:63]\n"
"v_max3_u32 v134, v134, v80, v81\n"
"v_max3_u32 v134, v134, v82, v83\n"
"v_mfma_f32_16x16x32_bf16 v[40:43], v[172:175], v[80:83], v[40:43]\n"
"v_mfma_f32_16x16x32_bf16 v[48:51], v[176:179], v[80:83], v[48:51]\n"
"v_mfma_f32_16x16x32_bf16 v[64:67], v[180:183], v[80:83], v[64:67]\n"
"s_waitcnt lgkmcnt(2)\n"
"v_mfma_f32_16x16x32_bf16 v[36:39], v[220:223], v[84:87], v[36:39]\n"
"ds_read_b128 v[76:79], v5 offset:34816\n"
"v_max3_u32 v134, v134, v84, v85\n"
"v_max3_u32 v134, v134, v86, v87\n"
"v_mfma_f32_16x16x32_bf16 v[44:47], v[224:227], v[84:87], v[44:47]\n"
"ds_read_b128 v[80:83], v5 offset:51200\n"
"v_mfma_f32_16x16x32_bf16 v[60:63], v[228:231], v[84:87], v[60:63]\n"
"v_max3_u32 v134, v134, v88, v89\n"
"v_max3_u32 v134, v134, v90, v91\n"
"v_mfma_f32_16x16x32_bf16 v[40:43], v[220:223], v[88:91], v[40:43]\n"
"v_mfma_f32_16x16x32_bf16 v[48:51], v[224:227], v[88:91], v[48:51]\n"
"v_mfma_f32_16x16x32_bf16 v[64:67], v[228:231], v[88:91], v[64:67]\n"
"s_waitcnt lgkmcnt(2)\n"
"v_mfma_f32_16x16x32_bf16 v[36:39], a[212:215], v[68:71], v[36:39]\n"
"ds_read_b128 v[84:87], v6 offset:34816\n"
"v_max3_u32 v134, v134, v68, v69\n"
"v_max3_u32 v134, v134, v70, v71\n"
"v_mfma_f32_16x16x32_bf16 v[44:47], a[216:219], v[68:71], v[44:47]\n"
"ds_read_b128 v[88:91], v6 offset:51200\n"
"v_mfma_f32_16x16x32_bf16 v[60:63], a[220:223], v[68:71], v[60:63]\n"
"v_max3_u32 v134, v134, v72, v73\n"
"v_max3_u32 v134, v134, v74, v75\n"
"v_mfma_f32_16x16x32_bf16 v[40:43], a[212:215], v[72:75], v[40:43]\n"
"v_mfma_f32_16x16x32_bf16 v[48:51], a[216:219], v[72:75], v[48:51]\n"
"v_mfma_f32_16x16x32_bf16 v[64:67], a[220:223], v[72:75], v[64:67]\n"
"s_waitcnt lgkmcnt(2)\n"
"v_mfma_f32_16x16x32_bf16 v[36:39], a[248:251], v[76:79], v[36:39]\n"
"ds_read_b128 v[68:71], v7 offset:34816\n"
"v_max3_u32 v134, v134, v76, v77\n"
"v_max3_u32 v134, v134, v78, v79\n"
"v_mfma_f32_16x16x32_bf16 v[44:47], a[252:255], v[76:79], v[44:47]\n"
"ds_read_b128 v[72:75], v7 offset:51200\n"
"v_mfma_f32_16x16x32_bf16 v[60:63], v[144:147], v[76:79], v[60:63]\n"
"v_max3_u32 v134, v134, v80, v81\n"
"v_max3_u32 v134, v134, v82, v83\n"
"v_mfma_f32_16x16x32_bf16 v[40:43], a[248:251], v[80:83], v[40:43]\n"
"v_mfma_f32_16x16x32_bf16 v[48:51], a[252:255], v[80:83], v[48:51]\n"
"v_mfma_f32_16x16x32_bf16 v[64:67], v[144:147], v[80:83], v[64:67]\n"
"s_waitcnt lgkmcnt(2)\n"
"v_mfma_f32_16x16x32_bf16 v[36:39], v[184:187], v[84:87], v[36:39]\n"
"ds_read_b128 v[76:79], v5 offset:35840\n"
"v_max3_u32 v134, v134, v84, v85\n"
"v_max3_u32 v134, v134, v86, v87\n"
"v_mfma_f32_16x16x32_bf16 v[44:47], v[188:191], v[84:87], v[44:47]\n"
"ds_read_b128 v[80:83], v5 offset:52224\n"
"v_mfma_f32_16x16x32_bf16 v[60:63], v[192:195], v[84:87], v[60:63]\n"
"v_max3_u32 v134, v134, v88, v89\n"
"v_max3_u32 v134, v134, v90, v91\n"
"v_mfma_f32_16x16x32_bf16 v[40:43], v[184:187], v[88:91], v[40:43]\n"
"v_mfma_f32_16x16x32_bf16 v[48:51], v[188:191], v[88:91], v[48:51]\n"
"v_mfma_f32_16x16x32_bf16 v[64:67], v[192:195], v[88:91], v[64:67]\n"
"s_waitcnt lgkmcnt(2)\n"
"v_mfma_f32_16x16x32_bf16 v[36:39], v[232:235], v[68:71], v[36:39]\n"
"ds_read_b128 v[84:87], v6 offset:35840\n"
"v_max3_u32 v134, v134, v68, v69\n"
"v_max3_u32 v134, v134, v70, v71\n"
"v_mfma_f32_16x16x32_bf16 v[44:47], v[236:239], v[68:71], v[44:47]\n"
"ds_read_b128 v[88:91], v6 offset:52224\n"
"v_mfma_f32_16x16x32_bf16 v[60:63], v[240:243], v[68:71], v[60:63]\n"
"v_max3_u32 v134, v134, v72, v73\n"
"v_max3_u32 v134, v134, v74, v75\n"
"v_mfma_f32_16x16x32_bf16 v[40:43], v[232:235], v[72:75], v[40:43]\n"
"v_mfma_f32_16x16x32_bf16 v[48:51], v[236:239], v[72:75], v[48:51]\n"
"v_mfma_f32_16x16x32_bf16 v[64:67], v[240:243], v[72:75], v[64:67]\n"
"s_waitcnt lgkmcnt(2)\n"
"v_mfma_f32_16x16x32_bf16 v[36:39], v[148:151], v[76:79], v[36:39]\n"
"ds_read_b128 v[68:71], v7 offset:35840\n"
"v_max3_u32 v134, v134, v76, v77\n"
"v_max3_u32 v134, v134, v78, v79\n"
"v_mfma_f32_16x16x32_bf16 v[44:47], v[152:155], v[76:79], v[44:47]\n"
"ds_read_b128 v[72:75], v7 offset:52224\n"
"v_mfma_f32_16x16x32_bf16 v[60:63], v[156:159], v[76:79], v[60:63]\n"
"v_max3_u32 v134, v134, v80, v81\n"
"v_max3_u32 v134, v134, v82, v83\n"
"v_mfma_f32_16x16x32_bf16 v[40:43], v[148:151], v[80:83], v[40:43]\n"
"v_mfma_f32_16x16x32_bf16 v[48:51], v[152:155], v[80:83], v[48:51]\n"
"v_mfma_f32_16x16x32_bf16 v[64:67], v[156:159], v[80:83], v[64:67]\n"
"s_waitcnt lgkmcnt(2)\n"
"v_mfma_f32_16x16x32_bf16 v[36:39], v[196:199], v[84:87], v[36:39]\n"
"v_max3_u32 v134, v134, v84, v85\n"
"v_max3_u32 v134, v134, v86, v87\n"
"v_mfma_f32_16x16x32_bf16 v[44:47], v[200:203], v[84:87], v[44:47]\n"
"v_mfma_f32_16x16x32_bf16 v[60:63], v[204:207], v[84:87], v[60:63]\n"
"v_max3_u32 v134, v134, v88, v89\n"
"v_max3_u32 v134, v134, v90, v91\n"
"v_mfma_f32_16x16x32_bf16 v[40:43], v[196:199], v[88:91], v[40:43]\n"
"v_mfma_f32_16x16x32_bf16 v[48:51], v[200:203], v[88:91], v[48:51]\n"
"v_mfma_f32_16x16x32_bf16 v[64:67], v[204:207], v[88:91], v[64:67]\n"
"s_waitcnt lgkmcnt(0)\n"
"v_mfma_f32_16x16x32_bf16 v[36:39], v[244:247], v[68:71], v[36:39]\n"
"v_max3_u32 v134, v134, v68, v69\n"
"v_max3_u32 v134, v134, v70, v71\n"
"v_mfma_f32_16x16x32_bf16 v[44:47], v[248:251], v[68:71], v[44:47]\n"
"v_mfma_f32_16x16x32_bf16 v[60:63], v[252:255], v[68:71], v[60:63]\n"
"v_max3_u32 v134, v134, v72, v73\n"
"v_max3_u32 v134, v134, v74, v75\n"
"v_mfma_f32_16x16x32_bf16 v[40:43], v[244:247], v[72:75], v[40:43]\n"
"v_mfma_f32_16x16x32_bf16 v[48:51], v[248:251], v[72:75], v[48:51]\n"
"v_mfma_f32_16x16x32_bf16 v[64:67], v[252:255], v[72:75], v[64:67]\n"
"v_cmp_eq_u32 vcc, -1, v134\n"
"s_cbranch_vccz Lbackx11x\n"
"s_add_u32 s64, s64, 1\n"
"s_cmp_lt_u32 s64, 0x4000\n"
"s_cbranch_scc1 Lslowx11l\n"
"Lbackx11x:\n"
"ds_read_b128 v[76:79], v10\n"
"ds_read_b128 v[80:83], v10 offset:64\n"
"ds_read_b128 v[84:87], v10 offset:128\n"
"s_waitcnt vmcnt(0) lgkmcnt(0)\n"
"s_mov_b32 m0, s65\n"
"s_nop 7\n"
"s_nop 7\n"
"s_branch Lbackx11\n"
"Lslow22_h10:\n"
"s_waitcnt vmcnt(0) lgkmcnt(0)\n"
"s_nop 7\n"
"s_nop 7\n"
"s_mov_b32 s64, 0\n"
"s_mov_b32 s65, m0\n"
"Lslow22_h10l:\n"
"s_sleep 1\n"
"buffer_load_dwordx4 v[0:3], v142, s[8:11], s85 offen sc1\n"
"buffer_load_dwordx4 v[16:19], v142, s[8:11], s85 offen offset:1024 sc1\n"
"buffer_load_dwordx4 v[92:95], v142, s[8:11], s85 offen offset:2048 sc1\n"
"buffer_load_dwordx4 v[96:99], v142, s[8:11], s85 offen offset:3072 sc1\n"
"buffer_load_dwordx4 v[100:103], v142, s[8:11], s88 offen sc1\n"
"buffer_load_dwordx4 v[104:107], v142, s[8:11], s88 offen offset:1024 sc1\n"
"buffer_load_dwordx4 v[108:111], v142, s[8:11], s88 offen offset:2048 sc1\n"
"buffer_load_dwordx4 v[112:115], v142, s[8:11], s88 offen offset:3072 sc1\n"
"buffer_load_dwordx4 v[116:119], v142, s[8:11], s89 offen sc1\n"
"buffer_load_dwordx4 v[120:123], v142, s[8:11], s89 offen offset:1024 sc1\n"
"buffer_load_dwordx4 v[20:23], v142, s[8:11], s89 offen offset:2048 sc1\n"
"buffer_load_dwordx4 v[24:27], v142, s[8:11], s89 offen offset:3072 sc1\n"
"buffer_load_dwordx4 v[56:59], v142, s[8:11], s90 offen sc1\n"
"buffer_load_dwordx4 v[28:31], v142, s[8:11], s90 offen offset:1024 sc1\n"
"buffer_load_dwordx4 v[32:35], v142, s[8:11], s90 offen offset:2048 sc1\n"
"s_waitcnt vmcnt(14)\n"
"v_xor_b32 v0, v132, v0\n"
"s_waitcnt vmcnt(13)\n"
"v_xor_b32 v16, v132, v16\n"
"s_waitcnt vmcnt(12)\n"
"v_xor_b32 v92, v132, v92\n"
"s_waitcnt vmcnt(11)\n"
"v_xor_b32 v96, v132, v96\n"
"s_waitcnt vmcnt(0) lgkmcnt(0)\n"
"v_bitop3_b32 v134, v0, v1, v2 bitop3:0x80\n"
"v_bitop3_b32 v134, v134, v3, v16 bitop3:0x80\n"
"v_bitop3_b32 v134, v134, v17, v18 bitop3:0x80\n"
"v_bitop3_b32 v134, v134, v19, v92 bitop3:0x80\n"
"v_bitop3_b32 v134, v134, v93, v94 bitop3:0x80\n"
"v_bitop3_b32 v134, v134, v95, v96 bitop3:0x80\n"
"v_bitop3_b32 v134, v134, v97, v98 bitop3:0x80\n"
"v_bitop3_b32 v134, v134, v99, v99 bitop3:0x80\n"
"v_and_b32 v134, s38, v134\n"
"v_cmp_ne_u32 vcc, s38, v134\n"
"s_cbranch_vccz Lback22_h10x\n"
"s_add_u32 s64, s64, 1\n"
"s_cmp_lt_u32 s64, 0x4000\n"
"s_cbranch_scc1 Lslow22_h10l\n"
"Lback22_h10x:\n"
"s_waitcnt vmcnt(0) lgkmcnt(0)\n"
"s_mov_b32 m0, s65\n"
"s_nop 7\n"
"s_branch Lback22_h10\n"
"Lslow23_h11:\n"
"s_waitcnt vmcnt(0) lgkmcnt(0)\n"
"s_nop 7\n"
"s_nop 7\n"
"s_mov_b32 s64, 0\n"
"s_mov_b32 s65, m0\n"
"Lslow23_h11l:\n"
"s_sleep 1\n"
"buffer_load_dwordx4 v[100:103], v142, s[8:11], s88 offen sc1\n"
"buffer_load_dwordx4 v[104:107], v142, s[8:11], s88 offen offset:1024 sc1\n"
"buffer_load_dwordx4 v[108:111], v142, s[8:11], s88 offen offset:2048 sc1\n"
"buffer_load_dwordx4 v[112:115], v142, s[8:11], s88 offen offset:3072 sc1\n"
"buffer_load_dwordx4 v[116:119], v142, s[8:11], s89 offen sc1\n"
"buffer_load_dwordx4 v[120:123], v142, s[8:11], s89 offen offset:1024 sc1\n"
"buffer_load_dwordx4 v[20:23], v142, s[8:11], s89 offen offset:2048 sc1\n"
"buffer_load_dwordx4 v[24:27], v142, s[8:11], s89 offen offset:3072 sc1\n"
"buffer_load_dwordx4 v[56:59], v142, s[8:11], s90 offen sc1\n"
"buffer_load_dwordx4 v[28:31], v142, s[8:11], s90 offen offset:1024 sc1\n"
"buffer_load_dwordx4 v[32:35], v142, s[8:11], s90 offen offset:2048 sc1\n"
"buffer_load_dwordx4 v[76:79], v142, s[8:11], s90 offen offset:3072 sc1\n"
"buffer_load_dwordx4 v[80:83], v142, s[8:11], s91 offen sc1\n"
"s_waitcnt vmcnt(12)\n"
"v_xor_b32 v100, v132, v100\n"
"s_waitcnt vmcnt(11)\n"
"v_xor_b32 v104, v132, v104\n"
"s_waitcnt vmcnt(10)\n"
"v_xor_b32 v108, v132, v108\n"
"s_waitcnt vmcnt(9)\n"
"v_xor_b32 v112, v132, v112\n"
"s_waitcnt vmcnt(0) lgkmcnt(0)\n"
"v_bitop3_b32 v134, v100, v101, v102 bitop3:0x80\n"
"v_bitop3_b32 v134, v134, v103, v104 bitop3:0x80\n"
"v_bitop3_b32 v134, v134, v105, v106 bitop3:0x80\n"
"v_bitop3_b32 v134, v134, v107, v108 bitop3:0x80\n"
"v_bitop3_b32 v134, v134, v109, v110 bitop3:0x80\n"
"v_bitop3_b32 v134, v134, v111, v112 bitop3:0x80\n"
"v_bitop3_b32 v134, v134, v113, v114 bitop3:0x80\n"
"v_bitop3_b32 v134, v134, v115, v115 bitop3:0x80\n"
"v_and_b32 v134, s38, v134\n"
"v_cmp_ne_u32 vcc, s38, v134\n"
"s_cbranch_vccz Lback23_h11x\n"
"s_add_u32 s64, s64, 1\n"
"s_cmp_lt_u32 s64, 0x4000\n"
"s_cbranch_scc1 Lslow23_h11l\n"
"Lback23_h11x:\n"
"s_waitcnt vmcnt(0) lgkmcnt(0)\n"
"s_mov_b32 m0, s65\n"
"s_nop 7\n"
"s_branch Lback23_h11\n"
"Lslow24_h12:\n"
"s_waitcnt vmcnt(0) lgkmcnt(0)\n"
"s_nop 7\n"
"s_nop 7\n"
"s_mov_b32 s64, 0\n"
"s_mov_b32 s65, m0\n"
"Lslow24_h12l:\n"
"s_sleep 1\n"
"buffer_load_dwordx4 v[116:119], v142, s[8:11], s89 offen sc1\n"
"buffer_load_dwordx4 v[120:123], v142, s[8:11], s89 offen offset:1024 sc1\n"
"buffer_load_dwordx4 v[20:23], v142, s[8:11], s89 offen offset:2048 sc1\n"
"buffer_load_dwordx4 v[24:27], v142, s[8:11], s89 offen offset:3072 sc1\n"
"buffer_load_dwordx4 v[56:59], v142, s[8:11], s90 offen sc1\n"
"buffer_load_dwordx4 v[28:31], v142, s[8:11], s90 offen offset:1024 sc1\n"
"buffer_load_dwordx4 v[32:35], v142, s[8:11], s90 offen offset:2048 sc1\n"
"buffer_load_dwordx4 v[76:79], v142, s[8:11], s90 offen offset:3072 sc1\n"
"buffer_load_dwordx4 v[80:83], v142, s[8:11], s91 offen sc1\n"
"buffer_load_dwordx4 v[84:87], v142, s[8:11], s91 offen offset:1024 sc1\n"
"buffer_load_dwordx4 v[88:91], v142, s[8:11], s91 offen offset:2048 sc1\n"
"buffer_load_dwordx4 v[4:7], v142, s[8:11], s91 offen offset:3072 sc1\n"
"buffer_load_dwordx4 v[8:11], v142, s[8:11], s92 offen sc1\n"
"s_waitcnt vmcnt(12)\n"
"v_xor_b32 v116, v132, v116\n"
"s_waitcnt vmcnt(11)\n"
"v_xor_b32 v120, v132, v120\n"
"s_waitcnt vmcnt(10)\n"
"v_xor_b32 v20, v132, v20\n"
"s_waitcnt vmcnt(9)\n"
"v_xor_b32 v24, v132, v24\n"
"s_waitcnt vmcnt(0) lgkmcnt(0)\n"
"v_bitop3_b32 v134, v116, v117, v118 bitop3:0x80\n"
"v_bitop3_b32 v134, v134, v119, v120 bitop3:0x80\n"
"v_bitop3_b32 v134, v134, v121, v122 bitop3:0x80\n"
"v_bitop3_b32 v134, v134, v123, v20 bitop3:0x80\n"
"v_bitop3_b32 v134, v134, v21, v22 bitop3:0x80\n"
"v_bitop3_b32 v134, v134, v23, v24 bitop3:0x80\n"
"v_bitop3_b32 v134, v134, v25, v26 bitop3:0x80\n"
"v_bitop3_b32 v134, v134, v27, v27 bitop3:0x80\n"
"v_and_b32 v134, s38, v134\n"
"v_cmp_ne_u32 vcc, s38, v134\n"
"s_cbranch_vccz Lback24_h12x\n"
"s_add_u32 s64, s64, 1\n"
"s_cmp_lt_u32 s64, 0x4000\n"
"s_cbranch_scc1 Lslow24_h12l\n"
"Lback24_h12x:\n"
"s_waitcnt vmcnt(0) lgkmcnt(0)\n"
"s_mov_b32 m0, s65\n"
"s_nop 7\n"
"s_branch Lback24_h12\n"
"Lslow25_h13:\n"
"s_waitcnt vmcnt(0) lgkmcnt(0)\n"
"s_nop 7\n"
"s_nop 7\n"
"s_mov_b32 s64, 0\n"
"s_mov_b32 s65, m0\n"
"Lslow25_h13l:\n"
"s_sleep 1\n"
"buffer_load_dwordx4 v[56:59], v142, s[8:11], s90 offen sc1\n"
"buffer_load_dwordx4 v[28:31], v142, s[8:11], s90 offen offset:1024 sc1\n"
"buffer_load_dwordx4 v[32:35], v142, s[8:11], s90 offen offset:2048 sc1\n"
"buffer_load_dwordx4 v[76:79], v142, s[8:11], s90 offen offset:3072 sc1\n"
"buffer_load_dwordx4 v[80:83], v142, s[8:11], s91 offen sc1\n"
"buffer_load_dwordx4 v[84:87], v142, s[8:11], s91 offen offset:1024 sc1\n"
"buffer_load_dwordx4 v[88:91], v142, s[8:11], s91 offen offset:2048 sc1\n"
"buffer_load_dwordx4 v[4:7], v142, s[8:11], s91 offen offset:3072 sc1\n"
"buffer_load_dwordx4 v[8:11], v142, s[8:11], s92 offen sc1\n"
"buffer_load_dwordx4 v[12:15], v142, s[8:11], s92 offen offset:1024 sc1\n"
"s_waitcnt vmcnt(9)\n"
"v_xor_b32 v56, v132, v56\n"
"s_waitcnt vmcnt(8)\n"
"v_xor_b32 v28, v132, v28\n"
"s_waitcnt vmcnt(7)\n"
"v_xor_b32 v32, v132, v32\n"
"s_waitcnt vmcnt(6)\n"
"v_xor_b32 v76, v132, v76\n"
"s_waitcnt vmcnt(0) lgkmcnt(0)\n"
"v_bitop3_b32 v134, v56, v57, v58 bitop3:0x80\n"
"v_bitop3_b32 v134, v134, v59, v28 bitop3:0x80\n"
"v_bitop3_b32 v134, v134, v29, v30 bitop3:0x80\n"
"v_bitop3_b32 v134, v134, v31, v32 bitop3:0x80\n"
"v_bitop3_b32 v134, v134, v33, v34 bitop3:0x80\n"
"v_bitop3_b32 v134, v134, v35, v76 bitop3:0x80\n"
"v_bitop3_b32 v134, v134, v77, v78 bitop3:0x80\n"
"v_bitop3_b32 v134, v134, v79, v79 bitop3:0x80\n"
"v_and_b32 v134, s38, v134\n"
"v_cmp_ne_u32 vcc, s38, v134\n"
"s_cbranch_vccz Lback25_h13x\n"
"s_add_u32 s64, s64, 1\n"
"s_cmp_lt_u32 s64, 0x4000\n"
"s_cbranch_scc1 Lslow25_h13l\n"
"Lback25_h13x:\n"
"s_waitcnt vmcnt(0) lgkmcnt(0)\n"
"s_mov_b32 m0, s65\n"
"s_nop 7\n"
"s_branch Lback25_h13\n"
"Lslow26_h14:\n"
"s_waitcnt vmcnt(0) lgkmcnt(0)\n"
"s_nop 7\n"
"s_nop 7\n"
"s_mov_b32 s64, 0\n"
"s_mov_b32 s65, m0\n"
"Lslow26_h14l:\n"
"s_sleep 1\n"
"buffer_load_dwordx4 v[80:83], v142, s[8:11], s91 offen sc1\n"
"buffer_load_dwordx4 v[84:87], v142, s[8:11], s91 offen offset:1024 sc1\n"
"buffer_load_dwordx4 v[88:91], v142, s[8:11], s91 offen offset:2048 sc1\n"
"buffer_load_dwordx4 v[4:7], v142, s[8:11], s91 offen offset:3072 sc1\n"
"buffer_load_dwordx4 v[8:11], v142, s[8:11], s92 offen sc1\n"
"buffer_load_dwordx4 v[12:15], v142, s[8:11], s92 offen offset:1024 sc1\n"
"s_waitcnt vmcnt(5)\n"
"v_xor_b32 v80, v132, v80\n"
"s_waitcnt vmcnt(4)\n"
"v_xor_b32 v84, v132, v84\n"
"s_waitcnt vmcnt(3)\n"
"v_xor_b32 v88, v132, v88\n"
"s_waitcnt vmcnt(2)\n"
"v_xor_b32 v4, v132, v4\n"
"s_waitcnt vmcnt(1)\n"
"v_xor_b32 v8, v132, v8\n"
"s_waitcnt vmcnt(0)\n"
"v_xor_b32 v12, v132, v12\n"
"s_waitcnt vmcnt(0) lgkmcnt(0)\n"
"v_bitop3_b32 v134, v80, v81, v82 bitop3:0x80\n"
"v_bitop3_b32 v134, v134, v83, v84 bitop3:0x80\n"
"v_bitop3_b32 v134, v134, v85, v86 bitop3:0x80\n"
"v_bitop3_b32 v134, v134, v87, v88 bitop3:0x80\n"
"v_bitop3_b32 v134, v134, v89, v90 bitop3:0x80\n"
"v_bitop3_b32 v134, v134, v91, v4 bitop3:0x80\n"
"v_bitop3_b32 v134, v134, v5, v6 bitop3:0x80\n"
"v_bitop3_b32 v134, v134, v7, v8 bitop3:0x80\n"
"v_bitop3_b32 v134, v134, v9, v10 bitop3:0x80\n"
"v_bitop3_b32 v134, v134, v11, v12 bitop3:0x80\n"
"v_bitop3_b32 v134, v134, v13, v14 bitop3:0x80\n"
"v_bitop3_b32 v134, v134, v15, v15 bitop3:0x80\n"
"v_and_b32 v134, s38, v134\n"
"v_cmp_ne_u32 vcc, s38, v134\n"
"s_cbranch_vccz Lback26_h14x\n"
"s_add_u32 s64, s64, 1\n"
"s_cmp_lt_u32 s64, 0x4000\n"
"s_cbranch_scc1 Lslow26_h14l\n"
"Lback26_h14x:\n"
"s_waitcnt vmcnt(0) lgkmcnt(0)\n"
"s_mov_b32 m0, s65\n"
"s_nop 7\n"
"s_branch Lback26_h14\n"
"Lslow27_h15:\n"
"s_waitcnt vmcnt(0) lgkmcnt(0)\n"
"s_nop 7\n"
"s_nop 7\n"
"s_mov_b32 s64, 0\n"
"s_mov_b32 s65, m0\n"
"Lslow27_h15l:\n"
"s_sleep 1\n"
"buffer_load_dwordx4 v[0:3], v125, s[8:11], s85 offen sc1\n"
"buffer_load_dwordx4 v[16:19], v125, s[8:11], s85 offen offset:1024 sc1\n"
"buffer_load_dwordx4 v[92:95], v125, s[8:11], s85 offen offset:2048 sc1\n"
"buffer_load_dwordx4 v[96:99], v125, s[8:11], s85 offen offset:3072 sc1\n"
"buffer_load_dwordx4 v[100:103], v125, s[8:11], s88 offen sc1\n"
"buffer_load_dwordx4 v[104:107], v125, s[8:11], s88 offen offset:1024 sc1\n"
"buffer_load_dwordx4 v[108:111], v125, s[8:11], s88 offen offset:2048 sc1\n"
"buffer_load_dwordx4 v[112:115], v125, s[8:11], s88 offen offset:3072 sc1\n"
"s_waitcnt vmcnt(7)\n"
"v_xor_b32 v0, v132, v0\n"
"s_waitcnt vmcnt(6)\n"
"v_xor_b32 v16, v132, v16\n"
"s_waitcnt vmcnt(5)\n"
"v_xor_b32 v92, v132, v92\n"
"s_waitcnt vmcnt(4)\n"
"v_xor_b32 v96, v132, v96\n"
"s_waitcnt vmcnt(0) lgkmcnt(0)\n"
"v_bitop3_b32 v134, v0, v1, v2 bitop3:0x80\n"
"v_bitop3_b32 v134, v134, v3, v16 bitop3:0x80\n"
"v_bitop3_b32 v134, v134, v17, v18 bitop3:0x80\n"
"v_bitop3_b32 v134, v134, v19, v92 bitop3:0x80\n"
"v_bitop3_b32 v134, v134, v93, v94 bitop3:0x80\n"
"v_bitop3_b32 v134, v134, v95, v96 bitop3:0x80\n"
"v_bitop3_b32 v134, v134, v97, v98 bitop3:0x80\n"
"v_bitop3_b32 v134, v134, v99, v99 bitop3:0x80\n"
"v_and_b32 v134, s38, v134\n"
"v_cmp_ne_u32 vcc, s38, v134\n"
"s_cbranch_vccz Lback27_h15x\n"
"s_add_u32 s64, s64, 1\n"
"s_cmp_lt_u32 s64, 0x4000\n"
"s_cbranch_scc1 Lslow27_h15l\n"
"Lback27_h15x:\n"
"s_waitcnt vmcnt(0) lgkmcnt(0)\n"
"s_mov_b32 m0, s65\n"
"s_nop 7\n"
"s_branch Lback27_h15\n"
"Lslow28_h16:\n"
"s_waitcnt vmcnt(0) lgkmcnt(0)\n"
"s_nop 7\n"
"s_nop 7\n"
"s_mov_b32 s64, 0\n"
"s_mov_b32 s65, m0\n"
"Lslow28_h16l:\n"
"s_sleep 1\n"
"buffer_load_dwordx4 v[100:103], v125, s[8:11], s88 offen sc1\n"
"buffer_load_dwordx4 v[104:107], v125, s[8:11], s88 offen offset:1024 sc1\n"
"buffer_load_dwordx4 v[108:111], v125, s[8:11], s88 offen offset:2048 sc1\n"
"buffer_load_dwordx4 v[112:115], v125, s[8:11], s88 offen offset:3072 sc1\n"
"buffer_load_dwordx4 v[76:79], v125, s[8:11], s89 offen sc1\n"
"buffer_load_dwordx4 v[80:83], v125, s[8:11], s89 offen offset:1024 sc1\n"
"buffer_load_dwordx4 v[84:87], v125, s[8:11], s89 offen offset:2048 sc1\n"
"buffer_load_dwordx4 v[88:91], v125, s[8:11], s89 offen offset:3072 sc1\n"
"buffer_load_dwordx4 v[4:7], v125, s[8:11], s90 offen sc1\n"
"s_waitcnt vmcnt(8)\n"
"v_xor_b32 v100, v132, v100\n"
"s_waitcnt vmcnt(7)\n"
"v_xor_b32 v104, v132, v104\n"
"s_waitcnt vmcnt(6)\n"
"v_xor_b32 v108, v132, v108\n"
"s_waitcnt vmcnt(5)\n"
"v_xor_b32 v112, v132, v112\n"
"s_waitcnt vmcnt(0) lgkmcnt(0)\n"
"v_bitop3_b32 v134, v100, v101, v102 bitop3:0x80\n"
"v_bitop3_b32 v134, v134, v103, v104 bitop3:0x80\n"
"v_bitop3_b32 v134, v134, v105, v106 bitop3:0x80\n"
"v_bitop3_b32 v134, v134, v107, v108 bitop3:0x80\n"
"v_bitop3_b32 v134, v134, v109, v110 bitop3:0x80\n"
"v_bitop3_b32 v134, v134, v111, v112 bitop3:0x80\n"
"v_bitop3_b32 v134, v134, v113, v114 bitop3:0x80\n"
"v_bitop3_b32 v134, v134, v115, v115 bitop3:0x80\n"
"v_and_b32 v134, s38, v134\n"
"v_cmp_ne_u32 vcc, s38, v134\n"
"s_cbranch_vccz Lback28_h16x\n"
"s_add_u32 s64, s64, 1\n"
"s_cmp_lt_u32 s64, 0x4000\n"
"s_cbranch_scc1 Lslow28_h16l\n"
"Lback28_h16x:\n"
"s_waitcnt vmcnt(0) lgkmcnt(0)\n"
"s_mov_b32 m0, s65\n"
"s_nop 7\n"
"s_branch Lback28_h16\n"
"Lslow29_h17:\n"
"s_waitcnt vmcnt(0) lgkmcnt(0)\n"
"s_nop 7\n"
"s_nop 7\n"
"s_mov_b32 s64, 0\n"
"s_mov_b32 s65, m0\n"
"Lslow29_h17l:\n"
"s_sleep 1\n"
"buffer_load_dwordx4 v[76:79], v125, s[8:11], s89 offen sc1\n"
"buffer_load_dwordx4 v[80:83], v125, s[8:11], s89 offen offset:1024 sc1\n"
"buffer_load_dwordx4 v[84:87], v125, s[8:11], s89 offen offset:2048 sc1\n"
"buffer_load_dwordx4 v[88:91], v125, s[8:11], s89 offen offset:3072 sc1\n"
"buffer_load_dwordx4 v[4:7], v125, s[8:11], s90 offen sc1\n"
"buffer_load_dwordx4 v[8:11], v125, s[8:11], s90 offen offset:1024 sc1\n"
"buffer_load_dwordx4 v[12:15], v125, s[8:11], s90 offen offset:2048 sc1\n"
"buffer_load_dwordx4 v[0:3], v125, s[8:11], s90 offen offset:3072 sc1\n"
"buffer_load_dwordx4 v[16:19], v125, s[8:11], s91 offen sc1\n"
"buffer_load_dwordx4 v[92:95], v125, s[8:11], s91 offen offset:1024 sc1\n"
"buffer_load_dwordx4 v[96:99], v125, s[8:11], s91 offen offset:2048 sc1\n"
"buffer_load_dwordx4 v[100:103], v125, s[8:11], s91 offen offset:3072 sc1\n"
"s_waitcnt vmcnt(11)\n"
"v_xor_b32 v76, v132, v76\n"
"s_waitcnt vmcnt(10)\n"
"v_xor_b32 v80, v132, v80\n"
"s_waitcnt vmcnt(9)\n"
"v_xor_b32 v84, v132, v84\n"
"s_waitcnt vmcnt(8)\n"
"v_xor_b32 v88, v132, v88\n"
"s_waitcnt vmcnt(0) lgkmcnt(0)\n"
"v_bitop3_b32 v134, v76, v77, v78 bitop3:0x80\n"
"v_bitop3_b32 v134, v134, v79, v80 bitop3:0x80\n"
"v_bitop3_b32 v134, v134, v81, v82 bitop3:0x80\n"
"v_bitop3_b32 v134, v134, v83, v84 bitop3:0x80\n"
"v_bitop3_b32 v134, v134, v85, v86 bitop3:0x80\n"
"v_bitop3_b32 v134, v134, v87, v88 bitop3:0x80\n"
"v_bitop3_b32 v134, v134, v89, v90 bitop3:0x80\n"
"v_bitop3_b32 v134, v134, v91, v91 bitop3:0x80\n"
"v_and_b32 v134, s38, v134\n"
"v_cmp_ne_u32 vcc, s38, v134\n"
"s_cbranch_vccz Lback29_h17x\n"
"s_add_u32 s64, s64, 1\n"
"s_cmp_lt_u32 s64, 0x4000\n"
"s_cbranch_scc1 Lslow29_h17l\n"
"Lback29_h17x:\n"
"s_waitcnt vmcnt(0) lgkmcnt(0)\n"
"s_mov_b32 m0, s65\n"
"s_nop 7\n"
"s_branch Lback29_h17\n"
"Lslow30_h18:\n"
"s_waitcnt vmcnt(0) lgkmcnt(0)\n"
"s_nop 7\n"
"s_nop 7\n"
"s_mov_b32 s64, 0\n"
"s_mov_b32 s65, m0\n"
"Lslow30_h18l:\n"
"s_sleep 1\n"
"buffer_load_dwordx4 v[4:7], v125, s[8:11], s90 offen sc1\n"
"buffer_load_dwordx4 v[8:11], v125, s[8:11], s90 offen offset:1024 sc1\n"
"buffer_load_dwordx4 v[12:15], v125, s[8:11], s90 offen offset:2048 sc1\n"
"buffer_load_dwordx4 v[0:3], v125, s[8:11], s90 offen offset:3072 sc1\n"
"buffer_load_dwordx4 v[16:19], v125, s[8:11], s91 offen sc1\n"
"buffer_load_dwordx4 v[92:95], v125, s[8:11], s91 offen offset:1024 sc1\n"
"buffer_load_dwordx4 v[96:99], v125, s[8:11], s91 offen offset:2048 sc1\n"
"buffer_load_dwordx4 v[100:103], v125, s[8:11], s91 offen offset:3072 sc1\n"
"buffer_load_dwordx4 v[104:107], v125, s[8:11], s92 offen sc1\n"
"buffer_load_dwordx4 v[108:111], v125, s[8:11], s92 offen offset:1024 sc1\n"
"s_waitcnt vmcnt(9)\n"
"v_xor_b32 v4, v132, v4\n"
"s_waitcnt vmcnt(8)\n"
"v_xor_b32 v8, v132, v8\n"
"s_waitcnt vmcnt(7)\n"
"v_xor_b32 v12, v132, v12\n"
"s_waitcnt vmcnt(6)\n"
"v_xor_b32 v0, v132, v0\n"
"s_waitcnt vmcnt(0) lgkmcnt(0)\n"
"v_bitop3_b32 v134, v4, v5, v6 bitop3:0x80\n"
"v_bitop3_b32 v134, v134, v7, v8 bitop3:0x80\n"
"v_bitop3_b32 v134, v134, v9, v10 bitop3:0x80\n"
"v_bitop3_b32 v134, v134, v11, v12 bitop3:0x80\n"
"v_bitop3_b32 v134, v134, v13, v14 bitop3:0x80\n"
"v_bitop3_b32 v134, v134, v15, v0 bitop3:0x80\n"
"v_bitop3_b32 v134, v134, v1, v2 bitop3:0x80\n"
"v_bitop3_b32 v134, v134, v3, v3 bitop3:0x80\n"
"v_and_b32 v134, s38, v134\n"
"v_cmp_ne_u32 vcc, s38, v134\n"
"s_cbranch_vccz Lback30_h18x\n"
"s_add_u32 s64, s64, 1\n"
"s_cmp_lt_u32 s64, 0x4000\n"
"s_cbranch_scc1 Lslow30_h18l\n"
"Lback30_h18x:\n"
"s_waitcnt vmcnt(0) lgkmcnt(0)\n"
"s_mov_b32 m0, s65\n"
"s_nop 7\n"
"s_branch Lback30_h18\n"
"Lslow31_h19:\n"
"s_waitcnt vmcnt(0) lgkmcnt(0)\n"
"s_nop 7\n"
"s_nop 7\n"
"s_mov_b32 s64, 0\n"
"s_mov_b32 s65, m0\n"
"Lslow31_h19l:\n"
"s_sleep 1\n"
"buffer_load_dwordx4 v[16:19], v125, s[8:11], s91 offen sc1\n"
"buffer_load_dwordx4 v[92:95], v125, s[8:11], s91 offen offset:1024 sc1\n"
"buffer_load_dwordx4 v[96:99], v125, s[8:11], s91 offen offset:2048 sc1\n"
"buffer_load_dwordx4 v[100:103], v125, s[8:11], s91 offen offset:3072 sc1\n"
"buffer_load_dwordx4 v[104:107], v125, s[8:11], s92 offen sc1\n"
"buffer_load_dwordx4 v[108:111], v125, s[8:11], s92 offen offset:1024 sc1\n"
"s_waitcnt vmcnt(5)\n"
"v_xor_b32 v16, v132, v16\n"
"s_waitcnt vmcnt(4)\n"
"v_xor_b32 v92, v132, v92\n"
"s_waitcnt vmcnt(3)\n"
"v_xor_b32 v96, v132, v96\n"
"s_waitcnt vmcnt(2)\n"
"v_xor_b32 v100, v132, v100\n"
"s_waitcnt vmcnt(1)\n"
"v_xor_b32 v104, v132, v104\n"
"s_waitcnt vmcnt(0)\n"
"v_xor_b32 v108, v132, v108\n"
"s_waitcnt vmcnt(0) lgkmcnt(0)\n"
"v_bitop3_b32 v134, v16, v17, v18 bitop3:0x80\n"
"v_bitop3_b32 v134, v134, v19, v92 bitop3:0x80\n"
"v_bitop3_b32 v134, v134, v93, v94 bitop3:0x80\n"
"v_bitop3_b32 v134, v134, v95, v96 bitop3:0x80\n"
"v_bitop3_b32 v134, v134, v97, v98 bitop3:0x80\n"
"v_bitop3_b32 v134, v134, v99, v100 bitop3:0x80\n"
"v_bitop3_b32 v134, v134, v101, v102 bitop3:0x80\n"
"v_bitop3_b32 v134, v134, v103, v104 bitop3:0x80\n"
"v_bitop3_b32 v134, v134, v105, v106 bitop3:0x80\n"
"v_bitop3_b32 v134, v134, v107, v108 bitop3:0x80\n"
"v_bitop3_b32 v134, v134, v109, v110 bitop3:0x80\n"
"v_bitop3_b32 v134, v134, v111, v111 bitop3:0x80\n"
"v_and_b32 v134, s38, v134\n"
"v_cmp_ne_u32 vcc, s38, v134\n"
"s_cbranch_vccz Lback31_h19x\n"
"s_add_u32 s64, s64, 1\n"
"s_cmp_lt_u32 s64, 0x4000\n"
"s_cbranch_scc1 Lslow31_h19l\n"
"Lback31_h19x:\n"
"s_waitcnt vmcnt(0) lgkmcnt(0)\n"
"s_mov_b32 m0, s65\n"
"s_nop 7\n"
"s_branch Lback31_h19\n"
"Lkend:\n"
"s_nop 0\n"
"s_nop 0\n"
  : "+v"(tid)
  : "s"(__builtin_amdgcn_kernarg_segment_ptr()), "s"(blockIdx.x)
  : "v1", "v2", "v3", "v4", "v5", "v6", "v7", "v8", "v9", "v10", "v11", "v12", "v13", "v14", "v15", "v16", "v17", "v18", "v19", "v20", "v21", "v22", "v23", "v24", "v25", "v26", "v27", "v28", "v29", "v30", "v31", "v32", "v33", "v34", "v35", "v36", "v37", "v38", "v39", "v40", "v41", "v42", "v43", "v44", "v45", "v46", "v47", "v48", "v49", "v50", "v51", "v52", "v53", "v54", "v55", "v56", "v57", "v58", "v59", "v60", "v61", "v62", "v63", "v64", "v65", "v66", "v67", "v68", "v69", "v70", "v71", "v72", "v73", "v74", "v75", "v76", "v77", "v78", "v79", "v80", "v81", "v82", "v83", "v84", "v85", "v86", "v87", "v88", "v89", "v90", "v91", "v92", "v93", "v94", "v95", "v96", "v97", "v98", "v99", "v100", "v101", "v102", "v103", "v104", "v105", "v106", "v107", "v108", "v109", "v110", "v111", "v112", "v113", "v114", "v115", "v116", "v117", "v118", "v119", "v120", "v121", "v122", "v123", "v124", "v125", "v126", "v127", "v128", "v129", "v130", "v131", "v132", "v133", "v134", "v135", "v136", "v137", "v138", "v139", "v140", "v141", "v142", "v143", "v144", "v145", "v146", "v147", "v148", "v149", "v150", "v151", "v152", "v153", "v154", "v155", "v156", "v157", "v158", "v159", "v160", "v161", "v162", "v163", "v164", "v165", "v166", "v167", "v168", "v169", "v170", "v171", "v172", "v173", "v174", "v175", "v176", "v177", "v178", "v179", "v180", "v181", "v182", "v183", "v184", "v185", "v186", "v187", "v188", "v189", "v190", "v191", "v192", "v193", "v194", "v195", "v196", "v197", "v198", "v199", "v200", "v201", "v202", "v203", "v204", "v205", "v206", "v207", "v208", "v209", "v210", "v211", "v212", "v213", "v214", "v215", "v216", "v217", "v218", "v219", "v220", "v221", "v222", "v223", "v224", "v225", "v226", "v227", "v228", "v229", "v230", "v231", "v232", "v233", "v234", "v235", "v236", "v237", "v238", "v239", "v240", "v241", "v242", "v243", "v244", "v245", "v246", "v247", "v248", "v249", "v250", "v251", "v252", "v253", "v254", "v255", "a0", "a1", "a2", "a3", "a4", "a5", "a6", "a7", "a8", "a9", "a10", "a11", "a12", "a13", "a14", "a15", "a16", "a17", "a18", "a19", "a20", "a21", "a22", "a23", "a24", "a25", "a26", "a27", "a28", "a29", "a30", "a31", "a32", "a33", "a34", "a35", "a36", "a37", "a38", "a39", "a40", "a41", "a42", "a43", "a44", "a45", "a46", "a47", "a48", "a49", "a50", "a51", "a52", "a53", "a54", "a55", "a56", "a57", "a58", "a59", "a60", "a61", "a62", "a63", "a64", "a65", "a66", "a67", "a68", "a69", "a70", "a71", "a72", "a73", "a74", "a75", "a76", "a77", "a78", "a79", "a80", "a81", "a82", "a83", "a84", "a85", "a86", "a87", "a88", "a89", "a90", "a91", "a92", "a93", "a94", "a95", "a96", "a97", "a98", "a99", "a100", "a101", "a102", "a103", "a104", "a105", "a106", "a107", "a108", "a109", "a110", "a111", "a112", "a113", "a114", "a115", "a116", "a117", "a118", "a119", "a120", "a121", "a122", "a123", "a124", "a125", "a126", "a127", "a128", "a129", "a130", "a131", "a132", "a133", "a134", "a135", "a136", "a137", "a138", "a139", "a140", "a141", "a142", "a143", "a144", "a145", "a146", "a147", "a148", "a149", "a150", "a151", "a152", "a153", "a154", "a155", "a156", "a157", "a158", "a159", "a160", "a161", "a162", "a163", "a164", "a165", "a166", "a167", "a168", "a169", "a170", "a171", "a172", "a173", "a174", "a175", "a176", "a177", "a178", "a179", "a180", "a181", "a182", "a183", "a184", "a185", "a186", "a187", "a188", "a189", "a190", "a191", "a192", "a193", "a194", "a195", "a196", "a197", "a198", "a199", "a200", "a201", "a202", "a203", "a204", "a205", "a206", "a207", "a208", "a209", "a210", "a211", "a212", "a213", "a214", "a215", "a216", "a217", "a218", "a219", "a220", "a221", "a222", "a223", "a224", "a225", "a226", "a227", "a228", "a229", "a230", "a231", "a232", "a233", "a234", "a235", "a236", "a237", "a238", "a239", "a240", "a241", "a242", "a243", "a244", "a245", "a246", "a247", "a248", "a249", "a250", "a251", "a252", "a253", "a254", "a255", "s4", "s5", "s6", "s7", "s8", "s9", "s10", "s11", "s12", "s13", "s14", "s15", "s16", "s17", "s18", "s19", "s20", "s21", "s22", "s23", "s24", "s25", "s26", "s27", "s28", "s29", "s30", "s31", "s32", "s33", "s34", "s35", "s36", "s37", "s38", "s39", "s40", "s41", "s42", "s43", "s44", "s45", "s46", "s47", "s48", "s49", "s50", "s51", "s52", "s53", "s54", "s55", "s56", "s57", "s58", "s59", "s60", "s61", "s62", "s63", "s64", "s65", "s66", "s67", "s68", "s69", "s70", "s71", "s72", "s73", "s74", "s75", "s76", "s77", "s78", "s79", "s80", "s81", "s82", "s83", "s84", "s85", "s86", "s87", "s88", "s89", "s90", "s91", "s92", "s93", "s94", "s95", "s96", "s97", "s98", "s99", "vcc", "scc", "memory");
}

extern "C" void kernel_launch(void* const* d_in, const int* in_sizes, int n_in, void* d_out, int out_size, void* d_ws, size_t ws_size, hipStream_t stream) {
  Params p;
  p.q = d_in[0]; p.k = d_in[1]; p.v = d_in[2]; p.sk = d_in[3]; p.hk = d_in[4]; p.dv = d_in[5];
  p.w1 = d_in[7]; p.w2 = d_in[8]; p.out = d_out; p.ws = d_ws;
  (void)hipFuncSetAttribute((const void*)fused_kernel, hipFuncAttributeMaxDynamicSharedMemorySize, 163840);
  fused_kernel<<<256, 256, 163840, stream>>>(p);
}
```
